```python
import torch

def _softmax(x, axis=-1):
    return torch.softmax(x, dim=axis)

def _gelu(x):
    return torch.nn.functional.gelu(x, approximate='tanh')

def _silu(x):
    return torch.nn.functional.silu(x)

def _sigmoid(x):
    return torch.sigmoid(x)

def _layernorm(x, axis=-1, eps=1e-05):
    m = x.mean(dim=axis, keepdim=True)
    v = x.var(dim=axis, keepdim=True, unbiased=False)
    return (x - m) / torch.sqrt(v + eps)

def _scatter_add(tgt, idx, src, axis=0):
    return tgt.clone().index_add_(axis, idx, src)

def _segment_sum(src, seg, n):
    out = torch.zeros((n,) + src.shape[1:], dtype=src.dtype)
    return out.index_add_(0, seg, src)

def _pad(x, pw):
    flat = tuple((v for pair in reversed(pw) for v in pair))
    return torch.nn.functional.pad(x, flat)
kernel_name = 'sequential_preLN__sliding_local_dilated__mixer_gmlp__prompt'

def setup_inputs():

    _w = lambda a: (lambda t: t.to(torch.bfloat16) if t.is_floating_point() else t)(torch.as_tensor(a))

    def _A_setup():
        B, S, D = (1, 2048, 1024)
        return {'x': _w((lambda *s: torch.randn(*s, dtype=torch.bfloat16) * 0.05)(B, S, D)), 'wqkv': _w((lambda *s: torch.randn(*s, dtype=torch.bfloat16) * 0.05)(D, 3 * D)), 'wo': _w((lambda *s: torch.randn(*s, dtype=torch.bfloat16) * 0.05)(D, D))}
    _A_in = _A_setup()

    def _B_setup():
        B, S, D, F = (1, 2048, 1024, 3584)
        Fh = F // 2
        return {'x': _w((lambda *s: torch.randn(*s, dtype=torch.bfloat16) * 0.05)(B, S, D)), 'w_in': _w((lambda *s: torch.randn(*s, dtype=torch.bfloat16) * 0.4)(D, 2 * Fh)), 'w_sp': _w(0.01 * (lambda *s: torch.randn(*s, dtype=torch.bfloat16) * 0.4)(S, S)), 'b_sp': _w(1.0 + 0.01 * (lambda *s: torch.randn(*s, dtype=torch.bfloat16) * 0.05)(S)), 'w_out': _w((lambda *s: torch.randn(*s, dtype=torch.bfloat16) * 0.05)(Fh, D))}
    _B_in = _B_setup()
    return {'A_x': _A_in['x'], 'A_wqkv': _A_in['wqkv'], 'A_wo': _A_in['wo'], 'B_x': _B_in['x'], 'B_w_in': _B_in['w_in'], 'B_w_sp': _B_in['w_sp'], 'B_b_sp': _B_in['b_sp'], 'B_w_out': _B_in['w_out']}

def reference(A_x, A_wqkv, A_wo, B_x, B_w_in, B_w_sp, B_b_sp, B_w_out):

    def _A_ref(x, wqkv, wo):
        B, S, D = x.shape
        H = 32
        Dh = D // H
        W, dil = (8, 2)
        qkv = (lambda x, *d: x.permute(*d))((x @ wqkv).reshape(B, S, 3, H, Dh), 2, 0, 3, 1, 4)
        q, k, v = (qkv[0], qkv[1], qkv[2])
        scale = Dh ** (-0.5)
        pos = torch.arange(S)
        off = torch.arange(W) * dil
        idx = pos[:, None] - off[None, :]
        mask = idx >= 0
        idx_c = torch.where(mask, idx, 0).reshape(-1)
        kg = k[:, :, idx_c, :].reshape(B, H, S, W, Dh)
        vg = v[:, :, idx_c, :].reshape(B, H, S, W, Dh)
        logits = torch.einsum('bhsd,bhswd->bhsw', q, kg) * scale
        logits = torch.where(mask[None, None, :, :], logits, -1000000000.0)
        attn = _softmax(logits, axis=-1)
        o = torch.einsum('bhsw,bhswd->bhsd', attn, vg)
        return (lambda x, *d: x.permute(*d))(o, 0, 2, 1, 3).reshape(B, S, D) @ wo

    def _B_ref(x, w_in, w_sp, b_sp, w_out):
        B, S, D = x.shape
        F = w_in.shape[-1]
        Fh = F // 2
        z = _gelu(torch.einsum('bsd,df->bsf', x, w_in))
        z1 = z[..., :Fh].clone()
        z2 = _layernorm(z[..., Fh:], axis=-1)
        g = torch.einsum('ts,bsf->btf', w_sp, z2) + b_sp[None, :, None]
        return torch.einsum('bsf,fd->bsd', z1 * g, w_out)
    x = A_x
    h = x + _A_ref(_layernorm(x, axis=-1), A_wqkv, A_wo)
    h2 = h + _B_ref(_layernorm(h, axis=-1), B_w_in, B_w_sp, B_b_sp, B_w_out)
    return h2
```

```cpp
#include <hip/hip_runtime.h>

namespace mk {

#define LAS __attribute__((address_space(3)))
typedef LAS void* lds_vp;
typedef unsigned short u16;
typedef unsigned int u32;
typedef unsigned long long u64;
typedef short bf16x8 __attribute__((ext_vector_type(8)));
typedef short bf16x4 __attribute__((ext_vector_type(4)));
typedef float f32x2 __attribute__((ext_vector_type(2)));
typedef float f32x4 __attribute__((ext_vector_type(4)));
typedef u32 u32x2 __attribute__((ext_vector_type(2)));
typedef u32 u32x4 __attribute__((ext_vector_type(4)));
typedef __bf16 bf16x2n __attribute__((ext_vector_type(2)));
typedef __amdgpu_buffer_rsrc_t rsrc_t;

constexpr u32 WS_EPOCH = 0;
constexpr u32 WS_FLAGS = 4096;
enum { FX = 0, FW = 1, FO = 2, FHH = 3, FZ = 4, FU = 5 };
constexpr u32 WS_XSTAT = 256 * 1024;
constexpr u32 WS_HSTAT = 512 * 1024;
constexpr u32 WS_CS    = 768 * 1024;
constexpr u32 WS_ZSTAT = 1024 * 1024;
constexpr u32 WS_O     = 2u << 20;
constexpr u32 WS_H     = 6u << 20;
constexpr u32 WS_WINP  = 10u << 20;
constexpr u32 WS_Z2N   = 18u << 20;
constexpr u32 WS_U     = 26u << 20;
constexpr u32 WS_END   = 34u << 20;
__host__ __device__ constexpr u32 flag_off(int kind, int rp, int cj) { return WS_FLAGS + (u32)((kind * 256 + rp * 16 + cj) * 128); }

constexpr int P1_A = 144 * 128, P1_B = 192 * 128, P1_ST = P1_A + P1_B + 16;
constexpr int P1_ONES = P1_A + P1_B;
constexpr int WSTG = 3 * P1_ST;
constexpr int G2_B = 0, G2_A = 6 * 8192;
constexpr int AT_BASE = G2_A, AT_RS = 80, AT_BLK = 72 * AT_RS;
constexpr int G3_ST = 16384 + 14336;
constexpr int Z1BUF = 4 * G3_ST;
constexpr int TAB = 156 * 1024;
constexpr int T_XST = TAB, T_HST = TAB + 1152, T_ZST = T_HST + 1024, T_CS = T_ZST + 1024;
constexpr int LDS_BYTES = 160 * 1024;
static_assert(WSTG + 2 * 14336 <= TAB, "lds");
static_assert(AT_BASE + 12 * AT_BLK <= TAB, "lds");
static_assert(G2_A + 6 * 16384 <= TAB, "lds");
static_assert(Z1BUF + 28672 <= TAB, "lds");
static_assert(T_CS + 896 <= LDS_BYTES, "lds");

constexpr int NORM_T = 21;
constexpr int FLAG_T = 24;

struct Params {
  const u16* x; const u16* wqkv; const u16* wo; const u16* win; const u16* wsp; const u16* bsp; const u16* wout;
  u16* out; char* ws;
};

template <int I> struct IC { static constexpr int v = I; };
template <int N, int I = 0, class F> __device__ __forceinline__ void sfor(F&& f) { if constexpr (I < N) { f(IC<I>{}); sfor<N, I + 1>(f); } }
#define MK_WAITV(n) asm volatile("s_waitcnt vmcnt(%0)" ::"i"(n) : "memory")
#define MK_LGKM0() __builtin_amdgcn_s_waitcnt(0xC07F)
#define MK_SB() __builtin_amdgcn_sched_barrier(0)
__device__ __forceinline__ void bar_h() { asm volatile("s_waitcnt lgkmcnt(0)\n\ts_barrier" ::: "memory"); }
__device__ __forceinline__ void bar_c() { asm volatile("" ::: "memory"); __builtin_amdgcn_s_barrier(); asm volatile("" ::: "memory"); }

__device__ __forceinline__ rsrc_t mk_rsrc(const void* p, u32 bytes) { return __builtin_amdgcn_make_buffer_rsrc((void*)p, 0, bytes, 0x00020000); }
template <int OFF, int AUX> __device__ __forceinline__ void glds(rsrc_t r, u32 ldsdst, u32 voff, u32 soff) {
  __builtin_amdgcn_raw_ptr_buffer_load_lds(r, (lds_vp)(size_t)ldsdst, 16, (int)voff, (int)soff, OFF, AUX);
}
constexpr int SC1 = 16;
__device__ __forceinline__ u32 pk_bf16(float lo, float hi) { f32x2 v = {lo, hi}; return __builtin_bit_cast(u32, __builtin_convertvector(v, bf16x2n)); }
__device__ __forceinline__ float asf(u32 u) { return __builtin_bit_cast(float, u); }
__device__ __forceinline__ u32 asu(float f) { return __builtin_bit_cast(u32, f); }
__device__ __forceinline__ float bf_lo(u32 u) { return __builtin_bit_cast(float, u << 16); }
__device__ __forceinline__ float bf_hi(u32 u) { return __builtin_bit_cast(float, u & 0xffff0000u); }
template <int CTRL> __device__ __forceinline__ float dpp(float x) {
  return __builtin_bit_cast(float, __builtin_amdgcn_update_dpp(0, __builtin_bit_cast(int, x), CTRL, 0xf, 0xf, true));
}
__device__ __forceinline__ float sum16(float x) { x += dpp<0xB1>(x); x += dpp<0x4E>(x); x += dpp<0x141>(x); x += dpp<0x140>(x); return x; }
__device__ __forceinline__ float sumg(float x) {
  u32 u = __builtin_bit_cast(u32, x);
  auto a = __builtin_amdgcn_permlane16_swap(u, u, false, false);
  x = __builtin_bit_cast(float, (u32)a[0]) + __builtin_bit_cast(float, (u32)a[1]);
  u = __builtin_bit_cast(u32, x);
  auto b = __builtin_amdgcn_permlane32_swap(u, u, false, false);
  return __builtin_bit_cast(float, (u32)b[0]) + __builtin_bit_cast(float, (u32)b[1]);
}
__device__ __forceinline__ float maxg(float x) {
  u32 u = __builtin_bit_cast(u32, x);
  auto a = __builtin_amdgcn_permlane16_swap(u, u, false, false);
  x = fmaxf(__builtin_bit_cast(float, (u32)a[0]), __builtin_bit_cast(float, (u32)a[1]));
  u = __builtin_bit_cast(u32, x);
  auto b = __builtin_amdgcn_permlane32_swap(u, u, false, false);
  return fmaxf(__builtin_bit_cast(float, (u32)b[0]), __builtin_bit_cast(float, (u32)b[1]));
}
__device__ __forceinline__ u32x4 pair_swap(u32x2 a, u32x2 b) {
  auto r0 = __builtin_amdgcn_permlane16_swap(a[0], b[0], false, false);
  auto r1 = __builtin_amdgcn_permlane16_swap(a[1], b[1], false, false);
  return (u32x4){(u32)r0[0], (u32)r1[0], (u32)r0[1], (u32)r1[1]};
}
__device__ __forceinline__ float gelu_f(float x) {
  const float e = __builtin_amdgcn_exp2f(x * (x * x * -0.10294324f + -2.30220820f));
  return x * __builtin_amdgcn_rcpf(1.0f + e);
}
template <class T> __device__ __forceinline__ T lds_ld(u32 a) { return *(const LAS T*)(size_t)a; }
template <class T> __device__ __forceinline__ void lds_st(u32 a, T v) { *(LAS T*)(size_t)a = v; }
__device__ __forceinline__ bf16x4 lds_tr(u32 a) { return __builtin_amdgcn_ds_read_tr16_b64_v4i16((LAS bf16x4*)(size_t)a); }
__device__ __forceinline__ u32 opaque(u32 x) { asm("" : "+v"(x)); return x; }

struct Ctx {
  int lane, wid, rp, cj, r16, g, q, p;
  u32 lds;
  u32 tag_lo, tag_hi;
  rsrc_t rws;
};
constexpr int SPIN_CAP = 1 << 17;

__device__ __forceinline__ void load_tag(Ctx& c) {
  const u32x2 e = __builtin_amdgcn_raw_buffer_load_b64(c.rws, (int)WS_EPOCH, 0, SC1);
  const u64 t = (((u64)e[1] << 32) | e[0]) + 1;
  c.tag_lo = __builtin_amdgcn_readfirstlane((u32)t); c.tag_hi = __builtin_amdgcn_readfirstlane((u32)(t >> 32));
}
__device__ __forceinline__ void publish(const Ctx& c, int kind, int slot) {
  asm volatile("s_waitcnt vmcnt(0)" ::: "memory");
  if (c.lane == 0) __builtin_amdgcn_raw_buffer_store_b64((u32x2){c.tag_lo, c.tag_hi}, c.rws, (int)(flag_off(kind, c.rp, c.cj) + 8 * slot), 0, SC1);
}
__device__ __forceinline__ void poll64(const Ctx& c, u32 voff) {
#pragma unroll 1
  for (int s = 0; s < SPIN_CAP; ++s) {
    asm volatile("" ::: "memory");
    const u32x2 v = __builtin_amdgcn_raw_buffer_load_b64(c.rws, (int)voff, 0, SC1);
    if (__all(v[0] == c.tag_lo && v[1] == c.tag_hi)) break;
    __builtin_amdgcn_s_sleep(2);
  }
  asm volatile("" ::: "memory");
}
__device__ __forceinline__ void poll128(const Ctx& c, u32 voff) {
#pragma unroll 1
  for (int s = 0; s < SPIN_CAP; ++s) {
    asm volatile("" ::: "memory");
    const u32x4 v = __builtin_amdgcn_raw_buffer_load_b128(c.rws, (int)voff, 0, SC1);
    if (__all(v[0] == c.tag_lo && v[1] == c.tag_hi && v[2] == c.tag_lo && v[3] == c.tag_hi)) break;
    __builtin_amdgcn_s_sleep(2);
  }
  asm volatile("" ::: "memory");
}

struct Win { u32 b[3]; };
__device__ __forceinline__ Win mk_win(u32 base) { Win w; w.b[0] = opaque(base); w.b[1] = opaque(base + 65536u); w.b[2] = opaque(base + 131072u); return w; }
template <int OFF> __device__ __forceinline__ u32 wad(const Win& w) { return w.b[OFF >> 16] + (u32)(OFF & 0xffff); }

template <int KS, int MT, int NT> struct Set { bf16x8 a[KS][MT]; bf16x8 b[KS][NT]; bf16x4 t[KS][NT]; };

__device__ __forceinline__ void mfma_acc(f32x4& c, const bf16x8& a, const bf16x8& b) {
  asm volatile("v_mfma_f32_16x16x32_bf16 %0, %1, %2, %0" : "+v"(c) : "v"(a), "v"(b));
}
__device__ __forceinline__ void mfma_in() { MK_SB(); asm volatile("s_nop 1"); MK_SB(); }
__device__ __forceinline__ void mfma_out() { MK_SB(); asm volatile("s_nop 7\n\ts_nop 1"); MK_SB(); }
template <int KS, int MT, int NT, bool SWAP, int NRD, class GAP, class RD>
__device__ __forceinline__ void part(f32x4 (&acc)[MT][NT], const Set<KS, MT, NT>& X, GAP, RD&& rd) {
  constexpr int NMF = KS * MT * NT;
  MK_SB();
  sfor<NMF>([&](auto ic) {
    constexpr int i = decltype(ic)::v, ks = i / (MT * NT), m = (i / NT) % MT, n = i % NT;
    if constexpr (SWAP) mfma_acc(acc[m][n], X.b[ks][n], X.a[ks][m]); else mfma_acc(acc[m][n], X.a[ks][m], X.b[ks][n]);
    MK_SB();
    sfor<NRD>([&](auto kc) { if constexpr (GAP::gap(decltype(kc)::v) == i) rd(kc); });
    if constexpr (i == NMF - 2) MK_LGKM0();
    MK_SB();
  });
}
struct Gap1 { static constexpr int gap(int k) { return k; } };
struct GapG2 { static constexpr int gap(int k) { return k < 2 ? k : (k < 10 ? 2 + (k - 2) / 2 : (k < 12 ? k - 4 : 8 + (k - 12) / 2)); } };

struct AdP1 { Win a[2], a0[2], b[3]; };
template <int ST, int KSI, int K> __device__ __forceinline__ void rd_p1(const AdP1& ad, Set<1, 9, 3>& Y) {
  constexpr int SB = ST * P1_ST;
  if constexpr (K == 0) Y.a[0][0] = lds_ld<bf16x8>(wad<SB>(ad.a0[KSI]));
  else if constexpr (K <= 6) {
    constexpr int n = (K - 1) >> 1, h = (K - 1) & 1;
    const bf16x4 v = lds_tr(wad<SB + P1_A + 4096 * KSI + 512 * h>(ad.b[n]));
    if constexpr (h == 0) Y.t[0][n] = v; else Y.b[0][n] = __builtin_shufflevector(Y.t[0][n], v, 0, 1, 2, 3, 4, 5, 6, 7);
  } else { constexpr int m = K - 6; Y.a[0][m] = lds_ld<bf16x8>(wad<SB + m * 2048>(ad.a[KSI])); }
}
struct AdG3 { Win af, ar[2], b; };
template <int ST, int KSI, bool ROWS, int K> __device__ __forceinline__ void rd_g3(const AdG3& ad, Set<1, 2, 7>& Y) {
  constexpr int SB = ST * G3_ST;
  if constexpr (K == 0 || K == 8) {
    constexpr int m = K == 0 ? 0 : 1;
    if constexpr (ROWS) Y.a[0][m] = lds_ld<bf16x8>(wad<SB + m * 2048>(ad.ar[KSI]));
    else Y.a[0][m] = lds_ld<bf16x8>(wad<SB + KSI * 8192 + m * 1024>(ad.af));
  } else { constexpr int n = K - 1; Y.b[0][n] = lds_ld<bf16x8>(wad<SB + 16384 + (KSI * 7 + n) * 1024>(ad.b)); }
}
struct AdG2 { Win a, b[4]; };
template <int ST, int K> __device__ __forceinline__ void rd_g2(const AdG2& ad, Set<2, 2, 4>& Y) {
  constexpr int ks = K / 10, k = K % 10;
  if constexpr (k < 2) Y.a[ks][k] = lds_ld<bf16x8>(wad<G2_A + ST * 16384 + ks * 8192 + k * 1024>(ad.a));
  else {
    constexpr int n = (k - 2) >> 1, h = (k - 2) & 1;
    const bf16x4 v = lds_tr(wad<G2_B + ST * 8192 + 4096 * ks + 512 * h>(ad.b[n]));
    if constexpr (h == 0) Y.t[ks][n] = v; else Y.b[ks][n] = __builtin_shufflevector(Y.t[ks][n], v, 0, 1, 2, 3, 4, 5, 6, 7);
  }
}
__device__ __forceinline__ u32 tr64_lane(const Ctx& c, int strip) {
  const int f = (c.q >> 1) | ((c.g & 1) << 1);
  return (u32)((strip >> 2) * 8192 + 128 * (8 * c.g + c.q) + 16 * ((2 * (strip & 3) + (c.p >> 1)) ^ (2 * f)) + 8 * (c.p & 1));
}

template <int ST, bool LAST> __device__ __forceinline__ void tile_p1(f32x4 (&acc)[9][3], Set<1, 9, 3>& X, Set<1, 9, 3>& Y, const AdP1& ad) {
  part<1, 9, 3, true, 15>(acc, X, Gap1{}, [&](auto kc) { rd_p1<ST, 1, decltype(kc)::v>(ad, Y); });
  bar_c();
  part<1, 9, 3, true, LAST ? 0 : 15>(acc, Y, Gap1{}, [&](auto kc) { rd_p1<(ST + 1) % 3, 0, decltype(kc)::v>(ad, X); });
}
template <int ST, bool SWAP, bool ROWS, bool NROWS, bool LAST>
__device__ __forceinline__ void tile_g3(f32x4 (&acc)[2][7], Set<1, 2, 7>& X, Set<1, 2, 7>& Y, const AdG3& ad) {
  part<1, 2, 7, SWAP, 9>(acc, X, Gap1{}, [&](auto kc) { rd_g3<ST, 1, ROWS, decltype(kc)::v>(ad, Y); });
  bar_c();
  part<1, 2, 7, SWAP, LAST ? 0 : 9>(acc, Y, Gap1{}, [&](auto kc) { rd_g3<(ST + 1) % 4, 0, NROWS, decltype(kc)::v>(ad, X); });
}
template <int ST, bool BAR, bool LAST> __device__ __forceinline__ void tile_g2(f32x4 (&acc)[2][4], Set<2, 2, 4>& X, Set<2, 2, 4>& Y, const AdG2& ad) {
  if constexpr (BAR) bar_c();
  part<2, 2, 4, true, LAST ? 0 : 20>(acc, X, GapG2{}, [&](auto kc) { rd_g2<(ST + 1) % 6, decltype(kc)::v>(ad, Y); });
}
template <int NG, bool LASTBAR> __device__ __forceinline__ void gemm_g2(f32x4 (&acc)[2][4], const Ctx& c) {
  AdG2 ad;
  ad.a = mk_win(c.lds + (u32)(c.wid * 2048 + c.lane * 16));
#pragma unroll
  for (int n = 0; n < 4; ++n) ad.b[n] = mk_win(c.lds + tr64_lane(c, n));
  Set<2, 2, 4> X, Y;
  bar_c();
  sfor<20>([&](auto kc) { rd_g2<0, decltype(kc)::v>(ad, X); });
  MK_LGKM0();
#pragma unroll 1
  for (int it = 0; it < NG; ++it) {
    tile_g2<0, true, false>(acc, X, Y, ad); tile_g2<1, true, false>(acc, Y, X, ad); tile_g2<2, true, false>(acc, X, Y, ad);
    tile_g2<3, true, false>(acc, Y, X, ad); tile_g2<4, true, false>(acc, X, Y, ad); tile_g2<5, true, false>(acc, Y, X, ad);
  }
  tile_g2<0, true, false>(acc, X, Y, ad); tile_g2<1, true, false>(acc, Y, X, ad); tile_g2<2, true, false>(acc, X, Y, ad);
  tile_g2<3, LASTBAR, true>(acc, Y, X, ad);
  mfma_out();
}

__device__ __forceinline__ void attention(const Ctx& c) {
  const int hh = c.wid & 1, T = c.wid >> 1;
  const int lo = (c.rp == 0 && T == 0) ? 8 : 0;
  const float sc = 0.17677669529663687f * 1.4426950408889634f;
#pragma unroll
  for (int par = 0; par < 2; ++par) {
    const u32 bq = c.lds + AT_BASE + (u32)(((0 * 2 + hh) * 2 + par) * AT_BLK);
    const u32 bk = c.lds + AT_BASE + (u32)(((1 * 2 + hh) * 2 + par) * AT_BLK);
    const u32 bv = c.lds + AT_BASE + (u32)(((2 * 2 + hh) * 2 + par) * AT_BLK);
    const bf16x8 qf = lds_ld<bf16x8>(bq + (u32)((8 + 16 * T + c.r16) * AT_RS + 16 * c.g));
    f32x4 s[2];
    bf16x8 vf[2];
#pragma unroll
    for (int k2 = 0; k2 < 2; ++k2) {
      const int kr = min(16 * T + 16 * k2 + c.r16, 71);
      const bf16x8 kf = lds_ld<bf16x8>(bk + (u32)(kr * AT_RS + 16 * c.g));
      s[k2] = __builtin_amdgcn_mfma_f32_16x16x32_bf16(kf, qf, (f32x4){0.f, 0.f, 0.f, 0.f}, 0, 0, 0);
    }
#pragma unroll
    for (int dt = 0; dt < 2; ++dt) {
      const int v0 = 16 * T + 4 * c.g + c.q, v1 = min(v0 + 16, 71);
      const bf16x4 a = lds_tr(bv + (u32)(v0 * AT_RS + 32 * dt + 8 * c.p));
      const bf16x4 b = lds_tr(bv + (u32)(v1 * AT_RS + 32 * dt + 8 * c.p));
      vf[dt] = __builtin_shufflevector(a, b, 0, 1, 2, 3, 4, 5, 6, 7);
    }
    float e[8], mx = -3.0e38f;
#pragma unroll
    for (int j = 0; j < 8; ++j) {
      const int ak = 16 * (j >> 2) + 4 * c.g + (j & 3), w = 8 + c.r16 - ak;
      const bool ok = (w >= 0) && (w <= 7) && (ak >= lo);
      e[j] = ok ? s[j >> 2][j & 3] * sc : -3.0e38f;
      mx = fmaxf(mx, e[j]);
    }
    mx = maxg(mx);
    float sum = 0.f;
#pragma unroll
    for (int j = 0; j < 8; ++j) { e[j] = __builtin_amdgcn_exp2f(e[j] - mx); sum += e[j]; }
    sum = sumg(sum);
    const float inv = __builtin_amdgcn_rcpf(sum);
    const u32x4 pw = {pk_bf16(e[0] * inv, e[1] * inv), pk_bf16(e[2] * inv, e[3] * inv), pk_bf16(e[4] * inv, e[5] * inv), pk_bf16(e[6] * inv, e[7] * inv)};
    const bf16x8 pf = __builtin_bit_cast(bf16x8, pw);
    const f32x4 o0 = __builtin_amdgcn_mfma_f32_16x16x32_bf16(vf[0], pf, (f32x4){0.f, 0.f, 0.f, 0.f}, 0, 0, 0);
    const f32x4 o1 = __builtin_amdgcn_mfma_f32_16x16x32_bf16(vf[1], pf, (f32x4){0.f, 0.f, 0.f, 0.f}, 0, 0, 0);
    const u32x4 ov = pair_swap((u32x2){pk_bf16(o0[0], o0[1]), pk_bf16(o0[2], o0[3])}, (u32x2){pk_bf16(o1[0], o1[1]), pk_bf16(o1[2], o1[3])});
    const int G = ((c.g & 1) << 1) | (c.g >> 1);
    const u32 off = WS_O + (u32)(((c.rp * 32 + 2 * c.cj + hh) * 8 + 2 * T + par) * 1024 + (16 * G + c.r16) * 16);
    __builtin_amdgcn_raw_buffer_store_b128(ov, c.rws, (int)off, 0, SC1);
  }
  publish(c, FO, c.wid);
}

__device__ __forceinline__ void consumer(const Params& P, Ctx& c) {
  load_tag(c);
  const int w = c.wid, G = ((c.g & 1) << 1) | (c.g >> 1);
  {
    if (c.lane < 12) lds_st<u32>(c.lds + (u32)((c.lane >> 2) * P1_ST + P1_ONES + 4 * (c.lane & 3)), 0x3f803f80u);
    AdP1 ad;
#pragma unroll
    for (int ks = 0; ks < 2; ++ks) {
      const u32 a = (u32)(c.r16 * 128 + (((4 * ks + c.g) ^ (c.r16 >> 1)) * 16));
      ad.a[ks] = mk_win(c.lds + a);
      ad.a0[ks] = mk_win(c.lds + (c.r16 == 0 ? (u32)P1_ONES : a));
    }
#pragma unroll
    for (int n = 0; n < 3; ++n) ad.b[n] = mk_win(c.lds + tr64_lane(c, 3 * w + n));
    f32x4 acc[9][3];
#pragma unroll
    for (int m = 0; m < 9; ++m)
#pragma unroll
      for (int n = 0; n < 3; ++n) { acc[m][n] = (f32x4){0.f, 0.f, 0.f, 0.f}; asm volatile("" : "+v"(acc[m][n])); }
    Set<1, 9, 3> X, Y;
    MK_LGKM0();
    bar_c();
    sfor<15>([&](auto kc) { rd_p1<0, 0, decltype(kc)::v>(ad, X); });
    MK_LGKM0();
#pragma unroll 1
    for (int it = 0; it < 5; ++it) { tile_p1<0, false>(acc, X, Y, ad); tile_p1<1, false>(acc, X, Y, ad); tile_p1<2, false>(acc, X, Y, ad); }
    tile_p1<0, true>(acc, X, Y, ad);
    mfma_out();
    f32x4 cs[3];
#pragma unroll
    for (int n = 0; n < 3; ++n)
#pragma unroll
      for (int r = 0; r < 4; ++r) cs[n][r] = dpp<0x150>(acc[0][n][r]);
#pragma unroll
    for (int m = 0; m < 9; ++m) {
      const int par = m == 0 ? (c.r16 >> 3) : ((m - 1) & 1);
      const int a = m == 0 ? (c.r16 & 7) : 8 + 16 * ((m - 1) >> 1) + c.r16;
      const f32x2 st = lds_ld<f32x2>(c.lds + T_XST + (u32)((2 * a + par) * 8));
#pragma unroll
      for (int n = 0; n < 3; ++n) {
        const int S = 3 * w + n;
        const f32x4 v = (acc[m][n] - st[0] * cs[n]) * st[1];
        lds_st<u32x2>(c.lds + AT_BASE + (u32)(((S >> 1) * 2 + par) * AT_BLK + a * AT_RS + (S & 1) * 32 + 8 * c.g),
                      (u32x2){pk_bf16(v[0], v[1]), pk_bf16(v[2], v[3])});
      }
    }
  }
  MK_LGKM0();
  bar_c();
  const rsrc_t rx = mk_rsrc(P.x, 2048u * 2048u);
  u32x4 xr[2][2];
#pragma unroll
  for (int m = 0; m < 2; ++m)
#pragma unroll
    for (int pr = 0; pr < 2; ++pr)
      xr[m][pr] = __builtin_amdgcn_raw_buffer_load_b128(rx, (128 * c.rp + 32 * w + 2 * c.r16 + m) * 2048 + (64 * c.cj + 32 * pr + 8 * G) * 2, 0, 0);
  attention(c);
  u32x2 hb[2][4];
  {
    f32x4 acc[2][4];
#pragma unroll
    for (int m = 0; m < 2; ++m)
#pragma unroll
      for (int pr = 0; pr < 2; ++pr) {
        const u32x4 t = pair_swap((u32x2){xr[m][pr][0], xr[m][pr][1]}, (u32x2){xr[m][pr][2], xr[m][pr][3]});
        acc[m][2 * pr] = (f32x4){bf_lo(t[0]), bf_hi(t[0]), bf_lo(t[1]), bf_hi(t[1])};
        acc[m][2 * pr + 1] = (f32x4){bf_lo(t[2]), bf_hi(t[2]), bf_lo(t[3]), bf_hi(t[3])};
        asm volatile("" : "+v"(acc[m][2 * pr]), "+v"(acc[m][2 * pr + 1]));
      }
    bar_c();
    gemm_g2<2, true>(acc, c);
#pragma unroll
    for (int m = 0; m < 2; ++m) {
#pragma unroll
      for (int n = 0; n < 4; ++n) hb[m][n] = (u32x2){pk_bf16(acc[m][n][0], acc[m][n][1]), pk_bf16(acc[m][n][2], acc[m][n][3])};
#pragma unroll
      for (int pr = 0; pr < 2; ++pr)
        __builtin_amdgcn_raw_buffer_store_b128(pair_swap(hb[m][2 * pr], hb[m][2 * pr + 1]), c.rws,
            (int)(WS_H + (u32)(((c.rp * 32 + 2 * c.cj + pr) * 8 + 2 * w + m) * 1024 + (16 * G + c.r16) * 16)), 0, SC1);
    }
#pragma unroll
    for (int m = 0; m < 2; ++m) {
      float s = 0.f, q = 0.f;
#pragma unroll
      for (int n = 0; n < 4; ++n)
#pragma unroll
        for (int d = 0; d < 2; ++d) { const float a = bf_lo(hb[m][n][d]), b = bf_hi(hb[m][n][d]); s += a + b; q += a * a + b * b; }
      s = sumg(s); q = sumg(q);
      if (c.g == 0) __builtin_amdgcn_raw_buffer_store_b64((u32x2){__builtin_bit_cast(u32, s), __builtin_bit_cast(u32, q)}, c.rws,
          (int)(WS_HSTAT + (u32)(((c.rp * 16 + c.cj) * 128 + 32 * w + 2 * c.r16 + m) * 8)), 0, SC1);
    }
    publish(c, FHH, w);
  }
  {
    AdG3 ad;
    ad.af = mk_win(c.lds + (u32)(w * 2048 + c.lane * 16));
#pragma unroll
    for (int ks = 0; ks < 2; ++ks) ad.ar[ks] = mk_win(c.lds + (u32)((32 * w + c.r16) * 128 + (((4 * ks + c.g) ^ (c.r16 >> 1)) * 16)));
    ad.b = mk_win(c.lds + (u32)(c.lane * 16));
    float bsp[2];
#pragma unroll
    for (int m = 0; m < 2; ++m) bsp[m] = bf_lo((u32)P.bsp[128 * c.rp + 32 * w + 2 * c.r16 + m]);
    f32x4 acc[2][7];
#pragma unroll
    for (int m = 0; m < 2; ++m)
#pragma unroll
      for (int n = 0; n < 7; ++n) { acc[m][n] = (f32x4){0.f, 0.f, 0.f, 0.f}; asm volatile("" : "+v"(acc[m][n])); }
    asm volatile("" : "+v"(bsp[0]), "+v"(bsp[1]));
    Set<1, 2, 7> X, Y;
    bar_c();
    bar_c();
    sfor<9>([&](auto kc) { rd_g3<0, 0, false, decltype(kc)::v>(ad, X); });
    MK_LGKM0();
#pragma unroll 1
    for (int it = 0; it < 4; ++it) {
      tile_g3<0, false, false, false, false>(acc, X, Y, ad); tile_g3<1, false, false, false, false>(acc, X, Y, ad);
      tile_g3<2, false, false, false, false>(acc, X, Y, ad); tile_g3<3, false, false, false, false>(acc, X, Y, ad);
    }
    mfma_out();
    f32x4 z2[2][7];
    {
      f32x4 hs[4];
#pragma unroll
      for (int r = 0; r < 4; ++r) hs[r] = lds_ld<f32x4>(c.lds + T_HST + (u32)((32 * w + 8 * c.g + 2 * r) * 8));
      float sm[8], sq[8];
#pragma unroll
      for (int j = 0; j < 8; ++j) { sm[j] = 0.f; sq[j] = 0.f; }
#pragma unroll
      for (int n = 0; n < 7; ++n) {
        const float cs = lds_ld<float>(c.lds + T_CS + (u32)((16 * n + c.r16) * 4));
#pragma unroll
        for (int m = 0; m < 2; ++m)
#pragma unroll
          for (int r = 0; r < 4; ++r) {
            const float z = gelu_f((acc[m][n][r] - hs[r][2 * m] * cs) * hs[r][2 * m + 1]);
            z2[m][n][r] = z; sm[2 * r + m] += z; sq[2 * r + m] += z * z;
          }
      }
      float s = 0.f, q = 0.f;
#pragma unroll
      for (int j = 0; j < 8; ++j) { const float a = sum16(sm[j]), b = sum16(sq[j]); s = (c.r16 == j) ? a : s; q = (c.r16 == j) ? b : q; }
      const u32 su = __builtin_bit_cast(u32, s), qu = __builtin_bit_cast(u32, q);
      if (c.r16 < 8) __builtin_amdgcn_raw_buffer_store_b128((u32x4){su, c.tag_lo ^ qu, qu, ~c.tag_lo ^ su}, c.rws,
          (int)(WS_ZSTAT + (u32)(((c.rp * 16 + c.cj) * 128 + 32 * w + 8 * c.g + c.r16) * 16)), 0, SC1);
#pragma unroll
      for (int m = 0; m < 2; ++m) {
        const float nm = -lds_ld<float>(c.lds + T_HST + (u32)((32 * w + 2 * c.r16 + m) * 8));
#pragma unroll
        for (int n = 0; n < 7; ++n) { acc[m][n] = lds_ld<f32x4>(c.lds + T_CS + (u32)((112 + 16 * n + 4 * c.g) * 4)) * nm; asm volatile("" : "+v"(acc[m][n])); }
      }
    }
    mfma_in();
    sfor<16>([&](auto tc) {
      constexpr int t = 16 + decltype(tc)::v;
      tile_g3<t % 4, true, false, t == 31, false>(acc, X, Y, ad);
      if constexpr (t == NORM_T) {
        f32x4 zs[4];
#pragma unroll
        for (int r = 0; r < 4; ++r) zs[r] = lds_ld<f32x4>(c.lds + T_ZST + (u32)((32 * w + 8 * c.g + 2 * r) * 8));
#pragma unroll
        for (int n = 0; n < 7; ++n) {
          u32x4 v;
#pragma unroll
          for (int r = 0; r < 4; ++r) v[r] = pk_bf16((z2[0][n][r] - zs[r][0]) * zs[r][1], (z2[1][n][r] - zs[r][2]) * zs[r][3]);
          __builtin_amdgcn_raw_buffer_store_b128(v, c.rws, (int)(WS_Z2N + (u32)(((c.cj * 64 + 4 * c.rp + w) * 7 + n) * 1024 + c.lane * 16)), 0, SC1);
        }
      }
      if constexpr (t == FLAG_T) publish(c, FZ, w);
    });
    mfma_out();
#pragma unroll
    for (int m = 0; m < 2; ++m)
#pragma unroll
      for (int n = 0; n < 7; ++n) {
        lds_st<u32x2>(c.lds + Z1BUF + (u32)(((w * 2 + m) * 7 + n) * 512 + c.lane * 8),
                      (u32x2){pk_bf16(acc[m][n][0], acc[m][n][1]), pk_bf16(acc[m][n][2], acc[m][n][3])});
        acc[m][n] = (f32x4){bsp[m], bsp[m], bsp[m], bsp[m]};
        asm volatile("" : "+v"(acc[m][n]));
      }
    mfma_in();
#pragma unroll 1
    for (int it = 0; it < 7; ++it) {
      tile_g3<0, true, true, true, false>(acc, X, Y, ad); tile_g3<1, true, true, true, false>(acc, X, Y, ad);
      tile_g3<2, true, true, true, false>(acc, X, Y, ad); tile_g3<3, true, true, true, false>(acc, X, Y, ad);
    }
    tile_g3<0, true, true, true, false>(acc, X, Y, ad); tile_g3<1, true, true, true, false>(acc, X, Y, ad);
    tile_g3<2, true, true, true, false>(acc, X, Y, ad); tile_g3<3, true, true, true, true>(acc, X, Y, ad);
    mfma_out();
    {
      const int odd = c.cj & 1, kp = (7 * c.cj + 1) >> 1, ksg = (7 * c.cj + (odd ? -1 : 6)) >> 1;
#pragma unroll
      for (int m = 0; m < 2; ++m) {
        u32x2 uv[7];
#pragma unroll
        for (int n = 0; n < 7; ++n) {
          const u32x2 z = lds_ld<u32x2>(c.lds + Z1BUF + (u32)(((w * 2 + m) * 7 + n) * 512 + c.lane * 8));
          uv[n] = (u32x2){pk_bf16(bf_lo(z[0]) * acc[m][n][0], bf_hi(z[0]) * acc[m][n][1]), pk_bf16(bf_lo(z[1]) * acc[m][n][2], bf_hi(z[1]) * acc[m][n][3])};
        }
#pragma unroll
        for (int pr = 0; pr < 3; ++pr)
          __builtin_amdgcn_raw_buffer_store_b128((u32x4){uv[2 * pr][0], uv[2 * pr][1], uv[2 * pr + 1][0], uv[2 * pr + 1][1]}, c.rws,
              (int)(WS_U + (u32)(((c.rp * 56 + kp + pr) * 8 + 2 * w + m) * 1024 + c.lane * 16)), 0, SC1);
        __builtin_amdgcn_raw_buffer_store_b64(uv[6], c.rws,
            (int)(WS_U + (u32)(((c.rp * 56 + ksg) * 8 + 2 * w + m) * 1024 + (16 * (2 * odd + (c.g >> 1)) + c.r16) * 16 + 8 * (c.g & 1))), 0, SC1);
      }
      publish(c, FU, w);
    }
  }
  {
    f32x4 acc[2][4];
#pragma unroll
    for (int m = 0; m < 2; ++m)
#pragma unroll
      for (int n = 0; n < 4; ++n) {
        acc[m][n] = (f32x4){bf_lo(hb[m][n][0]), bf_hi(hb[m][n][0]), bf_lo(hb[m][n][1]), bf_hi(hb[m][n][1])};
        asm volatile("" : "+v"(acc[m][n]));
      }
    bar_c();
    gemm_g2<4, false>(acc, c);
    const rsrc_t ro = mk_rsrc(P.out, 2048u * 2048u);
#pragma unroll
    for (int m = 0; m < 2; ++m)
#pragma unroll
      for (int pr = 0; pr < 2; ++pr) {
        const f32x4 a = acc[m][2 * pr], b = acc[m][2 * pr + 1];
        __builtin_amdgcn_raw_buffer_store_b128(pair_swap((u32x2){pk_bf16(a[0], a[1]), pk_bf16(a[2], a[3])}, (u32x2){pk_bf16(b[0], b[1]), pk_bf16(b[2], b[3])}), ro,
            (128 * c.rp + 32 * w + 2 * c.r16 + m) * 2048 + (64 * c.cj + 32 * pr + 8 * G) * 2, 0, 0);
      }
    if (c.rp == 0 && c.cj == 0 && w == 0 && c.lane == 0)
      __builtin_amdgcn_raw_buffer_store_b64((u32x2){c.tag_lo, c.tag_hi}, c.rws, (int)WS_EPOCH, 0, SC1);
  }
}

template <int N, int AUX> __device__ __forceinline__ void issue_v(rsrc_t r, u32 dst, const u32 (&vo)[N], u32 so) {
  sfor<N>([&](auto ic) { constexpr int s = decltype(ic)::v; glds<(s & 3) * 1024, AUX>(r, dst + (s >> 2) * 4096, vo[s], so); });
}
template <int N, int AUX> __device__ __forceinline__ void issue_l(rsrc_t r, u32 dst, u32 vo, u32 so) {
  sfor<N>([&](auto ic) { constexpr int s = decltype(ic)::v; glds<(s & 3) * 1024, AUX>(r, dst + (s >> 2) * 4096, vo, so + (s >> 2) * 4096); });
}
__device__ __forceinline__ u32 tr64_src(int lane, int j, int ldb_bytes) {
  const int k = 8 * j + (lane >> 3), f = ((k >> 1) & 1) | (((k >> 3) & 1) << 1);
  return (u32)(k * ldb_bytes + (((lane & 7) ^ (2 * f)) * 16));
}
template <int T, bool LASTBAR>
__device__ __forceinline__ void load_g2(const Ctx& c, int lw, u32 abase, rsrc_t rb, const u32 (&vb)[4], u32 bstep) {
  const u32 va = abase + (u32)(lw * 8192 + c.lane * 16), da = c.lds + G2_A + (u32)(lw * 8192), db = c.lds + G2_B + (u32)(lw * 4096);
  auto A = [&](int t, int st) { issue_l<8, SC1>(c.rws, da + (u32)(st * 16384), va, (u32)(t * 16384)); };
  auto B = [&](int t, int st) { issue_v<4, 0>(rb, db + (u32)(st * 8192), vb, (u32)t * bstep); };
  A(0, 0); A(1, 1); MK_WAITV(8); bar_h();
  A(2, 2); A(3, 3); MK_WAITV(16); bar_h();
  A(4, 4); A(5, 5); A(6, 0); B(6, 0); MK_WAITV(36); bar_h();
  A(7, 1); B(7, 1); MK_WAITV(40); bar_h();
  A(8, 2); B(8, 2); MK_WAITV(44); bar_h();
  int st = 3;
#pragma unroll 1
  for (int t = 9; t < T; ++t) { A(t, st); B(t, st); MK_WAITV(48); bar_h(); st = st == 5 ? 0 : st + 1; }
  MK_WAITV(36); bar_h(); MK_WAITV(24); bar_h(); MK_WAITV(12); bar_h(); MK_WAITV(0); bar_h();
  if (LASTBAR) bar_h();
}

__device__ __forceinline__ void loader(const Params& P, Ctx& c) {
  const int lw = c.wid - 4, lane = c.lane;
  {
    const rsrc_t rx = mk_rsrc((const char*)P.x - 4096, 2048u * 2048u + 4096u);
    const rsrc_t rw = mk_rsrc((const char*)P.wqkv - 4096, 1024u * 6144u + 4096u);
    u32 va[9], vb[12];
#pragma unroll
    for (int s = 0; s < 9; ++s) {
      const int rho = 8 * (9 * lw + s) + (lane >> 3), m = rho >> 4, sl = rho & 15;
      const int il = m == 0 ? (2 * (sl & 7) + (sl >> 3)) : 16 + 32 * ((m - 1) >> 1) + 2 * sl + ((m - 1) & 1);
      const int row = max(128 * c.rp - 16 + il, 0);
      va[s] = (u32)(row * 2048 + (((lane & 7) ^ ((rho >> 1) & 7)) * 16) + 4096 - (s & 3) * 1024);
    }
#pragma unroll
    for (int s = 0; s < 12; ++s) {
      const int j = 12 * lw + s;
      vb[s] = tr64_src(lane, j & 7, 6144) + (u32)(((j >> 3) * 1024 + 64 * c.cj) * 2 + 4096 - (s & 3) * 1024);
    }
    auto I = [&](int t, int st) {
      const u32 sb = c.lds + (u32)(st * P1_ST);
      issue_v<9, 0>(rx, sb + (u32)(lw * 9216), va, (u32)(t * 128));
      issue_v<12, 0>(rw, sb + (u32)(P1_A + lw * 12288), vb, (u32)(t * 64 * 6144));
    };
    I(0, 0); I(1, 1); MK_WAITV(21); bar_h();
    I(2, 2); MK_WAITV(21); bar_h();
    int st = 0;
#pragma unroll 1
    for (int t = 3; t < 16; ++t) { I(t, st); MK_WAITV(21); bar_h(); st = st == 2 ? 0 : st + 1; }
    MK_WAITV(0); bar_h();
    load_tag(c);
    bar_h();
  }
  {
    const rsrc_t rb = mk_rsrc((const char*)P.wo - 4096, 1024u * 2048u + 4096u);
    u32 vb[4];
#pragma unroll
    for (int s = 0; s < 4; ++s) vb[s] = tr64_src(lane, 4 * lw + s, 2048) + (u32)(128 * c.cj + 4096 - s * 1024);
#pragma unroll 1
    for (int t = 0; t < 6; ++t) issue_v<4, 0>(rb, c.lds + G2_B + (u32)(lw * 4096 + t * 8192), vb, (u32)(t * 64 * 2048));
    bar_h();
    attention(c);
    bar_h();
    load_g2<16, true>(c, lw, WS_O + (u32)(c.rp * 32 * 8192), rb, vb, 64 * 2048);
  }
  {
    const rsrc_t rs = mk_rsrc((const char*)P.wsp - 4096, 2048u * 4096u + 4096u);
    const u32 vah = WS_H + (u32)(c.rp * 32 * 8192 + lw * 8192 + lane * 16);
    const u32 vbw = WS_WINP + (u32)(c.cj * 32 * 14336 + lw * 7168 + lane * 16);
    const u32 vbz = WS_Z2N + (u32)(c.cj * 32 * 14336 + lw * 7168 + lane * 16);
    const u32 da = c.lds + (u32)(lw * 8192), db = c.lds + (u32)(16384 + lw * 7168);
    auto A3 = [&](int t, int st) { issue_l<8, SC1>(c.rws, da + (u32)(st * G3_ST), vah, (u32)((t & 15) * 16384)); };
    auto B3 = [&](int t, int st) { issue_l<7, SC1>(c.rws, db + (u32)(st * G3_ST), vbw, (u32)(t * 14336)); };
    B3(0, 0); B3(1, 1); B3(2, 2); B3(3, 3);
    u32 vs[8];
#pragma unroll
    for (int s = 0; s < 8; ++s) {
      const int rho = 8 * (8 * lw + s) + (lane >> 3), m = rho >> 4, sl = rho & 15;
      const int row = 128 * c.rp + 32 * (m >> 1) + 2 * sl + (m & 1);
      vs[s] = (u32)(row * 4096 + (((lane & 7) ^ ((rho >> 1) & 7)) * 16) + 4096 - (s & 3) * 1024);
    }
    bar_h();
    A3(0, 0); A3(1, 1); MK_WAITV(8); bar_h();
    A3(2, 2); A3(3, 3); MK_WAITV(16); bar_h();
    A3(4, 0); B3(4, 0); MK_WAITV(23); bar_h();
#pragma unroll 1
    for (int t = 5; t < 32; ++t) { A3(t, t & 3); B3(t, t & 3); MK_WAITV(30); bar_h(); }
#pragma unroll 1
    for (int t = 0; t < 32; ++t) {
      issue_v<8, 0>(rs, da + (u32)((t & 3) * G3_ST), vs, (u32)(t * 128));
      issue_l<7, SC1>(c.rws, db + (u32)((t & 3) * G3_ST), vbz, (u32)(t * 14336));
      MK_WAITV(30); bar_h();
    }
    MK_WAITV(15); bar_h(); MK_WAITV(0); bar_h();
    bar_h();
  }
  {
    const rsrc_t rb = mk_rsrc((const char*)P.wout - 4096, 1792u * 2048u + 4096u);
    u32 vb[4];
#pragma unroll
    for (int s = 0; s < 4; ++s) vb[s] = tr64_src(lane, 4 * lw + s, 2048) + (u32)(128 * c.cj + 4096 - s * 1024);
#pragma unroll 1
    for (int t = 0; t < 6; ++t) issue_v<4, 0>(rb, c.lds + G2_B + (u32)(lw * 4096 + t * 8192), vb, (u32)(t * 64 * 2048));
    bar_h();
    load_g2<28, false>(c, lw, WS_U + (u32)(c.rp * 56 * 8192), rb, vb, 64 * 2048);
  }
}

__device__ __forceinline__ void bars(int n) {
#pragma unroll 1
  for (int i = 0; i < n; ++i) bar_h();
}
__device__ __forceinline__ void worker(const Params& P, Ctx& c) {
  const int v = c.wid - 6, lane = c.lane;
  {
    const rsrc_t rx = mk_rsrc(P.x, 2048u * 2048u);
    const rsrc_t rw = mk_rsrc((const char*)P.win - 4096, 1024u * 7168u + 4096u);
    u32x4 xr[4][2];
#pragma unroll
    for (int i = 0; i < 4; ++i)
#pragma unroll
      for (int h = 0; h < 2; ++h) xr[i][h] = __builtin_amdgcn_raw_buffer_load_b128(rx, (128 * c.rp + 8 * c.cj + 4 * v + i) * 2048 + h * 1024 + lane * 16, 0, 0);
    const u32 stg = c.lds + WSTG + (u32)(v * 14336);
    sfor<14>([&](auto ic) {
      constexpr int j = decltype(ic)::v;
      const int i = 64 * j + lane, k = i / 14, ch = i - 14 * k;
      glds<(j & 3) * 1024, 0>(rw, stg + (j >> 2) * 4096, (u32)((64 * c.rp + k) * 7168 + ((v == 0 ? 1792 : 0) + 112 * c.cj) * 2 + ch * 16 + 4096 - (j & 3) * 1024), 0);
    });
    bar_h();
    load_tag(c);
    float f[4][16], mean[4];
#pragma unroll
    for (int i = 0; i < 4; ++i) {
      float s = 0.f;
#pragma unroll
      for (int h = 0; h < 2; ++h)
#pragma unroll
        for (int d = 0; d < 4; ++d) { const float a = bf_lo(xr[i][h][d]), b = bf_hi(xr[i][h][d]); f[i][8 * h + 2 * d] = a; f[i][8 * h + 2 * d + 1] = b; s += a; s += b; }
      mean[i] = sumg(sum16(s)) * (1.0f / 1024.0f);
      asm volatile("" : "+v"(mean[i]));
      if (i & 1) bar_h();
    }
    float sm = 0.f, sr = 0.f;
#pragma unroll
    for (int i = 0; i < 4; ++i) {
      float q = 0.f;
#pragma unroll
      for (int e = 0; e < 16; ++e) { const float d = f[i][e] - mean[i]; q += d * d; }
      const float rs = __builtin_amdgcn_rsqf(sumg(sum16(q)) * (1.0f / 1024.0f) + 1e-5f);
      sm = lane == i ? mean[i] : sm; sr = lane == i ? rs : sr;
      asm volatile("" : "+v"(sm), "+v"(sr));
      if (i & 1) bar_h();
    }
    if (lane < 4) __builtin_amdgcn_raw_buffer_store_b64((u32x2){__builtin_bit_cast(u32, sm), __builtin_bit_cast(u32, sr)}, c.rws,
        (int)(WS_XSTAT + (u32)((128 * c.rp + 8 * c.cj + 4 * v + lane) * 8)), 0, SC1);
    bar_h();
    publish(c, FX, v);
    const int odd = c.cj & 1;
    float cs[7];
#pragma unroll
    for (int n = 0; n < 7; ++n) {
      const int cb = n < 6 ? ((odd ? 16 : 0) + 32 * (n >> 1) + 8 * c.p + 4 * (n & 1)) : ((odd ? 0 : 96) + 4 * c.p);
      float s = 0.f;
#pragma unroll
      for (int kl = 0; kl < 2; ++kl) {
        const u32 a = stg + (u32)((32 * kl + 8 * c.g + c.q) * 224 + cb * 2);
        const bf16x4 lo = lds_tr(a), hi = lds_tr(a + 4 * 224);
        const u32x4 fr = __builtin_bit_cast(u32x4, __builtin_shufflevector(lo, hi, 0, 1, 2, 3, 4, 5, 6, 7));
        __builtin_amdgcn_raw_buffer_store_b128(fr, c.rws, (int)(WS_WINP + (u32)((((c.cj * 2 + v) * 32 + 2 * c.rp + kl) * 7 + n) * 1024 + lane * 16)), 0, SC1);
#pragma unroll
        for (int d = 0; d < 4; ++d) { s += bf_lo(fr[d]); s += bf_hi(fr[d]); }
      }
      cs[n] = sumg(s);
      asm volatile("" : "+v"(cs[n]));
      if (n & 1) bar_h();
    }
    if (lane < 16) {
#pragma unroll
      for (int n = 0; n < 7; ++n)
        __builtin_amdgcn_raw_buffer_store_b32(asu(cs[n]), c.rws, (int)(WS_CS + (u32)(((c.rp * 16 + c.cj) * 256 + 112 * v + 16 * n + lane) * 4)), 0, SC1);
    }
    bar_h(); bar_h();
    publish(c, FW, v);
    bar_h();
    if (v == 0) {
      const int i = lane < 36 ? (lane >> 1) : 0;
      const int frp = i < 16 ? c.rp : max(c.rp - 1, 0), fcj = i < 16 ? i : i - 2;
      poll64(c, flag_off(FX, 0, 0) + (u32)((frp * 16 + fcj) * 128 + 8 * (lane & 1)));
#pragma unroll
      for (int i3 = 0; i3 < 3; ++i3) {
        const int il = min(64 * i3 + lane, 143);
        const u32x2 st = __builtin_amdgcn_raw_buffer_load_b64(c.rws, (int)(WS_XSTAT + (u32)(max(128 * c.rp - 16 + il, 0) * 8)), 0, SC1);
        lds_st<u32x2>(c.lds + T_XST + (u32)(il * 8), st);
      }
    }
    bars(5);
  }
  bar_h();
  attention(c);
  if (v == 0) poll128(c, flag_off(FO, 0, 0) + (u32)((c.rp * 16 + (lane >> 2)) * 128 + 16 * (lane & 3)));
  bar_h();
  if (v == 0) poll64(c, flag_off(FW, 0, 0) + (u32)((((lane >> 1) & 15) * 16 + c.cj) * 128 + 8 * (lane & 1)));
  bars(17);
  if (v == 0) poll64(c, flag_off(FHH, 0, 0) + (u32)((c.rp * 16 + (lane >> 2)) * 128 + 8 * (lane & 3)));
  bar_h();
  {
    u32x2 hp[16], cp[16];
    const int cl = min(lane, 55);
#pragma unroll
    for (int j = 0; j < 16; ++j) {
      hp[j] = __builtin_amdgcn_raw_buffer_load_b64(c.rws, (int)(WS_HSTAT + (u32)(((c.rp * 16 + j) * 128 + 64 * v + lane) * 8)), 0, SC1);
      cp[j] = __builtin_amdgcn_raw_buffer_load_b64(c.rws, (int)(WS_CS + (u32)(((j * 16 + c.cj) * 256 + 112 * v + 2 * cl) * 4)), 0, SC1);
    }
    bars(4);
    float s = 0.f, q = 0.f, c0 = 0.f, c1 = 0.f;
#pragma unroll
    for (int j = 0; j < 16; ++j) {
      s += asf(hp[j][0]); q += asf(hp[j][1]);
      c0 += asf(cp[j][0]); c1 += asf(cp[j][1]);
    }
    const float mean = s * (1.0f / 1024.0f);
    const float rs = __builtin_amdgcn_rsqf(fmaxf(q * (1.0f / 1024.0f) - mean * mean, 0.f) + 1e-5f);
    lds_st<f32x2>(c.lds + T_HST + (u32)((64 * v + lane) * 8), (f32x2){mean, rs});
    lds_st<f32x2>(c.lds + T_CS + (u32)((112 * v + 2 * cl) * 4), (f32x2){c0, c1});
    bars(15);
    u32x4 zp[16];
    auto sweep = [&]() {
#pragma unroll
      for (int j = 0; j < 16; ++j) zp[j] = __builtin_amdgcn_raw_buffer_load_b128(c.rws, (int)(WS_ZSTAT + (u32)(((c.rp * 16 + j) * 128 + 64 * v + lane) * 16)), 0, SC1);
    };
    sweep();
    bars(NORM_T - 19);
#pragma unroll 1
    for (int sp = 0; sp < SPIN_CAP; ++sp) {
      bool ok = true;
#pragma unroll
      for (int j = 0; j < 16; ++j) ok = ok && ((zp[j][1] ^ zp[j][2]) == c.tag_lo) && ((zp[j][3] ^ zp[j][0]) == ~c.tag_lo);
      if (__all(ok)) break;
      asm volatile("" ::: "memory");
      sweep();
    }
    float zs = 0.f, zq = 0.f;
#pragma unroll
    for (int j = 0; j < 16; ++j) { zs += asf(zp[j][0]); zq += asf(zp[j][2]); }
    const float zm = zs * (1.0f / 1792.0f);
    const float zr = __builtin_amdgcn_rsqf(fmaxf(zq * (1.0f / 1792.0f) - zm * zm, 0.f) + 1e-5f);
    lds_st<f32x2>(c.lds + T_ZST + (u32)((64 * v + lane) * 8), (f32x2){zm, zr});
    bars(29 - NORM_T);
    if (v == 0) poll64(c, flag_off(FZ, 0, 0) + (u32)(((lane >> 2) * 16 + c.cj) * 128 + 8 * (lane & 3)));
    bars(5);
#pragma unroll 1
    for (int i = 0; i < 28; ++i) {
      const int b = 28 * v + i, wm = b / 7;
      const float r = lds_ld<float>(c.lds + T_HST + (u32)((32 * (wm >> 1) + 2 * c.r16 + (wm & 1)) * 8 + 4));
      const u32 a = c.lds + Z1BUF + (u32)(b * 512 + lane * 8);
      const u32x2 z = lds_ld<u32x2>(a);
      lds_st<u32x2>(a, (u32x2){pk_bf16(gelu_f(bf_lo(z[0]) * r), gelu_f(bf_hi(z[0]) * r)), pk_bf16(gelu_f(bf_lo(z[1]) * r), gelu_f(bf_hi(z[1]) * r))});
      bar_h();
    }
    bars(3);
  }
  if (v == 0) poll64(c, flag_off(FU, 0, 0) + (u32)((c.rp * 16 + (lane >> 2)) * 128 + 8 * (lane & 3)));
  bar_h();
}

__global__ __launch_bounds__(512, 2) void fused_block(Params P) {
  extern __shared__ __attribute__((aligned(16))) char smem[];
  Ctx c;
  c.lane = threadIdx.x & 63;
  c.wid = __builtin_amdgcn_readfirstlane(threadIdx.x >> 6);
  const int xcd = blockIdx.x & 7, idx = blockIdx.x >> 3;
  c.rp = (xcd & 3) * 4 + (idx & 3);
  c.cj = (xcd >> 2) * 8 + (idx >> 2);
  c.r16 = c.lane & 15; c.g = c.lane >> 4; c.q = (c.lane >> 2) & 3; c.p = c.lane & 3;
  c.lds = (u32)(size_t)(LAS char*)smem;
  c.rws = mk_rsrc(P.ws, WS_END);
  c.tag_lo = 0; c.tag_hi = 0;
  if (c.wid < 4) consumer(P, c); else if (c.wid < 6) loader(P, c); else worker(P, c);
}

}

extern "C" void kernel_launch(void* const* d_in, const int* in_sizes, int n_in, void* d_out, int out_size, void* d_ws, size_t ws_size,
                              hipStream_t stream) {
  (void)in_sizes; (void)n_in; (void)out_size; (void)ws_size;
  mk::Params P;
  P.x = (const mk::u16*)d_in[0]; P.wqkv = (const mk::u16*)d_in[1]; P.wo = (const mk::u16*)d_in[2];
  P.win = (const mk::u16*)d_in[4]; P.wsp = (const mk::u16*)d_in[5]; P.bsp = (const mk::u16*)d_in[6]; P.wout = (const mk::u16*)d_in[7];
  P.out = (mk::u16*)d_out; P.ws = (char*)d_ws;
  (void)hipFuncSetAttribute((const void*)mk::fused_block, hipFuncAttributeMaxDynamicSharedMemorySize, mk::LDS_BYTES);
  hipLaunchKernelGGL(mk::fused_block, dim3(256), dim3(512), mk::LDS_BYTES, stream, P);
}
```

```cpp
#include <hip/hip_runtime.h>

#define MK_ABL 0
#define MK_PREFETCH 15
namespace mk {

#define LAS __attribute__((address_space(3)))
typedef LAS void* lds_vp;
typedef unsigned short u16;
typedef unsigned int u32;
typedef unsigned long long u64;
typedef short bf16x8 __attribute__((ext_vector_type(8)));
typedef short bf16x4 __attribute__((ext_vector_type(4)));
typedef float f32x2 __attribute__((ext_vector_type(2)));
typedef float f32x4 __attribute__((ext_vector_type(4)));
typedef u32 u32x2 __attribute__((ext_vector_type(2)));
typedef u32 u32x4 __attribute__((ext_vector_type(4)));
typedef __bf16 bf16x2n __attribute__((ext_vector_type(2)));
typedef __amdgpu_buffer_rsrc_t rsrc_t;

constexpr u32 WS_EPOCH = 0;
constexpr u32 WS_FLAGS = 4096;
enum { FW = 1, FO = 2, FHH = 3, FZ = 4, FU = 5 };
constexpr u32 WS_XSTAT = 256 * 1024;
constexpr u32 WS_HSTAT = 512 * 1024;
constexpr u32 WS_CS    = 768 * 1024;
constexpr u32 WS_ZSTAT = 1024 * 1024;
constexpr u32 WS_O     = 2u << 20;
constexpr u32 WS_H     = 6u << 20;
constexpr u32 WS_WINP  = 10u << 20;
constexpr u32 WS_Z2N   = 18u << 20;
constexpr u32 WS_U     = 26u << 20;
constexpr u32 WS_END   = 34u << 20;
__host__ __device__ constexpr u32 flag_off(int kind, int rp, int cj) { return WS_FLAGS + (u32)((kind * 256 + rp * 16 + cj) * 128); }

constexpr int P1_A = 144 * 128, P1_B = 192 * 128, P1_ST = P1_A + P1_B + 16;
constexpr int P1_ONES = P1_A + P1_B;
constexpr int WSTG = 3 * P1_ST;
constexpr int G2_B = 0, G2_A = 6 * 8192;
constexpr int AT_BASE = G2_A, AT_RS = 80, AT_BLK = 72 * AT_RS;
constexpr int G3_ST = 16384 + 14336;
constexpr int Z1BUF = 4 * G3_ST;
constexpr int TAB = 156 * 1024;
constexpr int T_XST = TAB;
constexpr int T_ZM = TAB, T_ZR = TAB + 512, T_HM = TAB + 1152, T_HR = T_HM + 512, T_CS = T_HR + 512;
constexpr int T_TAG = T_CS + 896, T_DUMMY = T_TAG + 16;
constexpr int LDS_BYTES = 160 * 1024;
static_assert(WSTG + 2 * 14336 <= TAB, "lds");
static_assert(AT_BASE + 12 * AT_BLK <= TAB, "lds");
static_assert(G2_A + 6 * 16384 <= TAB, "lds");
static_assert(Z1BUF + 28672 <= TAB, "lds");
static_assert(T_DUMMY + 256 <= LDS_BYTES, "lds");

constexpr int NORM_T = 21;
constexpr int FLAG_T = 24;

struct Params {
  const u16* x; const u16* wqkv; const u16* wo; const u16* win; const u16* wsp; const u16* bsp; const u16* wout;
  u16* out; char* ws;
};

template <int I> struct IC { static constexpr int v = I; };
template <int N, int I = 0, class F> __device__ __forceinline__ void sfor(F&& f) { if constexpr (I < N) { f(IC<I>{}); sfor<N, I + 1>(f); } }
#define MK_WAITV(n) asm volatile("s_waitcnt vmcnt(%0)" ::"i"(n) : "memory")
#define MK_LGKM0() __builtin_amdgcn_s_waitcnt(0xC07F)
#define MK_SB() __builtin_amdgcn_sched_barrier(0)
__device__ __forceinline__ void bar_h() { asm volatile("s_waitcnt lgkmcnt(0)\n\ts_barrier" ::: "memory"); }
__device__ __forceinline__ void bar_n() { asm volatile("s_barrier" ::: "memory"); }
__device__ __forceinline__ void bar_c() { asm volatile("" ::: "memory"); __builtin_amdgcn_s_barrier(); asm volatile("" ::: "memory"); }

__device__ __forceinline__ rsrc_t mk_rsrc(const void* p, u32 bytes) { return __builtin_amdgcn_make_buffer_rsrc((void*)p, 0, bytes, 0x00020000); }
template <int OFF, int AUX> __device__ __forceinline__ void glds(rsrc_t r, u32 ldsdst, u32 voff, u32 soff) {
  __builtin_amdgcn_raw_ptr_buffer_load_lds(r, (lds_vp)(size_t)ldsdst, 16, (int)voff, (int)soff, OFF, AUX);
}
constexpr int SC1 = 16;
__device__ __forceinline__ u32 pk_bf16(float lo, float hi) { f32x2 v = {lo, hi}; return __builtin_bit_cast(u32, __builtin_convertvector(v, bf16x2n)); }
__device__ __forceinline__ float asf(u32 u) { return __builtin_bit_cast(float, u); }
__device__ __forceinline__ u32 asu(float f) { return __builtin_bit_cast(u32, f); }
__device__ __forceinline__ float bf_lo(u32 u) { return __builtin_bit_cast(float, u << 16); }
__device__ __forceinline__ float bf_hi(u32 u) { return __builtin_bit_cast(float, u & 0xffff0000u); }
template <int CTRL> __device__ __forceinline__ float dpp(float x) {
  return __builtin_bit_cast(float, __builtin_amdgcn_update_dpp(0, __builtin_bit_cast(int, x), CTRL, 0xf, 0xf, true));
}
__device__ __forceinline__ float sum16(float x) { x += dpp<0xB1>(x); x += dpp<0x4E>(x); x += dpp<0x141>(x); x += dpp<0x140>(x); return x; }
__device__ __forceinline__ float sumg(float x) {
  u32 u = __builtin_bit_cast(u32, x);
  auto a = __builtin_amdgcn_permlane16_swap(u, u, false, false);
  x = __builtin_bit_cast(float, (u32)a[0]) + __builtin_bit_cast(float, (u32)a[1]);
  u = __builtin_bit_cast(u32, x);
  auto b = __builtin_amdgcn_permlane32_swap(u, u, false, false);
  return __builtin_bit_cast(float, (u32)b[0]) + __builtin_bit_cast(float, (u32)b[1]);
}
__device__ __forceinline__ float maxg(float x) {
  u32 u = __builtin_bit_cast(u32, x);
  auto a = __builtin_amdgcn_permlane16_swap(u, u, false, false);
  x = fmaxf(__builtin_bit_cast(float, (u32)a[0]), __builtin_bit_cast(float, (u32)a[1]));
  u = __builtin_bit_cast(u32, x);
  auto b = __builtin_amdgcn_permlane32_swap(u, u, false, false);
  return fmaxf(__builtin_bit_cast(float, (u32)b[0]), __builtin_bit_cast(float, (u32)b[1]));
}
__device__ __forceinline__ u32x4 pair_swap(u32x2 a, u32x2 b) {
  auto r0 = __builtin_amdgcn_permlane16_swap(a[0], b[0], false, false);
  auto r1 = __builtin_amdgcn_permlane16_swap(a[1], b[1], false, false);
  return (u32x4){(u32)r0[0], (u32)r1[0], (u32)r0[1], (u32)r1[1]};
}
__device__ __forceinline__ float gelu_f(float x) {
  const float e = __builtin_amdgcn_exp2f(x * (x * x * -0.10294324f + -2.30220820f));
  return x * __builtin_amdgcn_rcpf(1.0f + e);
}
__device__ __forceinline__ f32x4 gelu4(f32x4 x) {
  const f32x4 y = x * (x * x * -0.10294324f + -2.30220820f);
  f32x4 e;
#pragma unroll
  for (int i = 0; i < 4; ++i) e[i] = __builtin_amdgcn_exp2f(y[i]);
  e += 1.0f;
#pragma unroll
  for (int i = 0; i < 4; ++i) e[i] = __builtin_amdgcn_rcpf(e[i]);
  return x * e;
}
__device__ __forceinline__ u32x2 gelu_pk4(u32x2 z, float r) {
  float a, b, c, d, ta, tb, tc, td; u32 o0, o1;
  asm("v_lshlrev_b32 %2, 16, %10\n\tv_and_b32 %3, 0xffff0000, %10\n\tv_lshlrev_b32 %4, 16, %11\n\tv_and_b32 %5, 0xffff0000, %11\n\t"
      "v_mul_f32 %2, %12, %2\n\tv_mul_f32 %3, %12, %3\n\tv_mul_f32 %4, %12, %4\n\tv_mul_f32 %5, %12, %5\n\t"
      "v_mul_f32 %6, %2, %2\n\tv_mul_f32 %7, %3, %3\n\tv_mul_f32 %8, %4, %4\n\tv_mul_f32 %9, %5, %5\n\t"
      "v_fma_f32 %6, %6, %13, %14\n\tv_fma_f32 %7, %7, %13, %14\n\tv_fma_f32 %8, %8, %13, %14\n\tv_fma_f32 %9, %9, %13, %14\n\t"
      "v_mul_f32 %6, %2, %6\n\tv_mul_f32 %7, %3, %7\n\tv_mul_f32 %8, %4, %8\n\tv_mul_f32 %9, %5, %9\n\t"
      "v_exp_f32 %6, %6\n\tv_exp_f32 %7, %7\n\tv_exp_f32 %8, %8\n\tv_exp_f32 %9, %9\n\t"
      "v_add_f32 %6, 1.0, %6\n\tv_add_f32 %7, 1.0, %7\n\tv_add_f32 %8, 1.0, %8\n\tv_add_f32 %9, 1.0, %9\n\t"
      "v_rcp_f32 %6, %6\n\tv_rcp_f32 %7, %7\n\tv_rcp_f32 %8, %8\n\tv_rcp_f32 %9, %9\n\t"
      "v_mul_f32 %2, %2, %6\n\tv_mul_f32 %3, %3, %7\n\tv_mul_f32 %4, %4, %8\n\tv_mul_f32 %5, %5, %9\n\t"
      "v_cvt_pk_bf16_f32 %0, %2, %3\n\tv_cvt_pk_bf16_f32 %1, %4, %5"
      : "=&v"(o0), "=&v"(o1), "=&v"(a), "=&v"(b), "=&v"(c), "=&v"(d), "=&v"(ta), "=&v"(tb), "=&v"(tc), "=&v"(td)
      : "v"((u32)z[0]), "v"((u32)z[1]), "v"(r), "s"(-0.10294324f), "v"(-2.30220820f));
  return (u32x2){o0, o1};
}
__device__ __forceinline__ void add2(float& s, float& q, u32 a, u32 b) {
  asm("v_add_f32 %0, %0, %2\n\tv_add_f32 %1, %1, %3" : "+v"(s), "+v"(q) : "v"(a), "v"(b));
}
template <class T> __device__ __forceinline__ T lds_ld(u32 a) { return *(const LAS T*)(size_t)a; }
template <class T> __device__ __forceinline__ void lds_st(u32 a, T v) { *(LAS T*)(size_t)a = v; }
__device__ __forceinline__ bf16x4 lds_tr(u32 a) { return __builtin_amdgcn_ds_read_tr16_b64_v4i16((LAS bf16x4*)(size_t)a); }
template <int O0, int O1, int O2, int O3> __device__ __forceinline__ void lds_tr4(u32 a, u32 b, bf16x8& f0, bf16x8& f1) {
  bf16x4 r0, r1, r2, r3;
  asm volatile("ds_read_b64_tr_b16 %0, %4 offset:%6\n\tds_read_b64_tr_b16 %1, %5 offset:%7\n\tds_read_b64_tr_b16 %2, %4 offset:%8\n\tds_read_b64_tr_b16 %3, %5 offset:%9\n\ts_waitcnt lgkmcnt(0)"
               : "=&v"(r0), "=&v"(r1), "=&v"(r2), "=&v"(r3) : "v"(a), "v"(b), "i"(O0), "i"(O1), "i"(O2), "i"(O3) : "memory");
  f0 = __builtin_shufflevector(r0, r1, 0, 1, 2, 3, 4, 5, 6, 7); f1 = __builtin_shufflevector(r2, r3, 0, 1, 2, 3, 4, 5, 6, 7);
}
__device__ __forceinline__ u32 opaque(u32 x) { asm("" : "+v"(x)); return x; }

struct Ctx {
  int lane, wid, rp, cj, r16, g, q, p;
  u32 lds;
  u32 tag_lo, tag_hi;
  rsrc_t rws;
};
constexpr int SPIN_CAP = 1 << 17;
#define STAMP(i) do { } while (0)

__device__ __forceinline__ void load_tag(Ctx& c) {
  const u32x2 e = __builtin_amdgcn_raw_buffer_load_b64(c.rws, (int)WS_EPOCH, 0, SC1);
  const u64 t = (((u64)e[1] << 32) | e[0]) + 1;
  c.tag_lo = __builtin_amdgcn_readfirstlane((u32)t); c.tag_hi = __builtin_amdgcn_readfirstlane((u32)(t >> 32));
}
__device__ __forceinline__ void publish(const Ctx& c, int kind, int slot) {
  asm volatile("s_waitcnt vmcnt(0)" ::: "memory");
  if (c.lane == 0) __builtin_amdgcn_raw_buffer_store_b64((u32x2){c.tag_lo, c.tag_hi}, c.rws, (int)(flag_off(kind, c.rp, c.cj) + 8 * slot), 0, SC1);
}
__device__ __forceinline__ void poll64(const Ctx& c, u32 voff) {
#pragma unroll 1
  for (int s = 0; s < SPIN_CAP; ++s) {
    asm volatile("" ::: "memory");
    const u32x2 v = __builtin_amdgcn_raw_buffer_load_b64(c.rws, (int)voff, 0, SC1);
    if (__all(v[0] == c.tag_lo && v[1] == c.tag_hi)) break;
    __builtin_amdgcn_s_sleep(1);
  }
  asm volatile("" ::: "memory");
}
__device__ __forceinline__ u32x2 poll_issue(const Ctx& c, u32 voff) { return __builtin_amdgcn_raw_buffer_load_b64(c.rws, (int)voff, 0, SC1); }
__device__ __forceinline__ void poll_check(const Ctx& c, u32 voff, u32x2 v) {
  if (!__all(v[0] == c.tag_lo && v[1] == c.tag_hi)) poll64(c, voff);
  asm volatile("" ::: "memory");
}
__device__ __forceinline__ u32x4 tag_pair(const Ctx& c, float a, float b) {
  return (u32x4){asu(a), c.tag_lo ^ asu(b), asu(b), ~c.tag_lo ^ asu(a)};
}
__device__ __forceinline__ u32 tag_bad(const Ctx& c, u32x4 v) { return (v[1] ^ v[2] ^ c.tag_lo) | (v[3] ^ v[0] ^ ~c.tag_lo); }
__device__ __forceinline__ void touch(const Ctx& c, rsrc_t r, u32 voff, u32 soff) {
  __builtin_amdgcn_raw_ptr_buffer_load_lds(r, (lds_vp)(size_t)(c.lds + T_DUMMY), 4, (int)voff, (int)soff, 0, 0);
}
__device__ __forceinline__ void poll128(const Ctx& c, u32 voff) {
#pragma unroll 1
  for (int s = 0; s < SPIN_CAP; ++s) {
    asm volatile("" ::: "memory");
    const u32x4 v = __builtin_amdgcn_raw_buffer_load_b128(c.rws, (int)voff, 0, SC1);
    if (__all(v[0] == c.tag_lo && v[1] == c.tag_hi && v[2] == c.tag_lo && v[3] == c.tag_hi)) break;
    __builtin_amdgcn_s_sleep(1);
  }
  asm volatile("" ::: "memory");
}

struct Win { u32 b[3]; };
__device__ __forceinline__ Win mk_win(u32 base) { Win w; w.b[0] = opaque(base); w.b[1] = opaque(base + 65536u); w.b[2] = opaque(base + 131072u); return w; }
template <int OFF> __device__ __forceinline__ u32 wad(const Win& w) { return w.b[OFF >> 16] + (u32)(OFF & 0xffff); }

template <int KS, int MT, int NT> struct Set { bf16x8 a[KS][MT]; bf16x8 b[KS][NT]; bf16x4 t[KS][NT]; };

__device__ __forceinline__ void mfma_acc(f32x4& c, const bf16x8& a, const bf16x8& b) {
  asm volatile("v_mfma_f32_16x16x32_bf16 %0, %1, %2, %0" : "+v"(c) : "v"(a), "v"(b));
}
__device__ __forceinline__ void mfma_in() { MK_SB(); asm volatile("s_nop 1"); MK_SB(); }
__device__ __forceinline__ void mfma_out() { MK_SB(); asm volatile("s_nop 7\n\ts_nop 1"); MK_SB(); }
template <int KS, int MT, int NT, bool SWAP, int NRD, class GAP, class RD>
__device__ __forceinline__ void part(f32x4 (&acc)[MT][NT], const Set<KS, MT, NT>& X, GAP, RD&& rd) {
  constexpr int NMF = KS * MT * NT;
  MK_SB();
  sfor<NMF>([&](auto ic) {
    constexpr int i = decltype(ic)::v, ks = i / (MT * NT), m = (i / NT) % MT, n = i % NT;
    if constexpr (SWAP) mfma_acc(acc[m][n], X.b[ks][n], X.a[ks][m]); else mfma_acc(acc[m][n], X.a[ks][m], X.b[ks][n]);
    MK_SB();
    sfor<NRD>([&](auto kc) { if constexpr (GAP::gap(decltype(kc)::v) == i) rd(kc); });
    if constexpr (i == NMF - 2) MK_LGKM0();
    MK_SB();
  });
}
struct Gap1 { static constexpr int gap(int k) { return k; } };
struct GapG2 { static constexpr int gap(int k) { return k < 2 ? k : (k < 10 ? 2 + (k - 2) / 2 : (k < 12 ? k - 4 : 8 + (k - 12) / 2)); } };

struct AdP1 { Win a[2], a0[2], b[3]; };
template <int ST, int KSI, int K> __device__ __forceinline__ void rd_p1(const AdP1& ad, Set<1, 9, 3>& Y) {
  constexpr int SB = ST * P1_ST;
  if constexpr (K == 0) Y.a[0][0] = lds_ld<bf16x8>(wad<SB>(ad.a0[KSI]));
  else if constexpr (K <= 6) {
    constexpr int n = (K - 1) >> 1, h = (K - 1) & 1;
    const bf16x4 v = lds_tr(wad<SB + P1_A + 4096 * KSI + 512 * h>(ad.b[n]));
    if constexpr (h == 0) Y.t[0][n] = v; else Y.b[0][n] = __builtin_shufflevector(Y.t[0][n], v, 0, 1, 2, 3, 4, 5, 6, 7);
  } else { constexpr int m = K - 6; Y.a[0][m] = lds_ld<bf16x8>(wad<SB + m * 2048>(ad.a[KSI])); }
}
struct AdG3 { Win af, ar[2], b; };
template <int ST, int KSI, bool ROWS, int K> __device__ __forceinline__ void rd_g3(const AdG3& ad, Set<1, 2, 7>& Y) {
  constexpr int SB = ST * G3_ST;
  if constexpr (K == 0 || K == 8) {
    constexpr int m = K == 0 ? 0 : 1;
    if constexpr (ROWS) Y.a[0][m] = lds_ld<bf16x8>(wad<SB + m * 2048>(ad.ar[KSI]));
    else Y.a[0][m] = lds_ld<bf16x8>(wad<SB + KSI * 8192 + m * 1024>(ad.af));
  } else { constexpr int n = K - 1; Y.b[0][n] = lds_ld<bf16x8>(wad<SB + 16384 + (KSI * 7 + n) * 1024>(ad.b)); }
}
struct AdG2 { Win a, b[4]; };
template <int ST, int K> __device__ __forceinline__ void rd_g2(const AdG2& ad, Set<2, 2, 4>& Y) {
  constexpr int ks = K / 10, k = K % 10;
  if constexpr (k < 2) Y.a[ks][k] = lds_ld<bf16x8>(wad<G2_A + ST * 16384 + ks * 8192 + k * 1024>(ad.a));
  else {
    constexpr int n = (k - 2) >> 1, h = (k - 2) & 1;
    const bf16x4 v = lds_tr(wad<G2_B + ST * 8192 + 4096 * ks + 512 * h>(ad.b[n]));
    if constexpr (h == 0) Y.t[ks][n] = v; else Y.b[ks][n] = __builtin_shufflevector(Y.t[ks][n], v, 0, 1, 2, 3, 4, 5, 6, 7);
  }
}
__device__ __forceinline__ u32 tr64_lane(const Ctx& c, int strip) {
  const int f = (c.q >> 1) | ((c.g & 1) << 1);
  return (u32)((strip >> 2) * 8192 + 128 * (8 * c.g + c.q) + 16 * ((2 * (strip & 3) + (c.p >> 1)) ^ (2 * f)) + 8 * (c.p & 1));
}

template <int ST, bool LAST> __device__ __forceinline__ void tile_p1(f32x4 (&acc)[9][3], Set<1, 9, 3>& X, Set<1, 9, 3>& Y, const AdP1& ad) {
  part<1, 9, 3, true, 15>(acc, X, Gap1{}, [&](auto kc) { rd_p1<ST, 1, decltype(kc)::v>(ad, Y); });
  bar_c();
  part<1, 9, 3, true, LAST ? 0 : 15>(acc, Y, Gap1{}, [&](auto kc) { rd_p1<(ST + 1) % 3, 0, decltype(kc)::v>(ad, X); });
}
template <int ST, bool SWAP, bool ROWS, bool NROWS, bool LAST>
__device__ __forceinline__ void tile_g3(f32x4 (&acc)[2][7], Set<1, 2, 7>& X, Set<1, 2, 7>& Y, const AdG3& ad) {
  part<1, 2, 7, SWAP, 9>(acc, X, Gap1{}, [&](auto kc) { rd_g3<ST, 1, ROWS, decltype(kc)::v>(ad, Y); });
  bar_c();
  part<1, 2, 7, SWAP, LAST ? 0 : 9>(acc, Y, Gap1{}, [&](auto kc) { rd_g3<(ST + 1) % 4, 0, NROWS, decltype(kc)::v>(ad, X); });
}
template <int ST, bool BAR, bool LAST> __device__ __forceinline__ void tile_g2(f32x4 (&acc)[2][4], Set<2, 2, 4>& X, Set<2, 2, 4>& Y, const AdG2& ad) {
  if constexpr (BAR) bar_c();
  part<2, 2, 4, true, LAST ? 0 : 20>(acc, X, GapG2{}, [&](auto kc) { rd_g2<(ST + 1) % 6, decltype(kc)::v>(ad, Y); });
}
template <int NG, bool LASTBAR> __device__ __forceinline__ void gemm_g2(f32x4 (&acc)[2][4], const Ctx& c) {
  AdG2 ad;
  ad.a = mk_win(c.lds + (u32)(c.wid * 2048 + c.lane * 16));
#pragma unroll
  for (int n = 0; n < 4; ++n) ad.b[n] = mk_win(c.lds + tr64_lane(c, n));
  Set<2, 2, 4> X, Y;
  bar_c();
  sfor<20>([&](auto kc) { rd_g2<0, decltype(kc)::v>(ad, X); });
  MK_LGKM0();
#pragma unroll 1
  for (int it = 0; it < NG; ++it) {
    tile_g2<0, true, false>(acc, X, Y, ad); tile_g2<1, true, false>(acc, Y, X, ad); tile_g2<2, true, false>(acc, X, Y, ad);
    tile_g2<3, true, false>(acc, Y, X, ad); tile_g2<4, true, false>(acc, X, Y, ad); tile_g2<5, true, false>(acc, Y, X, ad);
  }
  tile_g2<0, true, false>(acc, X, Y, ad); tile_g2<1, true, false>(acc, Y, X, ad); tile_g2<2, true, false>(acc, X, Y, ad);
  tile_g2<3, LASTBAR, true>(acc, Y, X, ad);
  mfma_out();
}

__device__ __forceinline__ void attention(const Ctx& c) {
  const int hh = c.wid & 1, T = c.wid >> 1;
  const int lo = (c.rp == 0 && T == 0) ? 8 : 0;
  const float sc = 0.17677669529663687f * 1.4426950408889634f;
#pragma unroll
  for (int par = 0; par < 2; ++par) {
    const u32 bq = c.lds + AT_BASE + (u32)(((0 * 2 + hh) * 2 + par) * AT_BLK);
    const u32 bk = c.lds + AT_BASE + (u32)(((1 * 2 + hh) * 2 + par) * AT_BLK);
    const u32 bv = c.lds + AT_BASE + (u32)(((2 * 2 + hh) * 2 + par) * AT_BLK);
    const bf16x8 qf = lds_ld<bf16x8>(bq + (u32)((8 + 16 * T + c.r16) * AT_RS + 16 * c.g));
    f32x4 s[2];
    bf16x8 vf[2];
#pragma unroll
    for (int k2 = 0; k2 < 2; ++k2) {
      const int kr = min(16 * T + 16 * k2 + c.r16, 71);
      const bf16x8 kf = lds_ld<bf16x8>(bk + (u32)(kr * AT_RS + 16 * c.g));
      s[k2] = __builtin_amdgcn_mfma_f32_16x16x32_bf16(kf, qf, (f32x4){0.f, 0.f, 0.f, 0.f}, 0, 0, 0);
    }
    {
      const int v0 = 16 * T + 4 * c.g + c.q, v1 = min(v0 + 16, 71);
      lds_tr4<0, 0, 32, 32>(bv + (u32)(v0 * AT_RS + 8 * c.p), bv + (u32)(v1 * AT_RS + 8 * c.p), vf[0], vf[1]);
    }
    float e[8], mx = -3.0e38f;
#pragma unroll
    for (int j = 0; j < 8; ++j) {
      const int ak = 16 * (j >> 2) + 4 * c.g + (j & 3), w = 8 + c.r16 - ak;
      const bool ok = (w >= 0) && (w <= 7) && (ak >= lo);
      e[j] = ok ? s[j >> 2][j & 3] * sc : -3.0e38f;
      mx = fmaxf(mx, e[j]);
    }
    mx = maxg(mx);
    float sum = 0.f;
#pragma unroll
    for (int j = 0; j < 8; ++j) { e[j] = __builtin_amdgcn_exp2f(e[j] - mx); sum += e[j]; }
    sum = sumg(sum);
    const float inv = __builtin_amdgcn_rcpf(sum);
    const u32x4 pw = {pk_bf16(e[0] * inv, e[1] * inv), pk_bf16(e[2] * inv, e[3] * inv), pk_bf16(e[4] * inv, e[5] * inv), pk_bf16(e[6] * inv, e[7] * inv)};
    const bf16x8 pf = __builtin_bit_cast(bf16x8, pw);
    const f32x4 o0 = __builtin_amdgcn_mfma_f32_16x16x32_bf16(vf[0], pf, (f32x4){0.f, 0.f, 0.f, 0.f}, 0, 0, 0);
    const f32x4 o1 = __builtin_amdgcn_mfma_f32_16x16x32_bf16(vf[1], pf, (f32x4){0.f, 0.f, 0.f, 0.f}, 0, 0, 0);
    const u32x4 ov = pair_swap((u32x2){pk_bf16(o0[0], o0[1]), pk_bf16(o0[2], o0[3])}, (u32x2){pk_bf16(o1[0], o1[1]), pk_bf16(o1[2], o1[3])});
    const int G = ((c.g & 1) << 1) | (c.g >> 1);
    const u32 off = WS_O + (u32)(((c.rp * 32 + 2 * c.cj + hh) * 8 + 2 * T + par) * 1024 + (16 * G + c.r16) * 16);
    __builtin_amdgcn_raw_buffer_store_b128(ov, c.rws, (int)off, 0, SC1);
  }
  publish(c, FO, c.wid);
}

__device__ __forceinline__ float red8(const float (&v)[8], int r16) {
  const bool b2 = r16 & 4, b1 = r16 & 2, b0 = r16 & 1;
  float u[4], t[2];
#pragma unroll
  for (int i = 0; i < 4; ++i) u[i] = (b2 ? v[4 + i] : v[i]) + dpp<0x141>(b2 ? v[i] : v[4 + i]);
#pragma unroll
  for (int i = 0; i < 2; ++i) t[i] = (b1 ? u[2 + i] : u[i]) + dpp<0x4E>(b1 ? u[i] : u[2 + i]);
  const float x = (b0 ? t[1] : t[0]) + dpp<0xB1>(b0 ? t[0] : t[1]);
  return x + dpp<0x128>(x);
}

__device__ __forceinline__ void consumer(const Params& P, Ctx& c) {
  STAMP(0);
  const int w = c.wid, G = ((c.g & 1) << 1) | (c.g >> 1);
  const rsrc_t rx = mk_rsrc(P.x, 2048u * 2048u);
  load_tag(c);
  {
    if (c.lane < 12) lds_st<u32>(c.lds + (u32)((c.lane >> 2) * P1_ST + P1_ONES + 4 * (c.lane & 3)), 0x3f803f80u);
    if (w == 0 && c.lane == 0) lds_st<u32x2>(c.lds + T_TAG, (u32x2){c.tag_lo, c.tag_hi});
    AdP1 ad;
#pragma unroll
    for (int ks = 0; ks < 2; ++ks) {
      const u32 a = (u32)(c.r16 * 128 + (((4 * ks + c.g) ^ (c.r16 >> 1)) * 16));
      ad.a[ks] = mk_win(c.lds + a);
      ad.a0[ks] = mk_win(c.lds + (c.r16 == 0 ? (u32)P1_ONES : a));
    }
#pragma unroll
    for (int n = 0; n < 3; ++n) ad.b[n] = mk_win(c.lds + tr64_lane(c, 3 * w + n));
    f32x4 acc[9][3];
#pragma unroll
    for (int m = 0; m < 9; ++m)
#pragma unroll
      for (int n = 0; n < 3; ++n) { acc[m][n] = (f32x4){0.f, 0.f, 0.f, 0.f}; asm volatile("" : "+v"(acc[m][n])); }
    Set<1, 9, 3> X, Y;
    MK_LGKM0();
    bar_c();
    STAMP(1);
    sfor<15>([&](auto kc) { rd_p1<0, 0, decltype(kc)::v>(ad, X); });
    MK_LGKM0();
#pragma unroll 1
    for (int it = 0; it < 5; ++it) { tile_p1<0, false>(acc, X, Y, ad); tile_p1<1, false>(acc, X, Y, ad); tile_p1<2, false>(acc, X, Y, ad); }
    part<1, 9, 3, true, 15>(acc, X, Gap1{}, [&](auto kc) { rd_p1<0, 1, decltype(kc)::v>(ad, Y); });
    bar_c();
    f32x2 xs[9];
    const u32 xa = opaque(c.lds + T_XST + (u32)(c.r16 * 8));
    part<1, 9, 3, true, 9>(acc, Y, Gap1{}, [&](auto kc) { constexpr int m = decltype(kc)::v; xs[m] = lds_ld<f32x2>(xa + m * 128); });
    mfma_out();
    STAMP(2);
#pragma unroll
    for (int n = 0; n < 3; ++n) {
      const int S = 3 * w + n;
      f32x4 cs;
#pragma unroll
      for (int r = 0; r < 4; ++r) cs[r] = dpp<0x150>(acc[0][n][r]);
      const u32 sb = c.lds + AT_BASE + (u32)((S >> 1) * 2 * AT_BLK + (S & 1) * 32 + 8 * c.g);
      const u32 s0 = opaque(sb + (u32)((c.r16 >> 3) * AT_BLK + (c.r16 & 7) * AT_RS)), s1 = opaque(sb + (u32)((8 + c.r16) * AT_RS));
#pragma unroll
      for (int m = 0; m < 9; ++m) {
        const f32x4 v = (acc[m][n] - xs[m][0] * cs) * xs[m][1];
        lds_st<u32x2>(m == 0 ? s0 : s1 + (u32)(((m - 1) & 1) * AT_BLK + ((m - 1) >> 1) * 16 * AT_RS), (u32x2){pk_bf16(v[0], v[1]), pk_bf16(v[2], v[3])});
      }
    }
  }
  MK_LGKM0();
  bar_c();
  STAMP(3);
  u32x4 xr[2][2];
#pragma unroll
  for (int m = 0; m < 2; ++m)
#pragma unroll
    for (int pr = 0; pr < 2; ++pr)
      xr[m][pr] = __builtin_amdgcn_raw_buffer_load_b128(rx, (128 * c.rp + 32 * w + 2 * c.r16 + m) * 2048 + (64 * c.cj + 32 * pr + 8 * G) * 2, 0, 0);
  attention(c);
  STAMP(4);
  u32x2 hb[2][4];
  {
    f32x4 acc[2][4];
#pragma unroll
    for (int m = 0; m < 2; ++m)
#pragma unroll
      for (int pr = 0; pr < 2; ++pr) {
        const u32x4 t = pair_swap((u32x2){xr[m][pr][0], xr[m][pr][1]}, (u32x2){xr[m][pr][2], xr[m][pr][3]});
        acc[m][2 * pr] = (f32x4){bf_lo(t[0]), bf_hi(t[0]), bf_lo(t[1]), bf_hi(t[1])};
        acc[m][2 * pr + 1] = (f32x4){bf_lo(t[2]), bf_hi(t[2]), bf_lo(t[3]), bf_hi(t[3])};
        asm volatile("" : "+v"(acc[m][2 * pr]), "+v"(acc[m][2 * pr + 1]));
      }
    bar_c();
    STAMP(5);
    gemm_g2<2, true>(acc, c);
    STAMP(6);
    const u32 ho = WS_H + (u32)(((c.rp * 32 + 2 * c.cj) * 8 + 2 * w) * 1024 + (16 * G + c.r16) * 16);
#pragma unroll
    for (int m = 0; m < 2; ++m) {
#pragma unroll
      for (int n = 0; n < 4; ++n) hb[m][n] = (u32x2){pk_bf16(acc[m][n][0], acc[m][n][1]), pk_bf16(acc[m][n][2], acc[m][n][3])};
#pragma unroll
      for (int pr = 0; pr < 2; ++pr) __builtin_amdgcn_raw_buffer_store_b128(pair_swap(hb[m][2 * pr], hb[m][2 * pr + 1]), c.rws, (int)(ho + m * 1024), pr * 8192, SC1);
    }
    float s[2], q[2];
#pragma unroll
    for (int m = 0; m < 2; ++m) {
      f32x2 s2 = {0.f, 0.f}, q2 = {0.f, 0.f};
#pragma unroll
      for (int n = 0; n < 4; ++n)
#pragma unroll
        for (int d = 0; d < 2; ++d) { const f32x2 a = {bf_lo(hb[m][n][d]), bf_hi(hb[m][n][d])}; s2 += a; q2 += a * a; }
      s[m] = sumg(s2[0] + s2[1]); q[m] = sumg(q2[0] + q2[1]);
    }
    if (c.g < 2) __builtin_amdgcn_raw_buffer_store_b64((u32x2){asu(c.g ? s[1] : s[0]), asu(c.g ? q[1] : q[0])}, c.rws,
        (int)(WS_HSTAT + (u32)(((c.rp * 16 + c.cj) * 128 + 32 * w + c.lane) * 8)), 0, SC1);
    STAMP(28);
    bar_c();
    publish(c, FHH, w);
    STAMP(7);
  }
  {
    AdG3 ad;
    ad.af = mk_win(c.lds + (u32)(w * 2048 + c.lane * 16));
#pragma unroll
    for (int ks = 0; ks < 2; ++ks) ad.ar[ks] = mk_win(c.lds + (u32)((32 * w + c.r16) * 128 + (((4 * ks + c.g) ^ (c.r16 >> 1)) * 16)));
    ad.b = mk_win(c.lds + (u32)(c.lane * 16));
    float bsp[2];
#pragma unroll
    for (int m = 0; m < 2; ++m) bsp[m] = bf_lo((u32)P.bsp[128 * c.rp + 32 * w + 2 * c.r16 + m]);
    f32x4 acc[2][7];
#pragma unroll
    for (int m = 0; m < 2; ++m)
#pragma unroll
      for (int n = 0; n < 7; ++n) { acc[m][n] = (f32x4){0.f, 0.f, 0.f, 0.f}; asm volatile("" : "+v"(acc[m][n])); }
    asm volatile("" : "+v"(bsp[0]), "+v"(bsp[1]));
    const u32 tg = opaque(c.lds + TAB + (u32)((32 * w + 4 * c.g) * 4));
    const u32 tr = opaque(c.lds + TAB + (u32)((32 * w + c.r16) * 4));
    const u32 z1a = opaque(c.lds + Z1BUF + (u32)(w * 7168 + c.lane * 8));
    Set<1, 2, 7> X, Y;
    bar_c();
    STAMP(8);
    bar_c();
    STAMP(9);
    sfor<9>([&](auto kc) { rd_g3<0, 0, false, decltype(kc)::v>(ad, X); });
    MK_LGKM0();
#pragma unroll 1
    for (int it = 0; it < 4; ++it) {
      tile_g3<0, false, false, false, false>(acc, X, Y, ad); tile_g3<1, false, false, false, false>(acc, X, Y, ad);
      tile_g3<2, false, false, false, false>(acc, X, Y, ad); tile_g3<3, false, false, false, false>(acc, X, Y, ad);
    }
    f32x4 hm[2], hr[2], c1[7];
    float c2[7], nm[2];
#pragma unroll
    for (int m = 0; m < 2; ++m) {
      hm[m] = lds_ld<f32x4>(tg + (T_HM - TAB) + 64 * m); hr[m] = lds_ld<f32x4>(tg + (T_HR - TAB) + 64 * m);
      nm[m] = lds_ld<float>(tr + (T_HM - TAB) + 64 * m);
    }
#pragma unroll
    for (int n = 0; n < 7; ++n) c2[n] = lds_ld<float>(c.lds + T_CS + (u32)((16 * n + c.r16) * 4));
    mfma_out();
    STAMP(10);
    f32x4 z2[2][7];
    {
      f32x4 sm[2] = {{0.f, 0.f, 0.f, 0.f}, {0.f, 0.f, 0.f, 0.f}}, sq[2] = {{0.f, 0.f, 0.f, 0.f}, {0.f, 0.f, 0.f, 0.f}};
#pragma unroll
      for (int n = 0; n < 7; ++n) {
        c1[n] = lds_ld<f32x4>(c.lds + T_CS + (u32)((112 + 16 * n + 4 * c.g) * 4));
#pragma unroll
        for (int m = 0; m < 2; ++m) {
          const f32x4 z = gelu4((acc[m][n] - hm[m] * c2[n]) * hr[m]);
          z2[m][n] = z; sm[m] += z; sq[m] += z * z;
        }
      }
      const float sv[8] = {sm[0][0], sm[0][1], sm[0][2], sm[0][3], sm[1][0], sm[1][1], sm[1][2], sm[1][3]};
      const float qv[8] = {sq[0][0], sq[0][1], sq[0][2], sq[0][3], sq[1][0], sq[1][1], sq[1][2], sq[1][3]};
      const float s = red8(sv, c.r16), q = red8(qv, c.r16);
      if (c.r16 < 8) __builtin_amdgcn_raw_buffer_store_b128(tag_pair(c, s, q), c.rws,
          (int)(WS_ZSTAT + (u32)(((c.rp * 16 + c.cj) * 128 + 32 * w + 16 * (c.r16 >> 2) + 4 * c.g + (c.r16 & 3)) * 16)), 0, SC1);
#pragma unroll
      for (int m = 0; m < 2; ++m)
#pragma unroll
        for (int n = 0; n < 7; ++n) { acc[m][n] = c1[n] * -nm[m]; asm volatile("" : "+v"(acc[m][n])); }
    }
    STAMP(11);
    mfma_in();
    sfor<16>([&](auto tc) {
      constexpr int t = 16 + decltype(tc)::v;
      tile_g3<t % 4, true, false, t == 31, false>(acc, X, Y, ad);
      if constexpr (t == NORM_T) {
        f32x4 zr[2], zo[2];
#pragma unroll
        for (int m = 0; m < 2; ++m) { zr[m] = lds_ld<f32x4>(tg + (T_ZR - TAB) + 64 * m); zo[m] = lds_ld<f32x4>(tg + (T_ZM - TAB) + 64 * m); }
        const u32 zo_ = WS_Z2N + (u32)((c.cj * 64 + 4 * c.rp + w) * 7168 + c.lane * 16);
        sfor<7>([&](auto nc) {
          constexpr int n = decltype(nc)::v;
          const f32x4 a = z2[0][n] * zr[0] + zo[0], b = z2[1][n] * zr[1] + zo[1];
          __builtin_amdgcn_raw_buffer_store_b128((u32x4){pk_bf16(a[0], b[0]), pk_bf16(a[1], b[1]), pk_bf16(a[2], b[2]), pk_bf16(a[3], b[3])}, c.rws,
              (int)(zo_ + (n & 3) * 1024), (n >> 2) * 4096, SC1);
        });
      }
      if constexpr (t == FLAG_T) publish(c, FZ, w);
      if constexpr (t == NORM_T - 1) STAMP(18);
      if constexpr (t == NORM_T) STAMP(19);
      if constexpr (t == FLAG_T) STAMP(20);
      if constexpr (t == 27) STAMP(21);
      if constexpr (t == 28) STAMP(22);
    });
    mfma_out();
    STAMP(12);
#pragma unroll
    for (int m = 0; m < 2; ++m)
#pragma unroll
      for (int n = 0; n < 7; ++n) {
        lds_st<u32x2>(z1a + (u32)((m * 7 + n) * 512), (u32x2){pk_bf16(acc[m][n][0], acc[m][n][1]), pk_bf16(acc[m][n][2], acc[m][n][3])});
        acc[m][n] = (f32x4){bsp[m], bsp[m], bsp[m], bsp[m]};
        asm volatile("" : "+v"(acc[m][n]));
      }
    mfma_in();
#pragma unroll 1
    for (int it = 0; it < 7; ++it) {
      tile_g3<0, true, true, true, false>(acc, X, Y, ad); tile_g3<1, true, true, true, false>(acc, X, Y, ad);
      tile_g3<2, true, true, true, false>(acc, X, Y, ad); tile_g3<3, true, true, true, false>(acc, X, Y, ad);
    }
    tile_g3<0, true, true, true, false>(acc, X, Y, ad); tile_g3<1, true, true, true, false>(acc, X, Y, ad);
    tile_g3<2, true, true, true, false>(acc, X, Y, ad);
    part<1, 2, 7, true, 9>(acc, X, Gap1{}, [&](auto kc) { rd_g3<3, 1, true, decltype(kc)::v>(ad, Y); });
    bar_c();
    u32x2 z1[12 + 2];
    part<1, 2, 7, true, 12>(acc, Y, Gap1{}, [&](auto kc) { constexpr int k = decltype(kc)::v; z1[k] = lds_ld<u32x2>(z1a + k * 512); });
    z1[12] = lds_ld<u32x2>(z1a + 12 * 512); z1[13] = lds_ld<u32x2>(z1a + 13 * 512);
    mfma_out();
    STAMP(13);
    {
      const int odd = c.cj & 1, kp = (7 * c.cj + 1) >> 1, ksg = (7 * c.cj + (odd ? -1 : 6)) >> 1;
      const u32 up = WS_U + (u32)(((c.rp * 56 + kp) * 8 + 2 * w) * 1024 + c.lane * 16);
      const u32 us = WS_U + (u32)(((c.rp * 56 + ksg) * 8 + 2 * w) * 1024 + (16 * (2 * odd + (c.g >> 1)) + c.r16) * 16 + 8 * (c.g & 1));
#pragma unroll
      for (int m = 0; m < 2; ++m) {
        u32x2 uv[7];
#pragma unroll
        for (int n = 0; n < 7; ++n) {
          const u32x2 z = z1[m * 7 + n];
          const f32x4 u = (f32x4){bf_lo(z[0]), bf_hi(z[0]), bf_lo(z[1]), bf_hi(z[1])} * acc[m][n];
          uv[n] = (u32x2){pk_bf16(u[0], u[1]), pk_bf16(u[2], u[3])};
          if (n & 1) __builtin_amdgcn_raw_buffer_store_b128((u32x4){uv[n - 1][0], uv[n - 1][1], uv[n][0], uv[n][1]}, c.rws, (int)(up + m * 1024), (n >> 1) * 8192, SC1);
        }
        __builtin_amdgcn_raw_buffer_store_b64(uv[6], c.rws, (int)(us + m * 1024), 0, SC1);
      }
      STAMP(29);
      bar_c();
      publish(c, FU, w);
      STAMP(14);
    }
  }
  {
    f32x4 acc[2][4];
#pragma unroll
    for (int m = 0; m < 2; ++m)
#pragma unroll
      for (int n = 0; n < 4; ++n) {
        acc[m][n] = (f32x4){bf_lo(hb[m][n][0]), bf_hi(hb[m][n][0]), bf_lo(hb[m][n][1]), bf_hi(hb[m][n][1])};
        asm volatile("" : "+v"(acc[m][n]));
      }
    bar_c();
    STAMP(15);
    gemm_g2<4, false>(acc, c);
    STAMP(16);
    const rsrc_t ro = mk_rsrc(P.out, 2048u * 2048u);
#pragma unroll
    for (int m = 0; m < 2; ++m)
#pragma unroll
      for (int pr = 0; pr < 2; ++pr) {
        const f32x4 a = acc[m][2 * pr], b = acc[m][2 * pr + 1];
        __builtin_amdgcn_raw_buffer_store_b128(pair_swap((u32x2){pk_bf16(a[0], a[1]), pk_bf16(a[2], a[3])}, (u32x2){pk_bf16(b[0], b[1]), pk_bf16(b[2], b[3])}), ro,
            (128 * c.rp + 32 * w + 2 * c.r16 + m) * 2048 + (64 * c.cj + 32 * pr + 8 * G) * 2, 0, 0);
      }
    if (c.rp == 0 && c.cj == 0 && w == 0 && c.lane == 0)
      __builtin_amdgcn_raw_buffer_store_b64((u32x2){c.tag_lo, c.tag_hi}, c.rws, (int)WS_EPOCH, 0, SC1);
    STAMP(17);
  }
}

template <int N, int AUX> __device__ __forceinline__ void issue_v(rsrc_t r, u32 dst, const u32 (&vo)[N], u32 so) {
  sfor<N>([&](auto ic) { constexpr int s = decltype(ic)::v; glds<(s & 3) * 1024, AUX>(r, dst + (s >> 2) * 4096, vo[s], so); });
}
template <int N, int AUX> __device__ __forceinline__ void issue_v2(rsrc_t r, u32 dst, const u32 (&vo)[N], u32 so) {
  sfor<N>([&](auto ic) { constexpr int s = decltype(ic)::v; glds<(s & 1) * 2048, AUX>(r, dst + (s >> 1) * 4096, vo[s], so); });
}
template <int N, int AUX> __device__ __forceinline__ void issue_l(rsrc_t r, u32 dst, u32 vo, u32 so) {
  sfor<N>([&](auto ic) { constexpr int s = decltype(ic)::v; glds<(s & 3) * 1024, AUX>(r, dst + (s >> 2) * 4096, vo, so + (s >> 2) * 4096); });
}
__device__ __forceinline__ u32 tr64_src(int lane, int lw, int ldb_bytes) {
  const int f = ((lane >> 4) & 1) | (lw << 1);
  return (u32)((8 * lw + (lane >> 3)) * ldb_bytes + (((lane & 7) ^ (2 * f)) * 16));
}
template <int T, bool LASTBAR>
__device__ __forceinline__ void load_g2(const Ctx& c, int lw, u32 abase, rsrc_t rb, const u32 (&vb)[4], u32 bstep) {
  const u32 va = abase + (u32)(lw * 8192 + c.lane * 16), da = c.lds + G2_A + (u32)(lw * 8192), db = c.lds + G2_B + (u32)(lw * 1024);
  auto A = [&](int t, int st) { issue_l<8, SC1>(c.rws, da + (u32)(st * 16384), va, (u32)(t * 16384)); };
  auto B = [&](int t, int st) { issue_v2<4, 0>(rb, db + (u32)(st * 8192), vb, (u32)t * bstep); };
  A(0, 0); A(1, 1); MK_WAITV(8); bar_h();
  A(2, 2); A(3, 3); MK_WAITV(16); bar_h();
  A(4, 4); A(5, 5); A(6, 0); B(6, 0); MK_WAITV(36); bar_h();
  A(7, 1); B(7, 1); MK_WAITV(40); bar_h();
  A(8, 2); B(8, 2); MK_WAITV(44); bar_h();
  int st = 3;
#pragma unroll 1
  for (int t = 9; t < T; ++t) { A(t, st); B(t, st); MK_WAITV(48); bar_h(); st = st == 5 ? 0 : st + 1; }
  MK_WAITV(36); bar_h(); MK_WAITV(24); bar_h(); MK_WAITV(12); bar_h(); MK_WAITV(0); bar_h();
  if (LASTBAR) bar_h();
}
__device__ __forceinline__ void pre_g2(const Ctx& c, int lw, rsrc_t rb, u32 (&vb)[4]) {
  const u32 b0 = tr64_src(c.lane, lw, 2048) + (u32)(128 * c.cj + 4096);
#pragma unroll
  for (int s = 0; s < 4; ++s) vb[s] = b0 + (u32)(s * 16 * 2048 - (s & 1) * 2048);
#pragma unroll 1
  for (int t = 0; t < 6; ++t) issue_v2<4, 0>(rb, c.lds + G2_B + (u32)(lw * 1024 + t * 8192), vb, (u32)(t * 64 * 2048));
}

__device__ __forceinline__ void loader(const Params& P, Ctx& c) {
  const int lw = c.wid - 4, lane = c.lane;
  {
    const rsrc_t rx = mk_rsrc((const char*)P.x - 4096, 2048u * 2048u + 4096u);
    const rsrc_t rw = mk_rsrc((const char*)P.wqkv - 4096, 1024u * 6144u + 4096u);
    u32 va[9], vb[12];
    const u32 ch = (u32)((((lane & 7) ^ (4 * lw + (lane >> 4))) * 16) + 4096);
    va[0] = (u32)(max(128 * c.rp - 16 + 2 * (lane >> 3) + lw, 0) * 2048) + ch;
    const u32 a1 = (u32)((128 * c.rp + 16 * lw + 2 * (lane >> 3)) * 2048) + ch;
#pragma unroll
    for (int m = 1; m < 9; ++m) va[m] = a1 + (u32)((32 * ((m - 1) >> 1) + ((m - 1) & 1)) * 2048 - (m & 1) * 2048);
    const u32 b0 = tr64_src(lane, lw, 6144) + (u32)(128 * c.cj + 4096);
#pragma unroll
    for (int s = 0; s < 12; ++s) vb[s] = b0 + (u32)((s >> 2) * 2048 + (s & 3) * 16 * 6144 - (s & 1) * 2048);
    auto I = [&](int t, int st) {
      const u32 sb = c.lds + (u32)(st * P1_ST + lw * 1024);
      issue_v2<9, 0>(rx, sb, va, (u32)(t * 128));
      issue_v2<12, 0>(rw, sb + P1_A, vb, (u32)(t * 64 * 6144));
    };
    I(0, 0); I(1, 1); MK_WAITV(21); bar_h();
    I(2, 2); MK_WAITV(21); bar_h();
    int st = 0;
#pragma unroll 1
    for (int t = 3; t < 16; ++t) { I(t, st); MK_WAITV(21); bar_h(); st = st == 2 ? 0 : st + 1; }
    MK_WAITV(0); bar_h();
    bar_h();
  }
  {
    const rsrc_t rb = mk_rsrc((const char*)P.wo - 4096, 1024u * 2048u + 4096u);
    u32 vb[4];
    pre_g2(c, lw, rb, vb);
    const u32x2 tg = lds_ld<u32x2>(c.lds + T_TAG);
    c.tag_lo = __builtin_amdgcn_readfirstlane(tg[0]); c.tag_hi = __builtin_amdgcn_readfirstlane(tg[1]);
    bar_h();
    attention(c);
    STAMP(26);
    if (lw == 0) poll128(c, flag_off(FO, 0, 0) + (u32)((c.rp * 16 + (lane >> 2)) * 128 + 16 * (lane & 3)));
    STAMP(27);
    bar_h();
    load_g2<16, true>(c, lw, WS_O + (u32)(c.rp * 32 * 8192), rb, vb, 64 * 2048);
  }
  {
    const rsrc_t rs = mk_rsrc((const char*)P.wsp - 4096, 2048u * 4096u + 4096u);
    const u32 vah = WS_H + (u32)(c.rp * 32 * 8192 + lw * 8192 + lane * 16);
    const u32 vbw = WS_WINP + (u32)(c.cj * 32 * 14336 + lw * 7168 + lane * 16);
    const u32 vbz = WS_Z2N + (u32)(c.cj * 32 * 14336 + lw * 7168 + lane * 16);
    const u32 da = c.lds + (u32)(lw * 8192), db = c.lds + (u32)(16384 + lw * 7168);
    auto A3 = [&](int t, int st) { issue_l<8, SC1>(c.rws, da + (u32)(st * G3_ST), vah, (u32)((t & 15) * 16384)); };
    auto B3 = [&](int t, int st) { issue_l<7, SC1>(c.rws, db + (u32)(st * G3_ST), vbw, (u32)(t * 14336)); };
    u32 vs[8];
    const u32 s0 = (u32)((128 * c.rp + 16 * lw + 2 * (lane >> 3)) * 4096 + (((lane & 7) ^ (4 * lw + (lane >> 4))) * 16) + 4096);
#pragma unroll
    for (int m = 0; m < 8; ++m) vs[m] = s0 + (u32)((32 * (m >> 1) + (m & 1)) * 4096 - (m & 1) * 2048);
    bar_h();
    B3(0, 0); B3(1, 1); B3(2, 2); B3(3, 3);
    bar_h();
    A3(0, 0); A3(1, 1); MK_WAITV(8); bar_h();
    A3(2, 2); A3(3, 3); MK_WAITV(16); bar_h();
    A3(4, 0); B3(4, 0); MK_WAITV(23); bar_h();
#pragma unroll 1
    for (int t = 5; t < 32; ++t) { A3(t, t & 3); B3(t, t & 3); MK_WAITV(30); bar_h(); }
#pragma unroll 1
    for (int t = 0; t < 32; ++t) {
      issue_v2<8, 0>(rs, c.lds + (u32)((t & 3) * G3_ST + lw * 1024), vs, (u32)(t * 128));
      issue_l<7, SC1>(c.rws, db + (u32)((t & 3) * G3_ST), vbz, (u32)(t * 14336));
      MK_WAITV(30); bar_h();
    }
    MK_WAITV(15); bar_h(); MK_WAITV(0); bar_h();
    bar_h();
  }
  {
    const rsrc_t rb = mk_rsrc((const char*)P.wout - 4096, 1792u * 2048u + 4096u);
    u32 vb[4];
    bar_h();
    pre_g2(c, lw, rb, vb);
    bar_h();
    load_g2<28, false>(c, lw, WS_U + (u32)(c.rp * 56 * 8192), rb, vb, 64 * 2048);
  }
}

__device__ __forceinline__ void bars(int n) {
#pragma unroll 1
  for (int i = 0; i < n; ++i) bar_n();
}
__device__ __forceinline__ void sumsq2(float& s, float& q, u32 u) {
  float a, b;
  asm("v_lshlrev_b32 %2, 16, %4\n\tv_and_b32 %3, 0xffff0000, %4\n\tv_add_f32 %0, %0, %2\n\tv_fmac_f32 %1, %2, %2\n\tv_add_f32 %0, %0, %3\n\tv_fmac_f32 %1, %3, %3"
      : "+v"(s), "+v"(q), "=&v"(a), "=&v"(b) : "v"(u));
}
__device__ __forceinline__ void worker(const Params& P, Ctx& c) {
  const int v = c.wid - 6, lane = c.lane;
  const u32 stg = c.lds + WSTG + (u32)(v * 14336);
  {
    const rsrc_t rx = mk_rsrc(P.x, 2048u * 2048u);
    const rsrc_t rq = mk_rsrc(P.wqkv, 1024u * 6144u);
    const int b = blockIdx.x, idx = b >> 3;
    const int cslow = (c.cj & 8) + ((3 - (int)((size_t)P.wqkv >> 7)) & 7);
    auto warm_slow = [&](int row0, int nrow) {
      const int i = lane / 3, g = lane - 3 * i;
      if (MK_PREFETCH & 2) if (32 * i < nrow) touch(c, rq, (u32)((row0 + idx + 32 * i) * 6144 + g * 2048 + cslow * 128), 0);
    };
    bar_n();
    if (v == 0) warm_slow(128, 320);
    else if (MK_PREFETCH & 1) {
#pragma unroll
      for (int j = 0; j < 3; ++j) { const int i = 64 * j + lane, r = i / 48; touch(c, rq, (u32)(((b & 63) + 64 * (4 * r + (b >> 6))) * 6144 + (i - 48 * r) * 128), 0); }
    }
    u32x4 xr[4][2];
#pragma unroll
    for (int i = 0; i < 4; ++i)
#pragma unroll
      for (int h = 0; h < 2; ++h) xr[i][h] = __builtin_amdgcn_raw_buffer_load_b128(rx, (128 * c.rp + 8 * c.cj + 4 * v + i) * 2048 + lane * 16, h * 1024, 0);
    if (MK_PREFETCH & 4) {
      u64 pc0; u32 back, len;
      asm volatile("s_getpc_b64 %0\n.Lmk_pc%=:\n\ts_mov_b32 %1, .Lmk_pc%=-.Lmk_code_begin\n\ts_mov_b32 %2, .Lmk_code_end-.Lmk_code_begin" : "=s"(pc0), "=s"(back), "=s"(len));
      const rsrc_t rc = mk_rsrc((const void*)((pc0 - back) & ~127ull), len);
      if (v == 0 && lane < 16) touch(c, rc, (u32)((idx + 32 * lane) * 128), 0);
    }
    { const u32x2 tg = lds_ld<u32x2>(c.lds + T_TAG);
      c.tag_lo = __builtin_amdgcn_readfirstlane(tg[0]); c.tag_hi = __builtin_amdgcn_readfirstlane(tg[1]); }
    bars(3);
    float sm = 0.f, sr = 0.f;
#pragma unroll
    for (int i = 0; i < 4; ++i) {
      float s = 0.f, q = 0.f;
#pragma unroll
      for (int h = 0; h < 2; ++h)
#pragma unroll
        for (int d = 0; d < ((MK_ABL & 2) ? 0 : 4); ++d) { u32 u = xr[i][h][d]; sumsq2(s, q, u); }
      const float mean = sumg(sum16(s)) * (1.0f / 1024.0f);
      const float rs = __builtin_amdgcn_rsqf(fmaxf(sumg(sum16(q)) * (1.0f / 1024.0f) - mean * mean, 0.f) + 1e-5f);
      sm = lane == i ? mean : sm; sr = lane == i ? rs : sr;
      asm volatile("" : "+v"(sm), "+v"(sr));
      if (i == 3) { if (lane < 4) __builtin_amdgcn_raw_buffer_store_b128(tag_pair(c, sm, sr), c.rws, (int)(WS_XSTAT + (u32)((128 * c.rp + 8 * c.cj + 4 * v + lane) * 16)), 0, SC1); }
      bar_n();
      if (i == 0 && v == 1) warm_slow(448, 320);
    }
    bars(2);
    if (v == 0) warm_slow(768, 256);
    else if (MK_PREFETCH & 8) touch(c, mk_rsrc(P.wo, 1024u * 2048u), (u32)(lane * 128), b * 8192u);
    bar_n();
    u32x4 xg[2];
    u32 xo[2];
#pragma unroll
    for (int i = 0; i < 2; ++i) {
      const int rho = min(72 * v + 64 * i + lane, 72 * v + 71), m = rho >> 4, sl = rho & 15;
      const int il = m == 0 ? (2 * (sl & 7) + (sl >> 3)) : 16 + 32 * ((m - 1) >> 1) + 2 * sl + ((m - 1) & 1);
      xo[i] = WS_XSTAT + (u32)(max(128 * c.rp - 16 + il, 0) * 16);
      xg[i] = __builtin_amdgcn_raw_buffer_load_b128(c.rws, (int)xo[i], 0, SC1);
    }
    bars(3);
    if (!__all((tag_bad(c, xg[0]) | tag_bad(c, xg[1])) == 0)) {
#pragma unroll 1
      for (int sp = 0; sp < SPIN_CAP; ++sp) {
        asm volatile("" ::: "memory");
#pragma unroll
        for (int i = 0; i < 2; ++i) xg[i] = __builtin_amdgcn_raw_buffer_load_b128(c.rws, (int)xo[i], 0, SC1);
        if (__all((tag_bad(c, xg[0]) | tag_bad(c, xg[1])) == 0)) break;
      }
    }
#pragma unroll
    for (int i = 0; i < 2; ++i) lds_st<u32x2>(c.lds + T_XST + (u32)(min(72 * v + 64 * i + lane, 72 * v + 71) * 8), (u32x2){xg[i][0], xg[i][2]});
    bar_h();
    {
      const rsrc_t rw = mk_rsrc((const char*)P.win - 4096, 1024u * 7168u + 4096u);
      int k = lane / 14, ch = lane - 14 * k;
      u32 vo = (u32)((64 * c.rp + k) * 7168 + ((v == 0 ? 1792 : 0) + 112 * c.cj) * 2 + ch * 16 + 4096);
      sfor<14>([&](auto ic) {
        constexpr int j = decltype(ic)::v;
        glds<(j & 3) * 1024, 0>(rw, stg + (j >> 2) * 4096, vo - (j & 3) * 1024, 0);
        ch += 8; const bool wrap = ch >= 14; ch -= wrap ? 14 : 0; vo += wrap ? (u32)(5 * 7168 - 96) : (u32)(4 * 7168 + 128);
        if constexpr (j == 6) bar_n();
      });
    }
    bar_n();
  }
  bar_n();
  STAMP(23);
  attention(c);
  STAMP(24);
  {
    const int odd = c.cj & 1;
    const u32 ta = stg + (u32)((8 * c.g + c.q) * 224);
    const u32 tp = ta + (u32)(((odd ? 16 : 0) + 8 * c.p) * 2), ts = ta + (u32)(((odd ? 0 : 96) + 4 * c.p) * 2);
    const u32 wo_ = WS_WINP + (u32)(((c.cj * 2 + v) * 32 + 2 * c.rp) * 7168 + lane * 16);
    float cs = 0.f;
    sfor<7>([&](auto nc) {
      constexpr int n = decltype(nc)::v, o = n < 6 ? 64 * (n >> 1) + 8 * (n & 1) : 0;
      bf16x8 f2[2];
      lds_tr4<o, o + 896, o + 7168, o + 7168 + 896>(n < 6 ? tp : ts, n < 6 ? tp : ts, f2[0], f2[1]);
      f32x2 s2 = {0.f, 0.f};
#pragma unroll
      for (int kl = 0; kl < 2; ++kl) {
        const u32x4 fr = __builtin_bit_cast(u32x4, f2[kl]);
        __builtin_amdgcn_raw_buffer_store_b128(fr, c.rws, (int)(wo_ + (n & 3) * 1024), kl * 7168 + (n >> 2) * 4096, SC1);
#pragma unroll
        for (int d = 0; d < 4; ++d) s2 += (f32x2){bf_lo(fr[d]), bf_hi(fr[d])};
      }
      const float t = sumg(s2[0] + s2[1]);
      if constexpr (n < 4) cs = (c.g == n) ? t : cs;
      if constexpr (n == 3) __builtin_amdgcn_raw_buffer_store_b32(asu(cs), c.rws, (int)(WS_CS + (u32)(((c.rp * 16 + c.cj) * 256 + 112 * v + lane) * 4)), 0, SC1);
      if constexpr (n >= 4) cs = (c.g == n - 4) ? t : cs;
    });
    if (lane < 48) __builtin_amdgcn_raw_buffer_store_b32(asu(cs), c.rws, (int)(WS_CS + (u32)(((c.rp * 16 + c.cj) * 256 + 112 * v + 64 + lane) * 4)), 0, SC1);
  }
  STAMP(25);
  bar_h();
  {
    const u32 fo = flag_off(FW, 0, 0) + (u32)((((lane >> 1) & 15) * 16 + c.cj) * 128 + 8 * (lane & 1));
    bars(3);
    publish(c, FW, v);
    bars(7);
    u32x2 fv = {0, 0};
    if (v == 0) fv = poll_issue(c, fo);
    bars(5);
    if (v == 0) poll_check(c, fo, fv);
    bars(2);
  }
  bar_n();
  if ((MK_PREFETCH & 8) && v == 1) {
    const rsrc_t r3 = mk_rsrc(P.wout, 1792u * 2048u);
    touch(c, r3, (u32)(lane * 128), blockIdx.x * 14336u);
    if (lane < 48) touch(c, r3, (u32)(lane * 128), blockIdx.x * 14336u + 8192u);
  }
  const int hi = lane >> 5, l5 = lane & 31;
  u32x4 cp[8];
#pragma unroll
  for (int j = 0; j < 8; ++j)
    cp[j] = __builtin_amdgcn_raw_buffer_load_b128(c.rws, (int)(WS_CS + (u32)(((hi * 16 + c.cj) * 256 + 112 * v + 4 * min(l5, 27)) * 4)), j * 32768, SC1);
  if (v == 0) poll64(c, flag_off(FHH, 0, 0) + (u32)((c.rp * 16 + (lane >> 2)) * 128 + 8 * (lane & 3)));
  bar_n();
  {
    float c0 = 0.f, c1 = 0.f, c2 = 0.f, c3 = 0.f;
#pragma unroll
    for (int j = 0; j < 8; ++j) { add2(c0, c1, cp[j][0], cp[j][1]); add2(c2, c3, cp[j][2], cp[j][3]); }
    auto fold = [](float x) { const u32 u = asu(x); auto r = __builtin_amdgcn_permlane32_swap(u, u, false, false); return asf((u32)r[0]) + asf((u32)r[1]); };
    lds_st<f32x4>(c.lds + T_CS + (u32)((112 * v + 4 * min(l5, 27)) * 4), (f32x4){fold(c0), fold(c1), fold(c2), fold(c3)});
    bars(3);
    u32x4 hp[8];
#pragma unroll
    for (int j = 0; j < 8; ++j)
      hp[j] = __builtin_amdgcn_raw_buffer_load_b128(c.rws, (int)(WS_HSTAT + (u32)(((c.rp * 16 + hi) * 128 + 64 * v + 2 * l5) * 8)), j * 2048, SC1);
    bars(6);
    float s0 = 0.f, q0 = 0.f, s1 = 0.f, q1 = 0.f;
#pragma unroll
    for (int j = 0; j < 8; ++j) { add2(s0, q0, hp[j][0], hp[j][1]); add2(s1, q1, hp[j][2], hp[j][3]); }
    {
      s0 = fold(s0); q0 = fold(q0); s1 = fold(s1); q1 = fold(q1);
      const float sx = hi ? s1 : s0, qx = hi ? q1 : q0;
      const float mean = sx * (1.0f / 1024.0f);
      const float rs = __builtin_amdgcn_rsqf(fmaxf(qx * (1.0f / 1024.0f) - mean * mean, 0.f) + 1e-5f);
      lds_st<float>(c.lds + T_HM + (u32)((64 * v + 2 * l5 + hi) * 4), mean);
      lds_st<float>(c.lds + T_HR + (u32)((64 * v + 2 * l5 + hi) * 4), rs);
    }
    bar_h();
    bars(7);
    if (MK_PREFETCH & 8) {
      const rsrc_t r2 = mk_rsrc(P.wsp, 2048u * 4096u);
      touch(c, r2, (u32)(lane * 128), blockIdx.x * 32768u + v * 16384u);
      touch(c, r2, (u32)(lane * 128), blockIdx.x * 32768u + v * 16384u + 8192u);
    }
    bars(2);
    u32x4 zp[16];
    auto sweep = [&]() {
#pragma unroll
      for (int j = 0; j < 16; ++j) zp[j] = __builtin_amdgcn_raw_buffer_load_b128(c.rws, (int)(WS_ZSTAT + (u32)((c.rp * 2048 + 64 * v + lane) * 16)), j * 2048, SC1);
    };
    sweep();
    bars(NORM_T - 18);
#pragma unroll 1
    for (int sp = 0; sp < SPIN_CAP; ++sp) {
      u32 bad = 0;
#pragma unroll
      for (int j = 0; j < 16; ++j) bad |= tag_bad(c, zp[j]);
      if (__all(bad == 0)) break;
      asm volatile("" ::: "memory");
      sweep();
    }
    float zs = 0.f, zq = 0.f;
#pragma unroll
    for (int j = 0; j < 16; ++j) add2(zs, zq, zp[j][0], zp[j][2]);
    const float zm = zs * (1.0f / 1792.0f);
    const float zr = __builtin_amdgcn_rsqf(fmaxf(zq * (1.0f / 1792.0f) - zm * zm, 0.f) + 1e-5f);
    lds_st<float>(c.lds + T_ZM + (u32)((64 * v + lane) * 4), -zm * zr);
    lds_st<float>(c.lds + T_ZR + (u32)((64 * v + lane) * 4), zr);
    bar_h();
    bars(26 - NORM_T);
    const u32 fo = flag_off(FZ, 0, 0) + (u32)(((lane >> 2) * 16 + c.cj) * 128 + 8 * (lane & 3));
    u32x2 fv = {0, 0};
    if (v == 0) fv = poll_issue(c, fo);
    bar_n();
    if (v == 0) poll_check(c, fo, fv);
    bars(5);
    u32 za = c.lds + Z1BUF + (u32)(v * 14336 + lane * 8);
    const u32 ra = c.lds + T_HR + (u32)((64 * v + c.r16) * 4);
    float rr[4];
#pragma unroll
    for (int i = 0; i < 4; ++i) rr[i] = lds_ld<float>(ra + 64 * i);
    u32x2 zn = lds_ld<u32x2>(za), zo = zn;
    bar_h();
    u32 zw = za;
#pragma unroll
    for (int i = 0; i < 4; ++i) {
#pragma unroll 1
      for (int n = 0; n < 7; ++n) {
        const u32x2 z = zn;
        if (!(MK_ABL & 1)) lds_st<u32x2>(zw, zo);
        zn = lds_ld<u32x2>(za + 512);
        MK_SB();
        zo = gelu_pk4(z, rr[i]);
        zw = za; za += 512;
        MK_SB();
        MK_LGKM0();
        bar_n();
      }
    }
    if (!(MK_ABL & 1)) lds_st<u32x2>(zw, zo);
    bar_h(); bar_n();
  }
  bar_n();
  if (v == 0) poll64(c, flag_off(FU, 0, 0) + (u32)((c.rp * 16 + (lane >> 2)) * 128 + 8 * (lane & 3)));
  bar_n();
}

__global__ __launch_bounds__(512, 2) void fused_block(Params P) {
  extern __shared__ __attribute__((aligned(16))) char smem[];
  asm volatile(".Lmk_code_begin:");
  Ctx c;
  c.lane = threadIdx.x & 63;
  c.wid = __builtin_amdgcn_readfirstlane(threadIdx.x >> 6);
  const int xcd = blockIdx.x & 7, idx = blockIdx.x >> 3;
  c.rp = (xcd & 3) * 4 + (idx & 3);
  c.cj = (xcd >> 2) * 8 + (idx >> 2);
  c.r16 = c.lane & 15; c.g = c.lane >> 4; c.q = (c.lane >> 2) & 3; c.p = c.lane & 3;
  c.lds = (u32)(size_t)(LAS char*)smem;
  c.rws = mk_rsrc(P.ws, WS_END);
  c.tag_lo = 0; c.tag_hi = 0;
  if (c.wid >= 6) worker(P, c); else if (c.wid >= 4) loader(P, c); else consumer(P, c);
  asm volatile(".Lmk_code_end:");
}

}

extern "C" void kernel_launch(void* const* d_in, const int* in_sizes, int n_in, void* d_out, int out_size, void* d_ws, size_t ws_size,
                              hipStream_t stream) {
  (void)in_sizes; (void)n_in; (void)out_size; (void)ws_size;
  mk::Params P;
  P.x = (const mk::u16*)d_in[0]; P.wqkv = (const mk::u16*)d_in[1]; P.wo = (const mk::u16*)d_in[2];
  P.win = (const mk::u16*)d_in[4]; P.wsp = (const mk::u16*)d_in[5]; P.bsp = (const mk::u16*)d_in[6]; P.wout = (const mk::u16*)d_in[7];
  P.out = (mk::u16*)d_out; P.ws = (char*)d_ws;
  (void)hipFuncSetAttribute((const void*)mk::fused_block, hipFuncAttributeMaxDynamicSharedMemorySize, mk::LDS_BYTES);
  hipLaunchKernelGGL(mk::fused_block, dim3(256), dim3(512), mk::LDS_BYTES, stream, P);
}
```

```cpp
#include <hip/hip_runtime.h>

#define MK_ABL 0
#define MK_T0 1
#define MK_B2 2
#define MK_PREFETCH 15
namespace mk {

#define LAS __attribute__((address_space(3)))
typedef LAS void* lds_vp;
typedef unsigned short u16;
typedef unsigned int u32;
typedef unsigned long long u64;
typedef short bf16x8 __attribute__((ext_vector_type(8)));
typedef short bf16x4 __attribute__((ext_vector_type(4)));
typedef float f32x2 __attribute__((ext_vector_type(2)));
typedef float f32x4 __attribute__((ext_vector_type(4)));
typedef u32 u32x2 __attribute__((ext_vector_type(2)));
typedef u32 u32x4 __attribute__((ext_vector_type(4)));
typedef __bf16 bf16x2n __attribute__((ext_vector_type(2)));
typedef __amdgpu_buffer_rsrc_t rsrc_t;

constexpr u32 WS_EPOCH = 0;
constexpr u32 WS_FLAGS = 4096;
enum { FW = 1, FO = 2, FHH = 3, FZ = 4, FU = 5 };
constexpr u32 WS_XSTAT = 256 * 1024;
constexpr u32 WS_HSTAT = 512 * 1024;
constexpr u32 WS_CS    = 768 * 1024;
constexpr u32 WS_ZSTAT = 1024 * 1024;
constexpr u32 WS_O     = 2u << 20;
constexpr u32 WS_H     = 6u << 20;
constexpr u32 WS_WINP  = 10u << 20;
constexpr u32 WS_Z2N   = 18u << 20;
constexpr u32 WS_U     = 26u << 20;
constexpr u32 WS_END   = 34u << 20;
__host__ __device__ constexpr u32 flag_off(int kind, int rp, int cj) { return WS_FLAGS + (u32)((kind * 256 + rp * 16 + cj) * 128); }

constexpr int P1_A = 144 * 128, P1_B = 192 * 128, P1_ST = P1_A + P1_B + 16;
constexpr int P1_ONES = P1_A + P1_B;
constexpr int WSTG = 3 * P1_ST;
constexpr int G2_B = 0, G2_A = 6 * 8192;
constexpr int AT_BASE = G2_A, AT_RS = 80, AT_BLK = 72 * AT_RS;
constexpr int G3_ST = 16384 + 14336;
constexpr int Z1BUF = 4 * G3_ST;
constexpr int TAB = 156 * 1024;
constexpr int T_XST = TAB;
constexpr int T_ZM = TAB, T_ZR = TAB + 512, T_HM = TAB + 1152, T_HR = T_HM + 512, T_CS = T_HR + 512;
constexpr int T_TAG = T_CS + 896, T_DUMMY = T_TAG + 16;
constexpr int LDS_BYTES = 160 * 1024;
static_assert(WSTG + 2 * 14336 <= TAB, "lds");
static_assert(AT_BASE + 12 * AT_BLK <= TAB, "lds");
static_assert(G2_A + 6 * 16384 <= TAB, "lds");
static_assert(Z1BUF + 28672 <= TAB, "lds");
static_assert(T_DUMMY + 256 <= LDS_BYTES, "lds");

#define MK_NORM 21
#define MK_FLAG 25
#define MK_SWEEP 18
constexpr int NORM_T = MK_NORM;
constexpr int FLAG_T = MK_FLAG;

struct Params {
  const u16* x; const u16* wqkv; const u16* wo; const u16* win; const u16* wsp; const u16* bsp; const u16* wout;
  u16* out; char* ws;
};

template <int I> struct IC { static constexpr int v = I; };
template <int N, int I = 0, class F> __device__ __forceinline__ void sfor(F&& f) { if constexpr (I < N) { f(IC<I>{}); sfor<N, I + 1>(f); } }
#define MK_WAITV(n) asm volatile("s_waitcnt vmcnt(%0)" ::"i"(n) : "memory")
#define MK_LGKM0() __builtin_amdgcn_s_waitcnt(0xC07F)
#define MK_SB() __builtin_amdgcn_sched_barrier(0)
__device__ __forceinline__ void bar_h() { asm volatile("s_waitcnt lgkmcnt(0)\n\ts_barrier" ::: "memory"); }
__device__ __forceinline__ void bar_n() { asm volatile("s_barrier" ::: "memory"); }
__device__ __forceinline__ void bar_c() { asm volatile("" ::: "memory"); __builtin_amdgcn_s_barrier(); asm volatile("" ::: "memory"); }

__device__ __forceinline__ rsrc_t mk_rsrc(const void* p, u32 bytes) { return __builtin_amdgcn_make_buffer_rsrc((void*)p, 0, bytes, 0x00020000); }
template <int OFF, int AUX> __device__ __forceinline__ void glds(rsrc_t r, u32 ldsdst, u32 voff, u32 soff) {
  __builtin_amdgcn_raw_ptr_buffer_load_lds(r, (lds_vp)(size_t)ldsdst, 16, (int)voff, (int)soff, OFF, AUX);
}
constexpr int SC1 = 16;
__device__ __forceinline__ u32 pk_bf16(float lo, float hi) { f32x2 v = {lo, hi}; return __builtin_bit_cast(u32, __builtin_convertvector(v, bf16x2n)); }
__device__ __forceinline__ float asf(u32 u) { return __builtin_bit_cast(float, u); }
__device__ __forceinline__ u32 asu(float f) { return __builtin_bit_cast(u32, f); }
__device__ __forceinline__ float bf_lo(u32 u) { return __builtin_bit_cast(float, u << 16); }
__device__ __forceinline__ float bf_hi(u32 u) { return __builtin_bit_cast(float, u & 0xffff0000u); }
template <int CTRL> __device__ __forceinline__ float dpp(float x) {
  return __builtin_bit_cast(float, __builtin_amdgcn_update_dpp(0, __builtin_bit_cast(int, x), CTRL, 0xf, 0xf, true));
}
__device__ __forceinline__ float sum16(float x) { x += dpp<0xB1>(x); x += dpp<0x4E>(x); x += dpp<0x141>(x); x += dpp<0x140>(x); return x; }
__device__ __forceinline__ float sumg(float x) {
  u32 u = __builtin_bit_cast(u32, x);
  auto a = __builtin_amdgcn_permlane16_swap(u, u, false, false);
  x = __builtin_bit_cast(float, (u32)a[0]) + __builtin_bit_cast(float, (u32)a[1]);
  u = __builtin_bit_cast(u32, x);
  auto b = __builtin_amdgcn_permlane32_swap(u, u, false, false);
  return __builtin_bit_cast(float, (u32)b[0]) + __builtin_bit_cast(float, (u32)b[1]);
}
__device__ __forceinline__ float maxg(float x) {
  u32 u = __builtin_bit_cast(u32, x);
  auto a = __builtin_amdgcn_permlane16_swap(u, u, false, false);
  x = fmaxf(__builtin_bit_cast(float, (u32)a[0]), __builtin_bit_cast(float, (u32)a[1]));
  u = __builtin_bit_cast(u32, x);
  auto b = __builtin_amdgcn_permlane32_swap(u, u, false, false);
  return fmaxf(__builtin_bit_cast(float, (u32)b[0]), __builtin_bit_cast(float, (u32)b[1]));
}
__device__ __forceinline__ u32x4 pair_swap(u32x2 a, u32x2 b) {
  auto r0 = __builtin_amdgcn_permlane16_swap(a[0], b[0], false, false);
  auto r1 = __builtin_amdgcn_permlane16_swap(a[1], b[1], false, false);
  return (u32x4){(u32)r0[0], (u32)r1[0], (u32)r0[1], (u32)r1[1]};
}
__device__ __forceinline__ float gelu_f(float x) {
  const float e = __builtin_amdgcn_exp2f(x * (x * x * -0.10294324f + -2.30220820f));
  return x * __builtin_amdgcn_rcpf(1.0f + e);
}
__device__ __forceinline__ f32x4 gelu4(f32x4 x) {
  const f32x4 y = x * (x * x * -0.10294324f + -2.30220820f);
  f32x4 e;
#pragma unroll
  for (int i = 0; i < 4; ++i) e[i] = __builtin_amdgcn_exp2f(y[i]);
  e += 1.0f;
#pragma unroll
  for (int i = 0; i < 4; ++i) e[i] = __builtin_amdgcn_rcpf(e[i]);
  return x * e;
}
__device__ __forceinline__ u32x2 gelu_pk4(u32x2 z, float r) {
  float a, b, c, d, ta, tb, tc, td; u32 o0, o1;
  asm("v_lshlrev_b32 %2, 16, %10\n\tv_and_b32 %3, 0xffff0000, %10\n\tv_lshlrev_b32 %4, 16, %11\n\tv_and_b32 %5, 0xffff0000, %11\n\t"
      "v_mul_f32 %2, %12, %2\n\tv_mul_f32 %3, %12, %3\n\tv_mul_f32 %4, %12, %4\n\tv_mul_f32 %5, %12, %5\n\t"
      "v_mul_f32 %6, %2, %2\n\tv_mul_f32 %7, %3, %3\n\tv_mul_f32 %8, %4, %4\n\tv_mul_f32 %9, %5, %5\n\t"
      "v_fma_f32 %6, %6, %13, %14\n\tv_fma_f32 %7, %7, %13, %14\n\tv_fma_f32 %8, %8, %13, %14\n\tv_fma_f32 %9, %9, %13, %14\n\t"
      "v_mul_f32 %6, %2, %6\n\tv_mul_f32 %7, %3, %7\n\tv_mul_f32 %8, %4, %8\n\tv_mul_f32 %9, %5, %9\n\t"
      "v_exp_f32 %6, %6\n\tv_exp_f32 %7, %7\n\tv_exp_f32 %8, %8\n\tv_exp_f32 %9, %9\n\t"
      "v_add_f32 %6, 1.0, %6\n\tv_add_f32 %7, 1.0, %7\n\tv_add_f32 %8, 1.0, %8\n\tv_add_f32 %9, 1.0, %9\n\t"
      "v_rcp_f32 %6, %6\n\tv_rcp_f32 %7, %7\n\tv_rcp_f32 %8, %8\n\tv_rcp_f32 %9, %9\n\t"
      "v_mul_f32 %2, %2, %6\n\tv_mul_f32 %3, %3, %7\n\tv_mul_f32 %4, %4, %8\n\tv_mul_f32 %5, %5, %9\n\t"
      "v_cvt_pk_bf16_f32 %0, %2, %3\n\tv_cvt_pk_bf16_f32 %1, %4, %5"
      : "=&v"(o0), "=&v"(o1), "=&v"(a), "=&v"(b), "=&v"(c), "=&v"(d), "=&v"(ta), "=&v"(tb), "=&v"(tc), "=&v"(td)
      : "v"((u32)z[0]), "v"((u32)z[1]), "v"(r), "s"(-0.10294324f), "v"(-2.30220820f));
  return (u32x2){o0, o1};
}
__device__ __forceinline__ void add2(float& s, float& q, u32 a, u32 b) {
  asm("v_add_f32 %0, %0, %2\n\tv_add_f32 %1, %1, %3" : "+v"(s), "+v"(q) : "v"(a), "v"(b));
}
template <class T> __device__ __forceinline__ T lds_ld(u32 a) { return *(const LAS T*)(size_t)a; }
template <class T> __device__ __forceinline__ void lds_st(u32 a, T v) { *(LAS T*)(size_t)a = v; }
__device__ __forceinline__ bf16x4 lds_tr(u32 a) { return __builtin_amdgcn_ds_read_tr16_b64_v4i16((LAS bf16x4*)(size_t)a); }
template <int O0, int O1, int O2, int O3> __device__ __forceinline__ void lds_tr4(u32 a, u32 b, bf16x8& f0, bf16x8& f1) {
  bf16x4 r0, r1, r2, r3;
  asm volatile("ds_read_b64_tr_b16 %0, %4 offset:%6\n\tds_read_b64_tr_b16 %1, %5 offset:%7\n\tds_read_b64_tr_b16 %2, %4 offset:%8\n\tds_read_b64_tr_b16 %3, %5 offset:%9\n\ts_waitcnt lgkmcnt(0)"
               : "=&v"(r0), "=&v"(r1), "=&v"(r2), "=&v"(r3) : "v"(a), "v"(b), "i"(O0), "i"(O1), "i"(O2), "i"(O3) : "memory");
  f0 = __builtin_shufflevector(r0, r1, 0, 1, 2, 3, 4, 5, 6, 7); f1 = __builtin_shufflevector(r2, r3, 0, 1, 2, 3, 4, 5, 6, 7);
}
__device__ __forceinline__ u32 opaque(u32 x) { asm("" : "+v"(x)); return x; }

struct Ctx {
  int lane, wid, rp, cj, r16, g, q, p;
  u32 lds;
  u32 tag_lo, tag_hi;
  rsrc_t rws;
};
constexpr int SPIN_CAP = 1 << 17;
#define STAMP(i) do { } while (0)

__device__ __forceinline__ void load_tag(Ctx& c) {
  const u32x2 e = __builtin_amdgcn_raw_buffer_load_b64(c.rws, (int)WS_EPOCH, 0, SC1);
  const u64 t = (((u64)e[1] << 32) | e[0]) + 1;
  c.tag_lo = __builtin_amdgcn_readfirstlane((u32)t); c.tag_hi = __builtin_amdgcn_readfirstlane((u32)(t >> 32));
}
__device__ __forceinline__ void publish(const Ctx& c, int kind, int slot) {
  asm volatile("s_waitcnt vmcnt(0)" ::: "memory");
  if (c.lane == 0) __builtin_amdgcn_raw_buffer_store_b64((u32x2){c.tag_lo, c.tag_hi}, c.rws, (int)(flag_off(kind, c.rp, c.cj) + 8 * slot), 0, SC1);
}
__device__ __forceinline__ void poll64(const Ctx& c, u32 voff) {
#pragma unroll 1
  for (int s = 0; s < SPIN_CAP; ++s) {
    asm volatile("" ::: "memory");
    const u32x2 v = __builtin_amdgcn_raw_buffer_load_b64(c.rws, (int)voff, 0, SC1);
    if (__all(v[0] == c.tag_lo && v[1] == c.tag_hi)) break;
    __builtin_amdgcn_s_sleep(1);
  }
  asm volatile("" ::: "memory");
}
__device__ __forceinline__ u32x2 poll_issue(const Ctx& c, u32 voff) { return __builtin_amdgcn_raw_buffer_load_b64(c.rws, (int)voff, 0, SC1); }
__device__ __forceinline__ void poll_check(const Ctx& c, u32 voff, u32x2 v) {
  if (!__all(v[0] == c.tag_lo && v[1] == c.tag_hi)) poll64(c, voff);
  asm volatile("" ::: "memory");
}
__device__ __forceinline__ u32x4 tag_pair(const Ctx& c, float a, float b) {
  return (u32x4){asu(a), c.tag_lo ^ asu(b), asu(b), ~c.tag_lo ^ asu(a)};
}
__device__ __forceinline__ u32 tag_bad(const Ctx& c, u32x4 v) { return (v[1] ^ v[2] ^ c.tag_lo) | (v[3] ^ v[0] ^ ~c.tag_lo); }
template <int AUX = 0> __device__ __forceinline__ void touch(const Ctx& c, rsrc_t r, u32 voff, u32 soff) {
  __builtin_amdgcn_raw_ptr_buffer_load_lds(r, (lds_vp)(size_t)(c.lds + T_DUMMY), 4, (int)voff, (int)soff, 0, AUX);
}
__device__ __forceinline__ void poll128(const Ctx& c, u32 voff) {
#pragma unroll 1
  for (int s = 0; s < SPIN_CAP; ++s) {
    asm volatile("" ::: "memory");
    const u32x4 v = __builtin_amdgcn_raw_buffer_load_b128(c.rws, (int)voff, 0, SC1);
    if (__all(v[0] == c.tag_lo && v[1] == c.tag_hi && v[2] == c.tag_lo && v[3] == c.tag_hi)) break;
    __builtin_amdgcn_s_sleep(1);
  }
  asm volatile("" ::: "memory");
}

struct Win { u32 b[3]; };
__device__ __forceinline__ Win mk_win(u32 base) { Win w; w.b[0] = opaque(base); w.b[1] = opaque(base + 65536u); w.b[2] = opaque(base + 131072u); return w; }
template <int OFF> __device__ __forceinline__ u32 wad(const Win& w) { return w.b[OFF >> 16] + (u32)(OFF & 0xffff); }

template <int KS, int MT, int NT> struct Set { bf16x8 a[KS][MT]; bf16x8 b[KS][NT]; bf16x4 t[KS][NT]; };

__device__ __forceinline__ void mfma_acc(f32x4& c, const bf16x8& a, const bf16x8& b) {
  asm volatile("v_mfma_f32_16x16x32_bf16 %0, %1, %2, %0" : "+v"(c) : "v"(a), "v"(b));
}
__device__ __forceinline__ void mfma_in() { MK_SB(); asm volatile("s_nop 1"); MK_SB(); }
__device__ __forceinline__ void mfma_out() { MK_SB(); asm volatile("s_nop 7\n\ts_nop 1"); MK_SB(); }
template <int KS, int MT, int NT, bool SWAP, int NRD, class GAP, class RD>
__device__ __forceinline__ void part(f32x4 (&acc)[MT][NT], const Set<KS, MT, NT>& X, GAP, RD&& rd) {
  constexpr int NMF = KS * MT * NT;
  MK_SB();
  sfor<NMF>([&](auto ic) {
    constexpr int i = decltype(ic)::v, ks = i / (MT * NT), m = (i / NT) % MT, n = i % NT;
    if constexpr (SWAP) mfma_acc(acc[m][n], X.b[ks][n], X.a[ks][m]); else mfma_acc(acc[m][n], X.a[ks][m], X.b[ks][n]);
    MK_SB();
    sfor<NRD>([&](auto kc) { if constexpr (GAP::gap(decltype(kc)::v) == i) rd(kc); });
    if constexpr (i == NMF - 2) MK_LGKM0();
    MK_SB();
  });
}
struct Gap1 { static constexpr int gap(int k) { return k; } };
struct GapG2 { static constexpr int gap(int k) { return k < 2 ? k : (k < 10 ? 2 + (k - 2) / 2 : (k < 12 ? k - 4 : 8 + (k - 12) / 2)); } };

struct AdP1 { Win a[2], a0[2], b[3]; };
template <int ST, int KSI, int K> __device__ __forceinline__ void rd_p1(const AdP1& ad, Set<1, 9, 3>& Y) {
  constexpr int SB = ST * P1_ST;
  if constexpr (K == 0) Y.a[0][0] = lds_ld<bf16x8>(wad<SB>(ad.a0[KSI]));
  else if constexpr (K <= 6) {
    constexpr int n = (K - 1) >> 1, h = (K - 1) & 1;
    const bf16x4 v = lds_tr(wad<SB + P1_A + 4096 * KSI + 512 * h>(ad.b[n]));
    if constexpr (h == 0) Y.t[0][n] = v; else Y.b[0][n] = __builtin_shufflevector(Y.t[0][n], v, 0, 1, 2, 3, 4, 5, 6, 7);
  } else { constexpr int m = K - 6; Y.a[0][m] = lds_ld<bf16x8>(wad<SB + m * 2048>(ad.a[KSI])); }
}
struct AdG3 { Win af, ar[2], b; };
template <int ST, int KSI, bool ROWS, int K> __device__ __forceinline__ void rd_g3(const AdG3& ad, Set<1, 2, 7>& Y) {
  constexpr int SB = ST * G3_ST;
  if constexpr (K == 0 || K == 8) {
    constexpr int m = K == 0 ? 0 : 1;
    if constexpr (ROWS) Y.a[0][m] = lds_ld<bf16x8>(wad<SB + m * 2048>(ad.ar[KSI]));
    else Y.a[0][m] = lds_ld<bf16x8>(wad<SB + KSI * 8192 + m * 1024>(ad.af));
  } else { constexpr int n = K - 1; Y.b[0][n] = lds_ld<bf16x8>(wad<SB + 16384 + (KSI * 7 + n) * 1024>(ad.b)); }
}
struct AdG2 { Win a, b[4]; };
template <int ST, int K> __device__ __forceinline__ void rd_g2(const AdG2& ad, Set<2, 2, 4>& Y) {
  constexpr int ks = K / 10, k = K % 10;
  if constexpr (k < 2) Y.a[ks][k] = lds_ld<bf16x8>(wad<G2_A + ST * 16384 + ks * 8192 + k * 1024>(ad.a));
  else {
    constexpr int n = (k - 2) >> 1, h = (k - 2) & 1;
    const bf16x4 v = lds_tr(wad<G2_B + ST * 8192 + 4096 * ks + 512 * h>(ad.b[n]));
    if constexpr (h == 0) Y.t[ks][n] = v; else Y.b[ks][n] = __builtin_shufflevector(Y.t[ks][n], v, 0, 1, 2, 3, 4, 5, 6, 7);
  }
}
__device__ __forceinline__ u32 tr64_lane(const Ctx& c, int strip) {
  const int f = (c.q >> 1) | ((c.g & 1) << 1);
  return (u32)((strip >> 2) * 8192 + 128 * (8 * c.g + c.q) + 16 * ((2 * (strip & 3) + (c.p >> 1)) ^ (2 * f)) + 8 * (c.p & 1));
}

template <int ST, bool LAST> __device__ __forceinline__ void tile_p1(f32x4 (&acc)[9][3], Set<1, 9, 3>& X, Set<1, 9, 3>& Y, const AdP1& ad) {
  part<1, 9, 3, true, 15>(acc, X, Gap1{}, [&](auto kc) { rd_p1<ST, 1, decltype(kc)::v>(ad, Y); });
  bar_c();
  part<1, 9, 3, true, LAST ? 0 : 15>(acc, Y, Gap1{}, [&](auto kc) { rd_p1<(ST + 1) % 3, 0, decltype(kc)::v>(ad, X); });
}
template <int ST, bool SWAP, bool ROWS, bool NROWS, bool LAST, bool DRAIN = false>
__device__ __forceinline__ void tile_g3(f32x4 (&acc)[2][7], Set<1, 2, 7>& X, Set<1, 2, 7>& Y, const AdG3& ad) {
  part<1, 2, 7, SWAP, (ROWS && (MK_ABL & 128)) ? 0 : 9>(acc, X, Gap1{}, [&](auto kc) { rd_g3<ST, 1, ROWS, decltype(kc)::v>(ad, Y); });
  if constexpr (DRAIN) MK_WAITV(0);
  bar_c();
  part<1, 2, 7, SWAP, (LAST || (ROWS && (MK_ABL & 128))) ? 0 : 9>(acc, Y, Gap1{}, [&](auto kc) { rd_g3<(ST + 1) % 4, 0, NROWS, decltype(kc)::v>(ad, X); });
}
template <int ST, bool BAR, bool LAST> __device__ __forceinline__ void tile_g2(f32x4 (&acc)[2][4], Set<2, 2, 4>& X, Set<2, 2, 4>& Y, const AdG2& ad) {
  if constexpr (BAR) bar_c();
  part<2, 2, 4, true, (LAST || (MK_ABL & 64)) ? 0 : 20>(acc, X, GapG2{}, [&](auto kc) { rd_g2<(ST + 1) % 6, decltype(kc)::v>(ad, Y); });
}
template <int NG, bool LASTBAR> __device__ __forceinline__ void gemm_g2(f32x4 (&acc)[2][4], const Ctx& c, const Params& P) {
  AdG2 ad;
  ad.a = mk_win(c.lds + (u32)(c.wid * 2048 + c.lane * 16));
#pragma unroll
  for (int n = 0; n < 4; ++n) ad.b[n] = mk_win(c.lds + tr64_lane(c, n));
  Set<2, 2, 4> X, Y;
  bar_c();
  sfor<20>([&](auto kc) { rd_g2<0, decltype(kc)::v>(ad, X); });
  if (MK_ABL & 64) sfor<20>([&](auto kc) { rd_g2<0, decltype(kc)::v>(ad, Y); });
  MK_LGKM0();
#pragma unroll 1
  for (int it = 0; it < NG; ++it) {
    STAMP((NG == 4 ? 32 : 30) + it);
    tile_g2<0, true, false>(acc, X, Y, ad); tile_g2<1, true, false>(acc, Y, X, ad); tile_g2<2, true, false>(acc, X, Y, ad);
    tile_g2<3, true, false>(acc, Y, X, ad); tile_g2<4, true, false>(acc, X, Y, ad); tile_g2<5, true, false>(acc, Y, X, ad);
  }
  tile_g2<0, true, false>(acc, X, Y, ad); tile_g2<1, true, false>(acc, Y, X, ad); tile_g2<2, true, false>(acc, X, Y, ad);
  tile_g2<3, LASTBAR, true>(acc, Y, X, ad);
  mfma_out();
}

__device__ __forceinline__ void attention(const Ctx& c) {
  const int hh = c.wid & 1, T = c.wid >> 1;
  const int lo = (c.rp == 0 && T == 0) ? 8 : 0;
  const float sc = 0.17677669529663687f * 1.4426950408889634f;
#pragma unroll
  for (int par = 0; par < 2; ++par) {
    const u32 bq = c.lds + AT_BASE + (u32)(((0 * 2 + hh) * 2 + par) * AT_BLK);
    const u32 bk = c.lds + AT_BASE + (u32)(((1 * 2 + hh) * 2 + par) * AT_BLK);
    const u32 bv = c.lds + AT_BASE + (u32)(((2 * 2 + hh) * 2 + par) * AT_BLK);
    const bf16x8 qf = lds_ld<bf16x8>(bq + (u32)((8 + 16 * T + c.r16) * AT_RS + 16 * c.g));
    f32x4 s[2];
    bf16x8 vf[2];
#pragma unroll
    for (int k2 = 0; k2 < 2; ++k2) {
      const int kr = min(16 * T + 16 * k2 + c.r16, 71);
      const bf16x8 kf = lds_ld<bf16x8>(bk + (u32)(kr * AT_RS + 16 * c.g));
      s[k2] = __builtin_amdgcn_mfma_f32_16x16x32_bf16(kf, qf, (f32x4){0.f, 0.f, 0.f, 0.f}, 0, 0, 0);
    }
    {
      const int v0 = 16 * T + 4 * c.g + c.q, v1 = min(v0 + 16, 71);
      lds_tr4<0, 0, 32, 32>(bv + (u32)(v0 * AT_RS + 8 * c.p), bv + (u32)(v1 * AT_RS + 8 * c.p), vf[0], vf[1]);
    }
    float e[8], mx = -3.0e38f;
#pragma unroll
    for (int j = 0; j < 8; ++j) {
      const int ak = 16 * (j >> 2) + 4 * c.g + (j & 3), w = 8 + c.r16 - ak;
      const bool ok = (w >= 0) && (w <= 7) && (ak >= lo);
      e[j] = ok ? s[j >> 2][j & 3] : -3.0e38f;
      mx = fmaxf(mx, e[j]);
    }
    mx = maxg(mx) * -sc;
    float sum = 0.f;
#pragma unroll
    for (int j = 0; j < 8; ++j) { e[j] = __builtin_amdgcn_exp2f(__builtin_fmaf(e[j], sc, mx)); sum += e[j]; }
    sum = sumg(sum);
    const float inv = __builtin_amdgcn_rcpf(sum);
    const u32x4 pw = {pk_bf16(e[0] * inv, e[1] * inv), pk_bf16(e[2] * inv, e[3] * inv), pk_bf16(e[4] * inv, e[5] * inv), pk_bf16(e[6] * inv, e[7] * inv)};
    const bf16x8 pf = __builtin_bit_cast(bf16x8, pw);
    const f32x4 o0 = __builtin_amdgcn_mfma_f32_16x16x32_bf16(vf[0], pf, (f32x4){0.f, 0.f, 0.f, 0.f}, 0, 0, 0);
    const f32x4 o1 = __builtin_amdgcn_mfma_f32_16x16x32_bf16(vf[1], pf, (f32x4){0.f, 0.f, 0.f, 0.f}, 0, 0, 0);
    const u32x4 ov = pair_swap((u32x2){pk_bf16(o0[0], o0[1]), pk_bf16(o0[2], o0[3])}, (u32x2){pk_bf16(o1[0], o1[1]), pk_bf16(o1[2], o1[3])});
    const int G = ((c.g & 1) << 1) | (c.g >> 1);
    const u32 off = WS_O + (u32)(((c.rp * 32 + 2 * c.cj + hh) * 8 + 2 * T + par) * 1024 + (16 * G + c.r16) * 16);
    __builtin_amdgcn_raw_buffer_store_b128(ov, c.rws, (int)off, 0, SC1);
  }
  publish(c, FO, c.wid);
}

__device__ __forceinline__ float red8(const float (&v)[8], int r16) {
  const bool b2 = r16 & 4, b1 = r16 & 2, b0 = r16 & 1;
  float u[4], t[2];
#pragma unroll
  for (int i = 0; i < 4; ++i) u[i] = (b2 ? v[4 + i] : v[i]) + dpp<0x141>(b2 ? v[i] : v[4 + i]);
#pragma unroll
  for (int i = 0; i < 2; ++i) t[i] = (b1 ? u[2 + i] : u[i]) + dpp<0x4E>(b1 ? u[i] : u[2 + i]);
  const float x = (b0 ? t[1] : t[0]) + dpp<0xB1>(b0 ? t[0] : t[1]);
  return x + dpp<0x128>(x);
}

__device__ __forceinline__ void consumer(const Params& P, Ctx& c) {
  STAMP(0);
  const int w = c.wid, G = ((c.g & 1) << 1) | (c.g >> 1);
  const rsrc_t rx = mk_rsrc(P.x, 2048u * 2048u);
  bar_c();
  load_tag(c);
  {
    if (c.lane < 12) lds_st<u32>(c.lds + (u32)((c.lane >> 2) * P1_ST + P1_ONES + 4 * (c.lane & 3)), 0x3f803f80u);
    if (w == 0 && c.lane == 0) lds_st<u32x2>(c.lds + T_TAG, (u32x2){c.tag_lo, c.tag_hi});
    AdP1 ad;
#pragma unroll
    for (int ks = 0; ks < 2; ++ks) {
      const u32 a = (u32)(c.r16 * 128 + (((4 * ks + c.g) ^ (c.r16 >> 1)) * 16));
      ad.a[ks] = mk_win(c.lds + a);
      ad.a0[ks] = mk_win(c.lds + (c.r16 == 0 ? (u32)P1_ONES : a));
    }
#pragma unroll
    for (int n = 0; n < 3; ++n) ad.b[n] = mk_win(c.lds + tr64_lane(c, 3 * w + n));
    f32x4 acc[9][3];
#pragma unroll
    for (int m = 0; m < 9; ++m)
#pragma unroll
      for (int n = 0; n < 3; ++n) { acc[m][n] = (f32x4){0.f, 0.f, 0.f, 0.f}; asm volatile("" : "+v"(acc[m][n])); }
    Set<1, 9, 3> X, Y;
    MK_LGKM0();
    bar_c();
    STAMP(1);
    sfor<15>([&](auto kc) { rd_p1<0, 0, decltype(kc)::v>(ad, X); });
    MK_LGKM0();
#pragma unroll 1
    for (int it = 0; it < 5; ++it) { tile_p1<0, false>(acc, X, Y, ad); tile_p1<1, false>(acc, X, Y, ad); tile_p1<2, false>(acc, X, Y, ad); }
    part<1, 9, 3, true, 15>(acc, X, Gap1{}, [&](auto kc) { rd_p1<0, 1, decltype(kc)::v>(ad, Y); });
    bar_c();
    f32x2 xs[9];
    const u32 xa = opaque(c.lds + T_XST + (u32)(c.r16 * 8));
    part<1, 9, 3, true, 9>(acc, Y, Gap1{}, [&](auto kc) { constexpr int m = decltype(kc)::v; xs[m] = lds_ld<f32x2>(xa + m * 128); });
    mfma_out();
    STAMP(2);
#pragma unroll
    for (int n = 0; n < 3; ++n) {
      const int S = 3 * w + n;
      f32x4 cs;
#pragma unroll
      for (int r = 0; r < 4; ++r) cs[r] = dpp<0x150>(acc[0][n][r]);
      const u32 sb = c.lds + AT_BASE + (u32)((S >> 1) * 2 * AT_BLK + (S & 1) * 32 + 8 * c.g);
      const u32 s0 = opaque(sb + (u32)((c.r16 >> 3) * AT_BLK + (c.r16 & 7) * AT_RS)), s1 = opaque(sb + (u32)((8 + c.r16) * AT_RS));
#pragma unroll
      for (int m = 0; m < 9; ++m) {
        const f32x4 v = (acc[m][n] - xs[m][0] * cs) * xs[m][1];
        lds_st<u32x2>(m == 0 ? s0 : s1 + (u32)(((m - 1) & 1) * AT_BLK + ((m - 1) >> 1) * 16 * AT_RS), (u32x2){pk_bf16(v[0], v[1]), pk_bf16(v[2], v[3])});
      }
    }
  }
  MK_LGKM0();
  bar_c();
  STAMP(3);
  u32x4 xr[2][2];
#pragma unroll
  for (int m = 0; m < 2; ++m)
#pragma unroll
    for (int pr = 0; pr < 2; ++pr)
      xr[m][pr] = __builtin_amdgcn_raw_buffer_load_b128(rx, (128 * c.rp + 32 * w + 2 * c.r16 + m) * 2048 + (64 * c.cj + 32 * pr + 8 * G) * 2, 0, 0);
  attention(c);
  STAMP(4);
  u32x2 hb[2][4];
  {
    f32x4 acc[2][4];
#pragma unroll
    for (int m = 0; m < 2; ++m)
#pragma unroll
      for (int pr = 0; pr < 2; ++pr) {
        const u32x4 t = pair_swap((u32x2){xr[m][pr][0], xr[m][pr][1]}, (u32x2){xr[m][pr][2], xr[m][pr][3]});
        acc[m][2 * pr] = (f32x4){bf_lo(t[0]), bf_hi(t[0]), bf_lo(t[1]), bf_hi(t[1])};
        acc[m][2 * pr + 1] = (f32x4){bf_lo(t[2]), bf_hi(t[2]), bf_lo(t[3]), bf_hi(t[3])};
        asm volatile("" : "+v"(acc[m][2 * pr]), "+v"(acc[m][2 * pr + 1]));
      }
    bar_c();
    STAMP(5);
    gemm_g2<2, true>(acc, c, P);
    STAMP(6);
    const u32 ho = WS_H + (u32)(((c.rp * 32 + 2 * c.cj) * 8 + 2 * w) * 1024 + (16 * G + c.r16) * 16);
#pragma unroll
    for (int m = 0; m < 2; ++m) {
#pragma unroll
      for (int n = 0; n < 4; ++n) hb[m][n] = (u32x2){pk_bf16(acc[m][n][0], acc[m][n][1]), pk_bf16(acc[m][n][2], acc[m][n][3])};
#pragma unroll
      for (int pr = 0; pr < 2; ++pr) __builtin_amdgcn_raw_buffer_store_b128(pair_swap(hb[m][2 * pr], hb[m][2 * pr + 1]), c.rws, (int)(ho + m * 1024), pr * 8192, SC1);
    }
    float s[2], q[2];
#pragma unroll
    for (int m = 0; m < 2; ++m) {
      f32x2 s2 = {0.f, 0.f}, q2 = {0.f, 0.f};
#pragma unroll
      for (int n = 0; n < 4; ++n)
#pragma unroll
        for (int d = 0; d < 2; ++d) { const f32x2 a = {bf_lo(hb[m][n][d]), bf_hi(hb[m][n][d])}; s2 += a; q2 += a * a; }
      s[m] = sumg(s2[0] + s2[1]); q[m] = sumg(q2[0] + q2[1]);
    }
    if (c.g < 2) __builtin_amdgcn_raw_buffer_store_b64((u32x2){asu(c.g ? s[1] : s[0]), asu(c.g ? q[1] : q[0])}, c.rws,
        (int)(WS_HSTAT + (u32)(((c.rp * 16 + c.cj) * 128 + 32 * w + c.lane) * 8)), 0, SC1);
    STAMP(28);
    bar_c();
    publish(c, FHH, w);
    STAMP(7);
  }
  {
    AdG3 ad;
    ad.af = mk_win(c.lds + (u32)(w * 2048 + c.lane * 16));
#pragma unroll
    for (int ks = 0; ks < 2; ++ks) ad.ar[ks] = mk_win(c.lds + (u32)((32 * w + c.r16) * 128 + (((4 * ks + c.g) ^ (c.r16 >> 1)) * 16)));
    ad.b = mk_win(c.lds + (u32)(c.lane * 16));
    float bsp[2];
#pragma unroll
    for (int m = 0; m < 2; ++m) bsp[m] = bf_lo((u32)P.bsp[128 * c.rp + 32 * w + 2 * c.r16 + m]);
    f32x4 acc[2][7];
#pragma unroll
    for (int m = 0; m < 2; ++m)
#pragma unroll
      for (int n = 0; n < 7; ++n) { acc[m][n] = (f32x4){0.f, 0.f, 0.f, 0.f}; asm volatile("" : "+v"(acc[m][n])); }
    asm volatile("" : "+v"(bsp[0]), "+v"(bsp[1]));
    const u32 tg = opaque(c.lds + TAB + (u32)((32 * w + 4 * c.g) * 4));
    const u32 tr = opaque(c.lds + TAB + (u32)((32 * w + c.r16) * 4));
    const u32 z1a = opaque(c.lds + Z1BUF + (u32)(w * 7168 + c.lane * 8));
    Set<1, 2, 7> X, Y;
    bar_c();
    STAMP(8);
    bar_c();
    STAMP(9);
    sfor<9>([&](auto kc) { rd_g3<0, 0, false, decltype(kc)::v>(ad, X); });
    MK_LGKM0();
#pragma unroll 1
    for (int it = 0; it < 4; ++it) {
      tile_g3<0, false, false, false, false>(acc, X, Y, ad); tile_g3<1, false, false, false, false>(acc, X, Y, ad);
      tile_g3<2, false, false, false, false>(acc, X, Y, ad); tile_g3<3, false, false, false, false>(acc, X, Y, ad);
    }
    f32x4 hm[2], hr[2], c1[7];
    float c2[7], nm[2];
#pragma unroll
    for (int m = 0; m < 2; ++m) {
      hm[m] = lds_ld<f32x4>(tg + (T_HM - TAB) + 64 * m); hr[m] = lds_ld<f32x4>(tg + (T_HR - TAB) + 64 * m);
      nm[m] = lds_ld<float>(tr + (T_HM - TAB) + 64 * m);
    }
#pragma unroll
    for (int n = 0; n < 7; ++n) c2[n] = lds_ld<float>(c.lds + T_CS + (u32)((16 * n + c.r16) * 4));
    mfma_out();
    STAMP(10);
    f32x4 z2[2][7];
    {
      f32x4 sm[2] = {{0.f, 0.f, 0.f, 0.f}, {0.f, 0.f, 0.f, 0.f}}, sq[2] = {{0.f, 0.f, 0.f, 0.f}, {0.f, 0.f, 0.f, 0.f}}, ga[2], gb[2];
#pragma unroll
      for (int m = 0; m < 2; ++m) { ga[m] = hr[m] * -2.30220820f; gb[m] = hr[m] * hr[m] * hr[m] * -0.10294324f; }
#pragma unroll
      for (int n = 0; n < 7; ++n) {
#pragma unroll
        for (int m = 0; m < 2; ++m) {
          const f32x4 t = acc[m][n] - hm[m] * c2[n];
          f32x4 y = t * (t * t * gb[m] + ga[m]);
          asm("" : "+v"(y));
          f32x4 e;
#pragma unroll
          for (int i = 0; i < 4; ++i) e[i] = __builtin_amdgcn_exp2f(y[i]);
          e += 1.0f;
#pragma unroll
          for (int i = 0; i < 4; ++i) e[i] = __builtin_amdgcn_rcpf(e[i]);
          const f32x4 z = t * e;
          z2[m][n] = z; sm[m] += z; sq[m] += z * z;
        }
      }
#pragma unroll
      for (int m = 0; m < 2; ++m) { sm[m] *= hr[m]; sq[m] *= hr[m] * hr[m]; }
      MK_SB();
      const u32 ca = c.lds + T_CS + (u32)((112 + 4 * c.g) * 4);
#pragma unroll
      for (int n = 0; n < 7; ++n) c1[n] = lds_ld<f32x4>(ca + 64 * n);
      const float sv[8] = {sm[0][0], sm[0][1], sm[0][2], sm[0][3], sm[1][0], sm[1][1], sm[1][2], sm[1][3]};
      const float qv[8] = {sq[0][0], sq[0][1], sq[0][2], sq[0][3], sq[1][0], sq[1][1], sq[1][2], sq[1][3]};
      const float s = red8(sv, c.r16), q = red8(qv, c.r16);
      if (c.r16 < 8) __builtin_amdgcn_raw_buffer_store_b128(tag_pair(c, s, q), c.rws,
          (int)(WS_ZSTAT + (u32)(((c.rp * 16 + c.cj) * 128 + 32 * w + 16 * (c.r16 >> 2) + 4 * c.g + (c.r16 & 3)) * 16)), 0, SC1);
#pragma unroll
      for (int m = 0; m < 2; ++m)
#pragma unroll
        for (int n = 0; n < 7; ++n) { acc[m][n] = c1[n] * -nm[m]; asm volatile("" : "+v"(acc[m][n])); }
    }
    STAMP(11);
    mfma_in();
    sfor<16>([&](auto tc) {
      constexpr int t = 16 + decltype(tc)::v;
      tile_g3<t % 4, true, false, t == 31, false, t == FLAG_T>(acc, X, Y, ad);
      if constexpr (t == NORM_T) {
        f32x4 zr[2], zo[2];
#pragma unroll
        for (int m = 0; m < 2; ++m) {
          zr[m] = lds_ld<f32x4>(tg + (T_ZR - TAB) + 64 * m) * lds_ld<f32x4>(tg + (T_HR - TAB) + 64 * m);
          zo[m] = lds_ld<f32x4>(tg + (T_ZM - TAB) + 64 * m);
        }
#pragma unroll
        for (int n = 0; n < 7; ++n) {
          const f32x4 a = z2[0][n] * zr[0] + zo[0], b = z2[1][n] * zr[1] + zo[1];
          const u32x4 zf = {pk_bf16(a[0], b[0]), pk_bf16(a[1], b[1]), pk_bf16(a[2], b[2]), pk_bf16(a[3], b[3])};
          __builtin_amdgcn_raw_buffer_store_b128(zf, c.rws, (int)(WS_Z2N + (u32)((c.cj * 64 + 4 * c.rp + w) * 7168 + c.lane * 16 + (n & 3) * 1024)), (n >> 2) * 4096, SC1);
        }
      }
      if constexpr (t == NORM_T - 1) STAMP(18);
      if constexpr (t == NORM_T) STAMP(19);
      if constexpr (t == 24) STAMP(20);
      if constexpr (t == 27) STAMP(21);
      if constexpr (t == 28) STAMP(22);
    });
    mfma_out();
    STAMP(12);
#pragma unroll
    for (int m = 0; m < 2; ++m)
#pragma unroll
      for (int n = 0; n < 7; ++n) {
        lds_st<u32x2>(z1a + (u32)((m * 7 + n) * 512), (u32x2){pk_bf16(acc[m][n][0], acc[m][n][1]), pk_bf16(acc[m][n][2], acc[m][n][3])});
        acc[m][n] = (f32x4){bsp[m], bsp[m], bsp[m], bsp[m]};
        asm volatile("" : "+v"(acc[m][n]));
      }
    mfma_in();
#pragma unroll 1
    for (int it = 0; it < 7; ++it) {
      tile_g3<0, true, true, true, false>(acc, X, Y, ad); tile_g3<1, true, true, true, false>(acc, X, Y, ad);
      tile_g3<2, true, true, true, false>(acc, X, Y, ad); tile_g3<3, true, true, true, false>(acc, X, Y, ad);
    }
    tile_g3<0, true, true, true, false>(acc, X, Y, ad); tile_g3<1, true, true, true, false>(acc, X, Y, ad);
    tile_g3<2, true, true, true, false>(acc, X, Y, ad);
    part<1, 2, 7, true, 9>(acc, X, Gap1{}, [&](auto kc) { rd_g3<3, 1, true, decltype(kc)::v>(ad, Y); });
    bar_c();
    u32x2 z1[12 + 2];
    part<1, 2, 7, true, 12>(acc, Y, Gap1{}, [&](auto kc) { constexpr int k = decltype(kc)::v; z1[k] = lds_ld<u32x2>(z1a + k * 512); });
    z1[12] = lds_ld<u32x2>(z1a + 12 * 512); z1[13] = lds_ld<u32x2>(z1a + 13 * 512);
    mfma_out();
    STAMP(13);
    {
      const int odd = c.cj & 1, kp = (7 * c.cj + 1) >> 1, ksg = (7 * c.cj + (odd ? -1 : 6)) >> 1;
      const u32 up = WS_U + (u32)(((c.rp * 56 + kp) * 8 + 2 * w) * 1024 + c.lane * 16);
      const u32 us = WS_U + (u32)(((c.rp * 56 + ksg) * 8 + 2 * w) * 1024 + (16 * (2 * odd + (c.g >> 1)) + c.r16) * 16 + 8 * (c.g & 1));
#pragma unroll
      for (int m = 0; m < 2; ++m) {
        u32x2 uv[7];
#pragma unroll
        for (int n = 0; n < 7; ++n) {
          const u32x2 z = z1[m * 7 + n];
          const f32x4 u = (f32x4){bf_lo(z[0]), bf_hi(z[0]), bf_lo(z[1]), bf_hi(z[1])} * acc[m][n];
          uv[n] = (u32x2){pk_bf16(u[0], u[1]), pk_bf16(u[2], u[3])};
          if (n & 1) __builtin_amdgcn_raw_buffer_store_b128((u32x4){uv[n - 1][0], uv[n - 1][1], uv[n][0], uv[n][1]}, c.rws, (int)(up + m * 1024), (n >> 1) * 8192, SC1);
        }
        __builtin_amdgcn_raw_buffer_store_b64(uv[6], c.rws, (int)(us + m * 1024), 0, SC1);
      }
      STAMP(29);
      bar_c();
      publish(c, FU, w);
      STAMP(14);
    }
  }
  {
    f32x4 acc[2][4];
#pragma unroll
    for (int m = 0; m < 2; ++m)
#pragma unroll
      for (int n = 0; n < 4; ++n) {
        acc[m][n] = (f32x4){bf_lo(hb[m][n][0]), bf_hi(hb[m][n][0]), bf_lo(hb[m][n][1]), bf_hi(hb[m][n][1])};
        asm volatile("" : "+v"(acc[m][n]));
      }
    bar_c();
    STAMP(15);
    gemm_g2<4, false>(acc, c, P);
    STAMP(16);
    const rsrc_t ro = mk_rsrc(P.out, 2048u * 2048u);
#pragma unroll
    for (int m = 0; m < 2; ++m)
#pragma unroll
      for (int pr = 0; pr < 2; ++pr) {
        const f32x4 a = acc[m][2 * pr], b = acc[m][2 * pr + 1];
        __builtin_amdgcn_raw_buffer_store_b128(pair_swap((u32x2){pk_bf16(a[0], a[1]), pk_bf16(a[2], a[3])}, (u32x2){pk_bf16(b[0], b[1]), pk_bf16(b[2], b[3])}), ro,
            (128 * c.rp + 32 * w + 2 * c.r16 + m) * 2048 + (64 * c.cj + 32 * pr + 8 * G) * 2, 0, 0);
      }
    if (c.rp == 0 && c.cj == 0 && w == 0 && c.lane == 0)
      __builtin_amdgcn_raw_buffer_store_b64((u32x2){c.tag_lo, c.tag_hi}, c.rws, (int)WS_EPOCH, 0, SC1);
    STAMP(17);
  }
}

template <int N, int AUX> __device__ __forceinline__ void issue_v(rsrc_t r, u32 dst, const u32 (&vo)[N], u32 so) {
  sfor<N>([&](auto ic) { constexpr int s = decltype(ic)::v; glds<(s & 3) * 1024, AUX>(r, dst + (s >> 2) * 4096, vo[s], so); });
}
template <int N, int AUX> __device__ __forceinline__ void issue_v2(rsrc_t r, u32 dst, const u32 (&vo)[N], u32 so) {
  sfor<N>([&](auto ic) { constexpr int s = decltype(ic)::v; glds<(s & 1) * 2048, AUX>(r, dst + (s >> 1) * 4096, vo[s], so); });
}
template <int N, int AUX> __device__ __forceinline__ void issue_l(rsrc_t r, u32 dst, u32 vo, u32 so) {
  sfor<N>([&](auto ic) { constexpr int s = decltype(ic)::v; glds<(s & 3) * 1024, AUX>(r, dst + (s >> 2) * 4096, vo, so + (s >> 2) * 4096); });
}
#define MK_PIN2(a, b) asm volatile("" : "+s"(a), "+s"(b))
__device__ __forceinline__ u32 tr64_src(int lane, int lw, int ldb_bytes) {
  const int f = ((lane >> 4) & 1) | (lw << 1);
  return (u32)((8 * lw + (lane >> 3)) * ldb_bytes + (((lane & 7) ^ (2 * f)) * 16));
}
template <int T, bool LASTBAR>
__device__ __forceinline__ u32x2 load_g2(const Ctx& c, int lw, u32 abase, rsrc_t rb, const u32 (&vb)[4], u32 bstep, u32 fo) {
  const u32 va = abase + (u32)(lw * 8192 + c.lane * 16), da = c.lds + G2_A + (u32)(lw * 8192), db = c.lds + G2_B + (u32)(lw * 1024);
  auto A = [&](int t, int st) { issue_l<8, SC1>(c.rws, da + (u32)(st * 16384), va, (u32)(t * 16384)); };
  auto B = [&](int t, int st) { issue_v2<4, 0>(rb, db + (u32)(st * 8192), vb, (u32)t * bstep); };
  A(0, 0); A(1, 1); MK_WAITV(8); bar_n();
  A(2, 2); A(3, 3); MK_WAITV(16); bar_n();
  A(4, 4); A(5, 5); A(6, 0); B(6, 0); MK_WAITV(36); bar_n();
  A(7, 1); B(7, 1); MK_WAITV(40); bar_n();
  A(8, 2); B(8, 2);
  u32 dA = da + 3 * 16384, dB = db + 3 * 8192, sA = 9 * 16384, sB = 9 * bstep;
  MK_PIN2(dA, dB); MK_PIN2(sA, sB);
  MK_WAITV(44);
#pragma unroll 1
  for (int t = 9; t < T; ++t) {
    bar_n();
    issue_l<8, SC1>(c.rws, dA, va, sA); issue_v2<4, 0>(rb, dB, vb, sB);
    const bool wrap = dB == db + 5 * 8192;
    dA = wrap ? da : dA + 16384; dB = wrap ? db : dB + 8192; sA += 16384; sB += bstep;
    MK_PIN2(dA, dB); MK_PIN2(sA, sB);
    MK_WAITV(48);
  }
  bar_n();
  MK_WAITV(36); bar_n(); MK_WAITV(24); bar_n(); MK_WAITV(12); bar_n(); MK_WAITV(0); bar_n();
  u32x2 fv = {0, 0};
  if (LASTBAR) { fv = poll_issue(c, fo); bar_n(); }
  return fv;
}
__device__ __forceinline__ void pre_g2(const Ctx& c, int lw, rsrc_t rb, u32 (&vb)[4]) {
  const u32 b0 = tr64_src(c.lane, lw, 2048) + (u32)(128 * c.cj + 4096);
#pragma unroll
  for (int s = 0; s < 4; ++s) vb[s] = b0 + (u32)(s * 16 * 2048 - (s & 1) * 2048);
#pragma unroll 1
  for (int t = 0; t < 6; ++t) issue_v2<4, 0>(rb, c.lds + G2_B + (u32)(lw * 1024 + t * 8192), vb, (u32)(t * 64 * 2048));
}

__device__ __forceinline__ void loader(const Params& P, Ctx& c) {
  const int lw = c.wid - 4, lane = c.lane;
  {
    const rsrc_t rx = mk_rsrc((const char*)P.x - 4096, 2048u * 2048u + 4096u);
    const rsrc_t rw = mk_rsrc((const char*)P.wqkv - 4096, 1024u * 6144u + 4096u);
    u32 va[9], vb[12];
    const u32 ch = (u32)((((lane & 7) ^ (4 * lw + (lane >> 4))) * 16) + 4096);
    va[0] = (u32)(max(128 * c.rp - 16 + 2 * (lane >> 3) + lw, 0) * 2048) + ch;
    const u32 a1 = (u32)((128 * c.rp + 16 * lw + 2 * (lane >> 3)) * 2048) + ch;
#pragma unroll
    for (int m = 1; m < 9; ++m) va[m] = a1 + (u32)((32 * ((m - 1) >> 1) + ((m - 1) & 1)) * 2048 - (m & 1) * 2048);
    const u32 b0 = tr64_src(lane, lw, 6144) + (u32)(128 * c.cj + 4096);
#pragma unroll
    for (int s = 0; s < 12; ++s) vb[s] = b0 + (u32)((s >> 2) * 2048 + (s & 3) * 16 * 6144 - (s & 1) * 2048);
    auto I = [&](int t, int st) {
      const u32 sb = c.lds + (u32)(st * P1_ST + lw * 1024);
      issue_v2<9, 0>(rx, sb, va, (u32)(t * 128));
      issue_v2<12, 0>(rw, sb + P1_A, vb, (u32)(t * 64 * 6144));
    };
    I(0, 0); bar_n();
    MK_WAITV(0); bar_n(); I(1, 1);
    I(2, 2);
    const u32 d0 = c.lds + (u32)(lw * 1024);
    u32 d = d0, sA = 3 * 128, sB = 3 * 64 * 6144;
    MK_PIN2(d, sA); MK_PIN2(d, sB);
    MK_WAITV(21);
#pragma unroll 1
    for (int t = 3; t < 16; ++t) {
      bar_n();
      issue_v2<9, 0>(rx, d, va, sA); issue_v2<12, 0>(rw, d + P1_A, vb, sB);
      d = d == d0 + 2 * P1_ST ? d0 : d + P1_ST; sA += 128; sB += 64 * 6144;
      MK_PIN2(d, sA); MK_PIN2(d, sB);
      MK_WAITV(21);
    }
    bar_n();
    MK_WAITV(0); bar_n();
    bar_n();
  }
  const u32 fwo = flag_off(FW, 0, 0) + (u32)((((lane >> 1) & 15) * 16 + c.cj) * 128 + 8 * (lane & 1));
  u32x2 fwv;
  {
    const rsrc_t rb = mk_rsrc((const char*)P.wo - 4096, 1024u * 2048u + 4096u);
    u32 vb[4];
    pre_g2(c, lw, rb, vb);
    const u32x2 tg = lds_ld<u32x2>(c.lds + T_TAG);
    c.tag_lo = __builtin_amdgcn_readfirstlane(tg[0]); c.tag_hi = __builtin_amdgcn_readfirstlane(tg[1]);
    bar_h();
    attention(c);
    STAMP(26);
    if (lw == 0) poll128(c, flag_off(FO, 0, 0) + (u32)((c.rp * 16 + (lane >> 2)) * 128 + 16 * (lane & 3)));
    STAMP(27);
    bar_h();
    fwv = load_g2<16, true>(c, lw, WS_O + (u32)(c.rp * 32 * 8192), rb, vb, 64 * 2048, fwo);
  }
  {
    const rsrc_t rs = mk_rsrc((const char*)P.wsp - 4096, 2048u * 4096u + 4096u);
    const u32 vah = WS_H + (u32)(c.rp * 32 * 8192 + lw * 8192 + lane * 16);
    const u32 vbw = WS_WINP + (u32)(c.cj * 32 * 14336 + lw * 7168 + lane * 16);
    const u32 vbz = WS_Z2N + (u32)(((MK_ABL & 512) ? (c.cj & 1) : c.cj) * 32 * 14336 + lw * 7168 + lane * 16);
    const u32 da = c.lds + (u32)(lw * 8192), db = c.lds + (u32)(16384 + lw * 7168);
    auto A3 = [&](int t, int st) { issue_l<8, SC1>(c.rws, da + (u32)(st * G3_ST), vah, (u32)((t & 15) * 16384)); };
    auto B3 = [&](int t, int st) { issue_l<7, SC1>(c.rws, db + (u32)(st * G3_ST), vbw, (u32)(t * 14336)); };
    u32 vs[8];
    const u32 s0 = (u32)((128 * ((MK_ABL & 1024) ? (c.rp & 1) : c.rp) + 16 * lw + 2 * (lane >> 3)) * 4096 + (((lane & 7) ^ (4 * lw + (lane >> 4))) * 16) + 4096);
#pragma unroll
    for (int m = 0; m < 8; ++m) vs[m] = s0 + (u32)((32 * (m >> 1) + (m & 1)) * 4096 - (m & 1) * 2048);
    bar_h();
    poll_check(c, fwo, fwv);
    B3(0, 0); B3(1, 1); B3(2, 2); B3(3, 3);
    bar_h();
    A3(0, 0); A3(1, 1); MK_WAITV(8); bar_h();
    A3(2, 2); A3(3, 3); MK_WAITV(16); bar_h();
    A3(4, 0); B3(4, 0);
    u32 so = G3_ST, sA = 5 * 16384, sB = 5 * 14336;
    MK_PIN2(so, sA); MK_PIN2(so, sB);
    MK_WAITV(23);
#pragma unroll 1
    for (int t = 5; t < 32; ++t) {
      bar_n();
      issue_l<8, SC1>(c.rws, da + so, vah, sA); issue_l<7, SC1>(c.rws, db + so, vbw, sB);
      so = so == 3 * G3_ST ? 0 : so + G3_ST; sA = (sA + 16384) & (16 * 16384 - 1); sB += 14336;
      MK_PIN2(so, sA); MK_PIN2(so, sB);
      MK_WAITV(30);
    }
    const u32 dr = c.lds + (u32)(lw * 1024);
    auto A4 = [&](int t) { issue_v2<8, 0>(rs, dr + (u32)((t & 3) * G3_ST), vs, (u32)(t * 128)); };
    auto B4 = [&](int t) { issue_l<7, SC1>(c.rws, db + (u32)((t & 3) * G3_ST), vbz, (u32)(t * 14336)); };
    bar_n(); A4(0); MK_WAITV(23);
    bar_n(); B4(0); A4(1); B4(1);
    so = 2 * G3_ST; sA = 2 * 128; sB = 2 * 14336;
    MK_PIN2(so, sA); MK_PIN2(so, sB);
    MK_WAITV(30);
#pragma unroll 1
    for (int t = 2; t < 32; ++t) {
      bar_n();
      if (!(MK_ABL & 256)) { issue_v2<8, 0>(rs, dr + so, vs, sA); issue_l<7, SC1>(c.rws, db + so, vbz, sB); }
      so = so == 3 * G3_ST ? 0 : so + G3_ST; sA += 128; sB += 14336;
      MK_PIN2(so, sA); MK_PIN2(so, sB);
      MK_WAITV(30);
    }
    bar_n();
    MK_WAITV(15); bar_n(); MK_WAITV(0); bar_n();
    bar_n();
  }
  {
    const rsrc_t rb = mk_rsrc((const char*)P.wout - 4096, 1792u * 2048u + 4096u);
    u32 vb[4];
    bar_h();
    pre_g2(c, lw, rb, vb);
    bar_h();
    load_g2<28, false>(c, lw, WS_U + (u32)(c.rp * 56 * 8192), rb, vb, 64 * 2048, 0);
  }
}

__device__ __forceinline__ void bars(int n) {
#pragma unroll 1
  for (int i = 0; i < n; ++i) bar_n();
}
__device__ __forceinline__ void sumsq2(float& s, float& q, u32 u) {
  float a, b;
  asm("v_lshlrev_b32 %2, 16, %4\n\tv_and_b32 %3, 0xffff0000, %4\n\tv_add_f32 %0, %0, %2\n\tv_fmac_f32 %1, %2, %2\n\tv_add_f32 %0, %0, %3\n\tv_fmac_f32 %1, %3, %3"
      : "+v"(s), "+v"(q), "=&v"(a), "=&v"(b) : "v"(u));
}
__device__ __forceinline__ void worker(const Params& P, Ctx& c) {
  const int v = c.wid - 6, lane = c.lane;
  const u32 stg = c.lds + WSTG + (u32)(v * 14336);
  {
    const rsrc_t rx = mk_rsrc(P.x, 2048u * 2048u);
    const rsrc_t rq = mk_rsrc(P.wqkv, 1024u * 6144u);
    const int b = blockIdx.x, idx = b >> 3;
    const int cslow = (c.cj & 8) + ((3 - (int)((size_t)P.wqkv >> 7)) & 7);
    auto warm_slow = [&](int row0, int nrow) {
      const int i = lane / 3, g = lane - 3 * i;
      if (MK_PREFETCH & 2) if (32 * i < nrow) touch(c, rq, (u32)((row0 + idx + 32 * i) * 6144 + g * 2048 + cslow * 128), 0);
    };
    u32x4 xr[4][2];
    auto burst = [&](auto hc) {
      constexpr int h = decltype(hc)::v;
      if (v == 1 && (MK_PREFETCH & 1)) {
        const int r = lane / 48;
        touch(c, rq, (u32)(((b & 63) + 64 * (4 * (2 * h + r) + (b >> 6))) * 6144 + (lane - 48 * r) * 128), 0);
        if (lane < 32) touch(c, rq, (u32)(((b & 63) + 64 * (4 * (2 * h + 1) + (b >> 6))) * 6144 + (16 + lane) * 128), 0);
      }
#pragma unroll
      for (int i = 0; i < 4; ++i) xr[i][h] = __builtin_amdgcn_raw_buffer_load_b128(rx, (128 * c.rp + 8 * c.cj + 4 * v + i) * 2048 + lane * 16, h * 1024, 0);
    };
    bar_n();
    if (v == 0) warm_slow(64, 384);
    burst(IC<0>{});
    bar_n();
    if (MK_PREFETCH & 4) {
      u64 pc0; u32 back, len;
      asm volatile("s_getpc_b64 %0\n.Lmk_pc%=:\n\ts_mov_b32 %1, .Lmk_pc%=-.Lmk_code_begin\n\ts_mov_b32 %2, .Lmk_code_end-.Lmk_code_begin" : "=s"(pc0), "=s"(back), "=s"(len));
      const rsrc_t rc = mk_rsrc((const void*)((pc0 - back) & ~127ull), len);
      if (v == 0 && lane < 16) touch(c, rc, (u32)((idx + 32 * lane) * 128), 0);
    }
    { const u32x2 tg = lds_ld<u32x2>(c.lds + T_TAG);
      c.tag_lo = __builtin_amdgcn_readfirstlane(tg[0]); c.tag_hi = __builtin_amdgcn_readfirstlane(tg[1]); }
    bars(MK_B2);
    burst(IC<1>{});
    bars(5 - MK_B2);
    float sm = 0.f, sr = 0.f;
#pragma unroll
    for (int i = 0; i < 4; ++i) {
      float s = 0.f, q = 0.f;
#pragma unroll
      for (int h = 0; h < 2; ++h)
#pragma unroll
        for (int d = 0; d < ((MK_ABL & 2) ? 0 : 4); ++d) { u32 u = xr[i][h][d]; sumsq2(s, q, u); }
      const float mean = sumg(sum16(s)) * (1.0f / 1024.0f);
      const float rs = __builtin_amdgcn_rsqf(fmaxf(sumg(sum16(q)) * (1.0f / 1024.0f) - mean * mean, 0.f) + 1e-5f);
      sm = lane == i ? mean : sm; sr = lane == i ? rs : sr;
      asm volatile("" : "+v"(sm), "+v"(sr));
      if (i == 3) { if (lane < 4) __builtin_amdgcn_raw_buffer_store_b128(tag_pair(c, sm, sr), c.rws, (int)(WS_XSTAT + (u32)((128 * c.rp + 8 * c.cj + 4 * v + lane) * 16)), 0, SC1); }
      if (i == 0 && v == 1) warm_slow(448, 320);
      bar_n();
    }
    if (v == 0) warm_slow(768, 256);
    else if (MK_PREFETCH & 8) touch(c, mk_rsrc(P.wo, 1024u * 2048u), (u32)(lane * 128), b * 8192u);
    bars(2);
    u32x4 xg[2];
    u32 xo[2];
#pragma unroll
    for (int i = 0; i < 2; ++i) {
      const int rho = min(72 * v + 64 * i + lane, 72 * v + 71), m = rho >> 4, sl = rho & 15;
      const int il = m == 0 ? (2 * (sl & 7) + (sl >> 3)) : 16 + 32 * ((m - 1) >> 1) + 2 * sl + ((m - 1) & 1);
      xo[i] = WS_XSTAT + (u32)(max(128 * c.rp - 16 + il, 0) * 16);
      xg[i] = __builtin_amdgcn_raw_buffer_load_b128(c.rws, (int)xo[i], 0, SC1);
    }
    bars(2);
    if (!__all((tag_bad(c, xg[0]) | tag_bad(c, xg[1])) == 0)) {
#pragma unroll 1
      for (int sp = 0; sp < SPIN_CAP; ++sp) {
        asm volatile("" ::: "memory");
#pragma unroll
        for (int i = 0; i < 2; ++i) xg[i] = __builtin_amdgcn_raw_buffer_load_b128(c.rws, (int)xo[i], 0, SC1);
        if (__all((tag_bad(c, xg[0]) | tag_bad(c, xg[1])) == 0)) break;
      }
    }
#pragma unroll
    for (int i = 0; i < 2; ++i) lds_st<u32x2>(c.lds + T_XST + (u32)(min(72 * v + 64 * i + lane, 72 * v + 71) * 8), (u32x2){xg[i][0], xg[i][2]});
    bar_h();
    {
      const rsrc_t rw = mk_rsrc((const char*)P.win - 4096, 1024u * 7168u + 4096u);
      int k = lane / 14, ch = lane - 14 * k;
      u32 vo = (u32)((64 * c.rp + k) * 7168 + ((v == 0 ? 1792 : 0) + 112 * c.cj) * 2 + ch * 16 + 4096);
      sfor<14>([&](auto ic) {
        constexpr int j = decltype(ic)::v;
        glds<(j & 3) * 1024, 0>(rw, stg + (j >> 2) * 4096, vo - (j & 3) * 1024, 0);
        ch += 8; const bool wrap = ch >= 14; ch -= wrap ? 14 : 0; vo += wrap ? (u32)(5 * 7168 - 96) : (u32)(4 * 7168 + 128);
        if constexpr (j == 6) bar_n();
      });
    }
    bar_n();
  }
  bar_n();
  STAMP(23);
  attention(c);
  STAMP(24);
  {
    const int odd = c.cj & 1;
    const u32 ta = stg + (u32)((8 * c.g + c.q) * 224);
    const u32 tp = ta + (u32)(((odd ? 16 : 0) + 8 * c.p) * 2), ts = ta + (u32)(((odd ? 0 : 96) + 4 * c.p) * 2);
    const u32 wo_ = WS_WINP + (u32)(((c.cj * 2 + v) * 32 + 2 * c.rp) * 7168 + lane * 16);
    float cs = 0.f;
    sfor<7>([&](auto nc) {
      constexpr int n = decltype(nc)::v, o = n < 6 ? 64 * (n >> 1) + 8 * (n & 1) : 0;
      bf16x8 f2[2];
      lds_tr4<o, o + 896, o + 7168, o + 7168 + 896>(n < 6 ? tp : ts, n < 6 ? tp : ts, f2[0], f2[1]);
      f32x2 s2 = {0.f, 0.f};
#pragma unroll
      for (int kl = 0; kl < 2; ++kl) {
        const u32x4 fr = __builtin_bit_cast(u32x4, f2[kl]);
        __builtin_amdgcn_raw_buffer_store_b128(fr, c.rws, (int)(wo_ + (n & 3) * 1024), kl * 7168 + (n >> 2) * 4096, SC1);
#pragma unroll
        for (int d = 0; d < 4; ++d) s2 += (f32x2){bf_lo(fr[d]), bf_hi(fr[d])};
      }
      const float t = sumg(s2[0] + s2[1]);
      if constexpr (n < 4) cs = (c.g == n) ? t : cs;
      if constexpr (n == 3) __builtin_amdgcn_raw_buffer_store_b32(asu(cs), c.rws, (int)(WS_CS + (u32)(((c.rp * 16 + c.cj) * 256 + 112 * v + lane) * 4)), 0, SC1);
      if constexpr (n >= 4) cs = (c.g == n - 4) ? t : cs;
    });
    if (lane < 48) __builtin_amdgcn_raw_buffer_store_b32(asu(cs), c.rws, (int)(WS_CS + (u32)(((c.rp * 16 + c.cj) * 256 + 112 * v + 64 + lane) * 4)), 0, SC1);
  }
  STAMP(25);
  bar_h();
  bars(4);
  publish(c, FW, v);
  bars(13);
  bar_n();
  if ((MK_PREFETCH & 8) && v == 1) {
    const rsrc_t r3 = mk_rsrc(P.wout, 1792u * 2048u);
    touch(c, r3, (u32)(lane * 128), blockIdx.x * 14336u);
    if (lane < 48) touch(c, r3, (u32)(lane * 128), blockIdx.x * 14336u + 8192u);
  }
  if (v == 0) poll64(c, flag_off(FHH, 0, 0) + (u32)((c.rp * 16 + (lane >> 2)) * 128 + 8 * (lane & 3)));
  bar_n();
  {
    const int hi = lane >> 5, l5 = lane & 31;
    auto fold = [](float x) { const u32 u = asu(x); auto r = __builtin_amdgcn_permlane32_swap(u, u, false, false); return asf((u32)r[0]) + asf((u32)r[1]); };
    bars(3);
    u32x4 hp[8], cp[8];
#pragma unroll
    for (int j = 0; j < 8; ++j) {
      hp[j] = __builtin_amdgcn_raw_buffer_load_b128(c.rws, (int)(WS_HSTAT + (u32)(((c.rp * 16 + hi) * 128 + 64 * v + 2 * l5) * 8)), j * 2048, SC1);
      cp[j] = __builtin_amdgcn_raw_buffer_load_b128(c.rws, (int)(WS_CS + (u32)(((hi * 16 + c.cj) * 256 + 112 * v + 4 * min(l5, 27)) * 4)), j * 32768, SC1);
    }
    bars(6);
    float s0 = 0.f, q0 = 0.f, s1 = 0.f, q1 = 0.f;
#pragma unroll
    for (int j = 0; j < 8; ++j) { add2(s0, q0, hp[j][0], hp[j][1]); add2(s1, q1, hp[j][2], hp[j][3]); }
    {
      s0 = fold(s0); q0 = fold(q0); s1 = fold(s1); q1 = fold(q1);
      const float sx = hi ? s1 : s0, qx = hi ? q1 : q0;
      const float mean = sx * (1.0f / 1024.0f);
      const float rs = __builtin_amdgcn_rsqf(fmaxf(qx * (1.0f / 1024.0f) - mean * mean, 0.f) + 1e-5f);
      lds_st<float>(c.lds + T_HM + (u32)((64 * v + 2 * l5 + hi) * 4), mean);
      lds_st<float>(c.lds + T_HR + (u32)((64 * v + 2 * l5 + hi) * 4), rs);
    }
    bar_n();
    float c0 = 0.f, c1 = 0.f, c2 = 0.f, c3 = 0.f;
#pragma unroll
    for (int j = 0; j < 8; ++j) { add2(c0, c1, cp[j][0], cp[j][1]); add2(c2, c3, cp[j][2], cp[j][3]); }
    lds_st<f32x4>(c.lds + T_CS + (u32)((112 * v + 4 * min(l5, 27)) * 4), (f32x4){fold(c0), fold(c1), fold(c2), fold(c3)});
    bar_h();
    bars(6);
    if (MK_PREFETCH & 8) {
      const rsrc_t r2 = mk_rsrc(P.wsp, 2048u * 4096u);
      touch(c, r2, (u32)(lane * 128), blockIdx.x * 32768u + v * 16384u);
      touch(c, r2, (u32)(lane * 128), blockIdx.x * 32768u + v * 16384u + 8192u);
    }
    if (MK_PREFETCH & 16) {
      const u32 o = WS_WINP + (u32)(c.cj * 32 * 14336 + 20 * 14336 + (c.rp & 3) * 43008 + v * 21504);
#pragma unroll
      for (int i = 0; i < 3; ++i) if (i < 2 || lane < 40) touch<SC1>(c, c.rws, (u32)(lane * 128), o + i * 8192u);
    }
    bars(MK_SWEEP - 16);
    u32x4 zp[16];
    auto sweep = [&]() {
#pragma unroll
      for (int j = 0; j < 16; ++j) zp[j] = __builtin_amdgcn_raw_buffer_load_b128(c.rws, (int)(WS_ZSTAT + (u32)((c.rp * 2048 + 64 * v + lane) * 16)), j * 2048, SC1);
    };
    sweep();
    bars(NORM_T - 1 - MK_SWEEP);
    float zs = 0.f, zq = 0.f;
    u32 bad = 0;
    auto half = [&](int j0) {
#pragma unroll
      for (int j = j0; j < j0 + 8; ++j) {
        u32 t1, t2;
        asm("v_bitop3_b32 %3, %5, %6, %8 bitop3:0x96\n\tv_bitop3_b32 %4, %7, %9, %8 bitop3:0x69\n\tv_add_f32 %0, %0, %9\n\tv_add_f32 %1, %1, %6\n\tv_or3_b32 %2, %2, %3, %4"
            : "+v"(zs), "+v"(zq), "+v"(bad), "=&v"(t1), "=&v"(t2) : "v"(zp[j][1]), "v"(zp[j][2]), "v"(zp[j][3]), "s"(c.tag_lo), "v"(zp[j][0]));
      }
    };
    half(0);
    asm volatile("" : "+v"(zs), "+v"(zq), "+v"(bad));
    bar_n();
    half(8);
    if (!__all(bad == 0)) {
#pragma unroll 1
      for (int sp = 0; sp < SPIN_CAP; ++sp) {
        asm volatile("" ::: "memory");
        sweep();
        zs = 0.f; zq = 0.f; bad = 0;
        half(0); half(8);
        if (__all(bad == 0)) break;
      }
    }
    const float zm = zs * (1.0f / 1792.0f);
    const float zr = __builtin_amdgcn_rsqf(fmaxf(zq * (1.0f / 1792.0f) - zm * zm, 0.f) + 1e-5f);
    lds_st<float>(c.lds + T_ZM + (u32)((64 * v + lane) * 4), -zm * zr);
    lds_st<float>(c.lds + T_ZR + (u32)((64 * v + lane) * 4), zr);
    bar_h();
    bars(FLAG_T - NORM_T);
    if (v == 1 && lane == 0) __builtin_amdgcn_raw_buffer_store_b64((u32x2){c.tag_lo, c.tag_hi}, c.rws, (int)flag_off(FZ, c.rp, c.cj), 0, SC1);
    bars(27 - FLAG_T);
    const u32 fo = flag_off(FZ, 0, 0) + (u32)(((lane & 15) * 16 + c.cj) * 128);
    u32x2 fv = {0, 0};
    if (v == 0) fv = poll_issue(c, fo);
    bar_n();
    if (v == 0) poll_check(c, fo, fv);
    bars(4);
    u32 za = c.lds + Z1BUF + (u32)(v * 14336 + lane * 8);
    const u32 ra = c.lds + T_HR + (u32)((64 * v + c.r16) * 4);
    float rr[4];
#pragma unroll
    for (int i = 0; i < 4; ++i) rr[i] = lds_ld<float>(ra + 64 * i);
    u32x2 zn = lds_ld<u32x2>(za), zo = zn;
    bar_h();
    u32 zw = za;
#pragma unroll
    for (int i = 0; i < 4; ++i) {
#pragma unroll 1
      for (int n = 0; n < 7; ++n) {
        const u32x2 z = zn;
        if (!(MK_ABL & 1)) lds_st<u32x2>(zw, zo);
        zn = lds_ld<u32x2>(za + 512);
        MK_SB();
        zo = gelu_pk4(z, rr[i]);
        zw = za; za += 512;
        MK_SB();
        MK_LGKM0();
        bar_n();
      }
    }
    if (!(MK_ABL & 1)) lds_st<u32x2>(zw, zo);
    bar_h(); bar_n();
  }
  bar_n();
  if (v == 0) poll64(c, flag_off(FU, 0, 0) + (u32)((c.rp * 16 + (lane >> 2)) * 128 + 8 * (lane & 3)));
  bar_n();
}

__global__ __launch_bounds__(512, 2) void fused_block(Params P) {
  extern __shared__ __attribute__((aligned(16))) char smem[];
  asm volatile(".Lmk_code_begin:");
  Ctx c;
  c.lane = threadIdx.x & 63;
  c.wid = __builtin_amdgcn_readfirstlane(threadIdx.x >> 6);
  const int xcd = blockIdx.x & 7, idx = blockIdx.x >> 3;
  c.rp = (xcd & 3) * 4 + (idx & 3);
  c.cj = (xcd >> 2) * 8 + (idx >> 2);
  c.r16 = c.lane & 15; c.g = c.lane >> 4; c.q = (c.lane >> 2) & 3; c.p = c.lane & 3;
  c.lds = (u32)(size_t)(LAS char*)smem;
  c.rws = mk_rsrc(P.ws, WS_END);
  c.tag_lo = 0; c.tag_hi = 0;
  if (c.wid >= 6) worker(P, c); else if (c.wid >= 4) loader(P, c); else consumer(P, c);
  asm volatile(".Lmk_code_end:");
}

}

extern "C" void kernel_launch(void* const* d_in, const int* in_sizes, int n_in, void* d_out, int out_size, void* d_ws, size_t ws_size,
                              hipStream_t stream) {
  (void)in_sizes; (void)n_in; (void)out_size; (void)ws_size;
  mk::Params P;
  P.x = (const mk::u16*)d_in[0]; P.wqkv = (const mk::u16*)d_in[1]; P.wo = (const mk::u16*)d_in[2];
  P.win = (const mk::u16*)d_in[4]; P.wsp = (const mk::u16*)d_in[5]; P.bsp = (const mk::u16*)d_in[6]; P.wout = (const mk::u16*)d_in[7];
  P.out = (mk::u16*)d_out; P.ws = (char*)d_ws;
  (void)hipFuncSetAttribute((const void*)mk::fused_block, hipFuncAttributeMaxDynamicSharedMemorySize, mk::LDS_BYTES);
  hipLaunchKernelGGL(mk::fused_block, dim3(256), dim3(512), mk::LDS_BYTES, stream, P);
}
```

```cpp
#include <hip/hip_runtime.h>

namespace mk {

#define LAS __attribute__((address_space(3)))
typedef LAS void* lds_vp;
typedef unsigned short u16;
typedef unsigned int u32;
typedef unsigned long long u64;
typedef short bf16x8 __attribute__((ext_vector_type(8)));
typedef short bf16x4 __attribute__((ext_vector_type(4)));
typedef float f32x2 __attribute__((ext_vector_type(2)));
typedef float f32x4 __attribute__((ext_vector_type(4)));
typedef u32 u32x2 __attribute__((ext_vector_type(2)));
typedef u32 u32x4 __attribute__((ext_vector_type(4)));
typedef __bf16 bf16x2n __attribute__((ext_vector_type(2)));
typedef __amdgpu_buffer_rsrc_t rsrc_t;

constexpr u32 WS_EPOCH = 0;
constexpr u32 WS_FLAGS = 4096;
enum { FHS = 0, FW = 1, FO = 2, FHH = 3, FZ = 4, FU = 5 };
constexpr u32 WS_XSTAT = 256 * 1024;
constexpr u32 WS_HSTAT = 512 * 1024;
constexpr u32 WS_CS    = 768 * 1024;
constexpr u32 WS_ZSTAT = 1024 * 1024;
constexpr u32 GA = 10240, GB = 8192;
constexpr u32 WS_O     = 2u << 20;
constexpr u32 WS_H     = 8u << 20;
constexpr u32 WS_WINP  = 14u << 20;
constexpr u32 WS_Z2N   = 23u << 20;
constexpr u32 WS_U     = 32u << 20;
constexpr u32 WS_END   = 42u << 20;
__host__ __device__ constexpr u32 flag_off(int kind, int rp, int cj) { return WS_FLAGS + (u32)((kind * 256 + rp * 16 + cj) * 128); }

constexpr int P1_A = 144 * 128, P1_B = 192 * 128, P1_ST = P1_A + P1_B + 16;
constexpr int P1_ONES = P1_A + P1_B;
constexpr int G2_NB = 11, G2_B = 0, G2_A = G2_NB * 8192;
constexpr int WSTG = G2_A;
constexpr int G2_PRE = 10, AT_BASE = G2_PRE * 8192, AT_RS = 80, AT_BLK = 72 * AT_RS;
constexpr int G3_B = 6 * 16384;
__host__ __device__ constexpr int g3_aslot(int t) { return t < 32 ? t % 6 : (t + 2) % 4; }
constexpr int TAB = 156 * 1024;
constexpr int T_XST = TAB;
constexpr int T_ZM = TAB, T_ZR = TAB + 512, T_HM = TAB + 1152, T_HR = T_HM + 512, T_CS = T_HR + 512;
constexpr int T_TAG = T_CS + 896, T_CNT = T_TAG + 8, T_DUMMY = T_TAG + 32;
constexpr int LDS_BYTES = 160 * 1024;
static_assert(3 * P1_ST <= TAB && WSTG + 2 * 14336 <= TAB, "lds");
static_assert(AT_BASE + 12 * AT_BLK <= TAB, "lds");
static_assert(G2_A + 4 * 16384 <= TAB, "lds");
static_assert(G3_B + 4 * 14336 <= TAB, "lds");
static_assert(T_DUMMY + 256 <= LDS_BYTES, "lds");

constexpr int SWEEP_T = 18;
constexpr int NORM_T = 21;
constexpr int FLAG_T = 24;

struct Params {
  const u16* x; const u16* wqkv; const u16* wo; const u16* win; const u16* wsp; const u16* bsp; const u16* wout;
  u16* out; char* ws;
};

template <int I> struct IC { static constexpr int v = I; };
template <int N, int I = 0, class F> __device__ __forceinline__ void sfor(F&& f) { if constexpr (I < N) { f(IC<I>{}); sfor<N, I + 1>(f); } }
#define MK_WAITV(n) asm volatile("s_waitcnt vmcnt(%0)" ::"i"(n) : "memory")
#define MK_LGKM0() __builtin_amdgcn_s_waitcnt(0xC07F)
#define MK_SB() __builtin_amdgcn_sched_barrier(0)
__device__ __forceinline__ void bar_h() { asm volatile("s_waitcnt lgkmcnt(0)\n\ts_barrier" ::: "memory"); }
__device__ __forceinline__ void bar_n() { asm volatile("s_barrier" ::: "memory"); }
__device__ __forceinline__ void bar_c() { asm volatile("" ::: "memory"); __builtin_amdgcn_s_barrier(); asm volatile("" ::: "memory"); }

__device__ __forceinline__ rsrc_t mk_rsrc(const void* p, u32 bytes) { return __builtin_amdgcn_make_buffer_rsrc((void*)p, 0, bytes, 0x00020000); }
template <int OFF, int AUX> __device__ __forceinline__ void glds(rsrc_t r, u32 ldsdst, u32 voff, u32 soff) {
  __builtin_amdgcn_raw_ptr_buffer_load_lds(r, (lds_vp)(size_t)ldsdst, 16, (int)voff, (int)soff, OFF, AUX);
}
constexpr int SC1 = 16;
template <int AUX> __device__ __forceinline__ void st128(u32x4 v, rsrc_t r, int voff, int soff) {
  __builtin_amdgcn_raw_buffer_store_b128(v, r, voff, soff, AUX);
  asm volatile("s_nop 1" ::"v"(v) : "memory");
}
__device__ __forceinline__ u32 pk_bf16(float lo, float hi) { f32x2 v = {lo, hi}; return __builtin_bit_cast(u32, __builtin_convertvector(v, bf16x2n)); }
__device__ __forceinline__ float asf(u32 u) { return __builtin_bit_cast(float, u); }
__device__ __forceinline__ u32 asu(float f) { return __builtin_bit_cast(u32, f); }
__device__ __forceinline__ float bf_lo(u32 u) { return __builtin_bit_cast(float, u << 16); }
__device__ __forceinline__ float bf_hi(u32 u) { return __builtin_bit_cast(float, u & 0xffff0000u); }
template <int CTRL> __device__ __forceinline__ float dpp(float x) {
  return __builtin_bit_cast(float, __builtin_amdgcn_update_dpp(0, __builtin_bit_cast(int, x), CTRL, 0xf, 0xf, true));
}
__device__ __forceinline__ float sum16(float x) { x += dpp<0xB1>(x); x += dpp<0x4E>(x); x += dpp<0x141>(x); x += dpp<0x140>(x); return x; }
__device__ __forceinline__ float sumg(float x) {
  u32 u = __builtin_bit_cast(u32, x);
  auto a = __builtin_amdgcn_permlane16_swap(u, u, false, false);
  x = __builtin_bit_cast(float, (u32)a[0]) + __builtin_bit_cast(float, (u32)a[1]);
  u = __builtin_bit_cast(u32, x);
  auto b = __builtin_amdgcn_permlane32_swap(u, u, false, false);
  return __builtin_bit_cast(float, (u32)b[0]) + __builtin_bit_cast(float, (u32)b[1]);
}
__device__ __forceinline__ u32x4 pair_swap(u32x2 a, u32x2 b) {
  auto r0 = __builtin_amdgcn_permlane16_swap(a[0], b[0], false, false);
  auto r1 = __builtin_amdgcn_permlane16_swap(a[1], b[1], false, false);
  return (u32x4){(u32)r0[0], (u32)r1[0], (u32)r0[1], (u32)r1[1]};
}
__device__ __forceinline__ void add2(float& s, float& q, u32 a, u32 b) {
  asm("v_add_f32 %0, %0, %2\n\tv_add_f32 %1, %1, %3" : "+v"(s), "+v"(q) : "v"(a), "v"(b));
}
template <class T> __device__ __forceinline__ T lds_ld(u32 a) { return *(const LAS T*)(size_t)a; }
template <class T> __device__ __forceinline__ void lds_st(u32 a, T v) { *(LAS T*)(size_t)a = v; }
__device__ __forceinline__ bf16x4 lds_tr(u32 a) { return __builtin_amdgcn_ds_read_tr16_b64_v4i16((LAS bf16x4*)(size_t)a); }
template <int O0, int O1, int O2, int O3> __device__ __forceinline__ void lds_tr4(u32 a, u32 b, bf16x8& f0, bf16x8& f1) {
  bf16x4 r0, r1, r2, r3;
  asm volatile("ds_read_b64_tr_b16 %0, %4 offset:%6\n\tds_read_b64_tr_b16 %1, %5 offset:%7\n\tds_read_b64_tr_b16 %2, %4 offset:%8\n\tds_read_b64_tr_b16 %3, %5 offset:%9\n\ts_waitcnt lgkmcnt(0)"
               : "=&v"(r0), "=&v"(r1), "=&v"(r2), "=&v"(r3) : "v"(a), "v"(b), "i"(O0), "i"(O1), "i"(O2), "i"(O3) : "memory");
  f0 = __builtin_shufflevector(r0, r1, 0, 1, 2, 3, 4, 5, 6, 7); f1 = __builtin_shufflevector(r2, r3, 0, 1, 2, 3, 4, 5, 6, 7);
}
__device__ __forceinline__ u32 opaque(u32 x) { asm("" : "+v"(x)); return x; }

struct Ctx {
  int lane, wid, rp, cj, r16, g, q, p;
  u32 lds;
  u32 tag_lo, tag_hi;
  int skip;
  rsrc_t rws;
};
__device__ __forceinline__ u32 frag_off(const Ctx& c, int f, int p) {
  const u32 l = (u32)(8 * f + (p >> 3));
  return (l + (((l + (u32)c.skip) * 37u) >> 8)) * 128u + (u32)((p & 7) * 16);
}
__device__ __forceinline__ void refresh(Ctx& c) {
  u32 t = threadIdx.x;
  asm volatile("" : "+v"(t));
  c.lane = t & 63; c.r16 = t & 15; c.g = (t >> 4) & 3; c.q = (t >> 2) & 3; c.p = t & 3;
}
constexpr int SPIN_CAP = 1 << 17;

__device__ __forceinline__ void get_tag(Ctx& c) {
  const u32x2 tg = lds_ld<u32x2>(c.lds + T_TAG);
  c.tag_lo = __builtin_amdgcn_readfirstlane(tg[0]); c.tag_hi = __builtin_amdgcn_readfirstlane(tg[1]);
}
__device__ __forceinline__ void publish(const Ctx& c, int kind, int slot) {
  asm volatile("s_waitcnt vmcnt(0)" ::: "memory");
  if (c.lane == 0) __builtin_amdgcn_raw_buffer_store_b64((u32x2){c.tag_lo, c.tag_hi}, c.rws, (int)(flag_off(kind, c.rp, c.cj) + 8 * slot), 0, SC1);
}
template <int PRE = 0> __device__ __forceinline__ void poll64(const Ctx& c, u32 voff) {
  if (PRE) __builtin_amdgcn_s_sleep(PRE);
#pragma unroll 1
  for (int s = 0; s < SPIN_CAP; ++s) {
    asm volatile("" ::: "memory");
    const u32x2 v = __builtin_amdgcn_raw_buffer_load_b64(c.rws, (int)voff, 0, SC1);
    if (__all(v[0] == c.tag_lo && v[1] == c.tag_hi)) break;
    __builtin_amdgcn_s_sleep(1);
  }
  asm volatile("" ::: "memory");
}
__device__ __forceinline__ void publish_last(const Ctx& c, int kind, int idx, u32 n) {
  MK_WAITV(0);
  if (c.lane == 0) {
    u32 old;
    asm volatile("ds_add_rtn_u32 %0, %1, %2\n\ts_waitcnt lgkmcnt(0)" : "=&v"(old) : "v"(c.lds + T_CNT + 4 * idx), "v"(1u) : "memory");
    if (old == n - 1) __builtin_amdgcn_raw_buffer_store_b64((u32x2){c.tag_lo, c.tag_hi}, c.rws, (int)flag_off(kind, c.rp, c.cj), 0, SC1);
  }
}
template <int PRE> __device__ __forceinline__ void await_panel(const Ctx& c, int kind, int idx, u32 n) {
#pragma unroll 1
  for (int s = 0; s < (1 << 22); ++s) {
    if (__builtin_amdgcn_readfirstlane(__hip_atomic_load((LAS u32*)(size_t)(c.lds + T_CNT + 4 * idx), __ATOMIC_RELAXED, __HIP_MEMORY_SCOPE_WORKGROUP)) >= n) break;
    __builtin_amdgcn_s_sleep(1);
  }
  poll64<PRE>(c, flag_off(kind, 0, 0) + (u32)((c.rp * 16 + (c.lane & 15)) * 128));
}
__device__ __forceinline__ u32x2 poll_issue(const Ctx& c, u32 voff) { return __builtin_amdgcn_raw_buffer_load_b64(c.rws, (int)voff, 0, SC1); }
__device__ __forceinline__ void poll_check(const Ctx& c, u32 voff, u32x2 v) {
  if (!__all(v[0] == c.tag_lo && v[1] == c.tag_hi)) poll64(c, voff);
  asm volatile("" ::: "memory");
}
__device__ __forceinline__ u32x4 tag_pair(const Ctx& c, float a, float b) {
  return (u32x4){asu(a), c.tag_lo ^ asu(b), asu(b), ~c.tag_lo ^ asu(a)};
}
__device__ __forceinline__ u32 tag_bad(const Ctx& c, u32x4 v) { return (v[1] ^ v[2] ^ c.tag_lo) | (v[3] ^ v[0] ^ ~c.tag_lo); }
template <int AUX = 0> __device__ __forceinline__ void touch(const Ctx& c, rsrc_t r, u32 voff, u32 soff) {
  __builtin_amdgcn_raw_ptr_buffer_load_lds(r, (lds_vp)(size_t)(c.lds + T_DUMMY), 4, (int)voff, (int)soff, 0, AUX);
}
__device__ __forceinline__ void touch_code(const Ctx& c) {
  {
    u64 pc0; u32 back, e0, e1, e2;
    asm volatile("s_getpc_b64 %0\n.Lmk_pc%=:\n\ts_mov_b32 %1, .Lmk_pc%=-.Lmk_code_begin\n\ts_mov_b32 %2, .Lmk_end_c-.Lmk_code_begin\n\t"
                 "s_mov_b32 %3, .Lmk_end_l-.Lmk_code_begin\n\ts_mov_b32 %4, .Lmk_end_w-.Lmk_code_begin" : "=s"(pc0), "=s"(back), "=s"(e0), "=s"(e1), "=s"(e2));
    const rsrc_t rc = mk_rsrc((const void*)((pc0 - back) & ~127ull), max(e0, max(e1, e2)));
    if (c.lane < 24) touch(c, rc, (u32)(((blockIdx.x >> 3) + 32 * c.lane) * 128), 0);
  }
}

struct Win { u32 b[3]; };
__device__ __forceinline__ Win mk_win(u32 base) { Win w; w.b[0] = opaque(base); w.b[1] = opaque(base + 65536u); w.b[2] = opaque(base + 131072u); return w; }
template <int OFF> __device__ __forceinline__ u32 wad(const Win& w) { return w.b[OFF >> 16] + (u32)(OFF & 0xffff); }

template <int KS, int MT, int NT> struct Set { bf16x8 a[KS][MT]; bf16x8 b[KS][NT]; bf16x4 t[KS][NT]; };

__device__ __forceinline__ void mfma_acc(f32x4& c, const bf16x8& a, const bf16x8& b) {
  asm volatile("v_mfma_f32_16x16x32_bf16 %0, %1, %2, %0" : "+v"(c) : "v"(a), "v"(b));
}
__device__ __forceinline__ void mfma_in() { MK_SB(); asm volatile("s_nop 1"); MK_SB(); }
__device__ __forceinline__ void mfma_out() { MK_SB(); asm volatile("s_nop 7\n\ts_nop 1"); MK_SB(); }
template <int KS, int MT, int NT, bool SWAP, int NRD, class GAP, class RD>
__device__ __forceinline__ void part(f32x4 (&acc)[MT][NT], const Set<KS, MT, NT>& X, GAP, RD&& rd) {
  constexpr int NMF = KS * MT * NT;
  MK_SB();
  sfor<NMF>([&](auto ic) {
    constexpr int i = decltype(ic)::v, ks = i / (MT * NT), m = (i / NT) % MT, n = i % NT;
    if constexpr (SWAP) mfma_acc(acc[m][n], X.b[ks][n], X.a[ks][m]); else mfma_acc(acc[m][n], X.a[ks][m], X.b[ks][n]);
    MK_SB();
    sfor<NRD>([&](auto kc) { if constexpr (GAP::gap(decltype(kc)::v) == i) rd(kc); });
    if constexpr (i == NMF - 2) MK_LGKM0();
    MK_SB();
  });
}
struct Gap1 { static constexpr int gap(int k) { return k; } };
struct GapG2 { static constexpr int gap(int k) { return k < 2 ? k : (k < 10 ? 2 + (k - 2) / 2 : (k < 12 ? k - 4 : 8 + (k - 12) / 2)); } };

struct AdP1 { Win a[2], a0[2], b[3]; };
template <int ST, int KSI, int K> __device__ __forceinline__ void rd_p1(const AdP1& ad, Set<1, 9, 3>& Y) {
  constexpr int SB = ST * P1_ST;
  if constexpr (K == 0) Y.a[0][0] = lds_ld<bf16x8>(wad<SB>(ad.a0[KSI]));
  else if constexpr (K <= 6) {
    constexpr int n = (K - 1) >> 1, h = (K - 1) & 1;
    const bf16x4 v = lds_tr(wad<SB + P1_A + 4096 * KSI + 512 * h>(ad.b[n]));
    if constexpr (h == 0) Y.t[0][n] = v; else Y.b[0][n] = __builtin_shufflevector(Y.t[0][n], v, 0, 1, 2, 3, 4, 5, 6, 7);
  } else { constexpr int m = K - 6; Y.a[0][m] = lds_ld<bf16x8>(wad<SB + m * 2048>(ad.a[KSI])); }
}
struct AdG3 { Win af, ar[2], b; };
template <int T, int KSI, int K> __device__ __forceinline__ void rd_g3(const AdG3& ad, Set<1, 2, 7>& Y) {
  constexpr int SA = g3_aslot(T) * 16384, SB = G3_B + (T % 4) * 14336;
  if constexpr (K == 0 || K == 8) {
    constexpr int m = K == 0 ? 0 : 1;
    if constexpr (T >= 32) Y.a[0][m] = lds_ld<bf16x8>(wad<SA + m * 2048>(ad.ar[KSI]));
    else Y.a[0][m] = lds_ld<bf16x8>(wad<SA + KSI * 8192 + m * 1024>(ad.af));
  } else { constexpr int n = K - 1; Y.b[0][n] = lds_ld<bf16x8>(wad<SB + (KSI * 7 + n) * 1024>(ad.b)); }
}
struct AdG2 { Win a, b[4]; };
template <int T, int K> __device__ __forceinline__ void rd_g2(const AdG2& ad, Set<2, 2, 4>& Y) {
  constexpr int ks = K / 10, k = K % 10;
  if constexpr (k < 2) Y.a[ks][k] = lds_ld<bf16x8>(wad<G2_A + (T % 4) * 16384 + ks * 8192 + k * 1024>(ad.a));
  else {
    constexpr int n = (k - 2) >> 1, h = (k - 2) & 1;
    const bf16x4 v = lds_tr(wad<G2_B + (T % G2_NB) * 8192 + 4096 * ks + 512 * h>(ad.b[n]));
    if constexpr (h == 0) Y.t[ks][n] = v; else Y.b[ks][n] = __builtin_shufflevector(Y.t[ks][n], v, 0, 1, 2, 3, 4, 5, 6, 7);
  }
}
__device__ __forceinline__ u32 tr64_lane(const Ctx& c, int strip) {
  const int f = (c.q >> 1) | ((c.g & 1) << 1);
  return (u32)((strip >> 2) * 8192 + 128 * (8 * c.g + c.q) + 16 * ((2 * (strip & 3) + (c.p >> 1)) ^ (2 * f)) + 8 * (c.p & 1));
}

template <int ST, bool LAST> __device__ __forceinline__ void tile_p1(f32x4 (&acc)[9][3], Set<1, 9, 3>& X, Set<1, 9, 3>& Y, const AdP1& ad) {
  part<1, 9, 3, true, 15>(acc, X, Gap1{}, [&](auto kc) { rd_p1<ST, 1, decltype(kc)::v>(ad, Y); });
  bar_c();
  part<1, 9, 3, true, LAST ? 0 : 15>(acc, Y, Gap1{}, [&](auto kc) { rd_p1<(ST + 1) % 3, 0, decltype(kc)::v>(ad, X); });
}
template <int T, class H>
__device__ __forceinline__ void tile_g3(f32x4 (&acc)[2][7], Set<1, 2, 7>& X, Set<1, 2, 7>& Y, const AdG3& ad, H&& hook) {
  part<1, 2, 7, (T >= 16), 9>(acc, X, Gap1{}, [&](auto kc) { rd_g3<T, 1, decltype(kc)::v>(ad, Y); });
  hook();
  MK_SB();
  bar_c();
  part<1, 2, 7, (T >= 16), T < 63 ? 9 : 0>(acc, Y, Gap1{}, [&](auto kc) { rd_g3<T + 1, 0, decltype(kc)::v>(ad, X); });
}
template <int NT, bool LASTBAR> __device__ __forceinline__ void gemm_g2(f32x4 (&acc)[2][4], const Ctx& c) {
  AdG2 ad;
  ad.a = mk_win(c.lds + (u32)(c.wid * 2048 + c.lane * 16));
#pragma unroll
  for (int n = 0; n < 4; ++n) ad.b[n] = mk_win(c.lds + tr64_lane(c, n));
  Set<2, 2, 4> X, Y;
  bar_c();
  sfor<20>([&](auto kc) { rd_g2<0, decltype(kc)::v>(ad, X); });
  MK_LGKM0();
  sfor<NT>([&](auto tc) {
    constexpr int t = decltype(tc)::v;
    if constexpr (t + 1 < NT || LASTBAR) bar_c();
    part<2, 2, 4, true, (t + 1 < NT) ? 20 : 0>(acc, (t & 1) ? Y : X, GapG2{}, [&](auto kc) { rd_g2<t + 1, decltype(kc)::v>(ad, (t & 1) ? X : Y); });
  });
  mfma_out();
}

struct AtPre { u32 q, k0, k1, v0, v1, o[2]; u64 ok[8]; };
__device__ __forceinline__ AtPre attention_pre(const Ctx& c) {
  const int hh = c.wid & 1, T = c.wid >> 1;
  const int lo = (c.rp == 0 && T == 0) ? 8 : 0;
  const u32 bh = c.lds + AT_BASE + (u32)(hh * 2 * AT_BLK);
  AtPre a;
  a.q = bh + (u32)((8 + 16 * T + c.r16) * AT_RS + 16 * c.g);
  a.k0 = bh + (u32)(4 * AT_BLK + (16 * T + c.r16) * AT_RS + 16 * c.g);
  a.k1 = bh + (u32)(4 * AT_BLK + min(16 * T + 16 + c.r16, 71) * AT_RS + 16 * c.g);
  const int v0 = 16 * T + 4 * c.g + c.q;
  a.v0 = bh + (u32)(8 * AT_BLK + v0 * AT_RS + 8 * c.p);
  a.v1 = bh + (u32)(8 * AT_BLK + min(v0 + 16, 71) * AT_RS + 8 * c.p);
  const int G = ((c.g & 1) << 1) | (c.g >> 1);
#pragma unroll
  for (int par = 0; par < 2; ++par) a.o[par] = WS_O + (u32)(c.rp * 32 + 2 * c.cj + hh) * GA + frag_off(c, 2 * T + par, 16 * G + c.r16);
#pragma unroll
  for (int j = 0; j < 8; ++j) {
    const int ak = 16 * (j >> 2) + 4 * c.g + (j & 3), w = 8 + c.r16 - ak;
    a.ok[j] = __ballot((w >= 0) && (w <= 7) && (ak >= lo));
  }
  asm volatile("" : "+v"(a.q), "+v"(a.k0), "+v"(a.k1), "+v"(a.v0), "+v"(a.v1), "+v"(a.o[0]), "+v"(a.o[1]));
  asm volatile("" : "+s"(a.ok[0]), "+s"(a.ok[1]), "+s"(a.ok[2]), "+s"(a.ok[3]), "+s"(a.ok[4]), "+s"(a.ok[5]), "+s"(a.ok[6]), "+s"(a.ok[7]));
  return a;
}
__device__ __forceinline__ float vmax(float a, float b) { return __builtin_elementwise_maximum(a, b); }
__device__ __forceinline__ float vmax3(float a, float b, float d) { return vmax(vmax(a, b), d); }
__device__ __forceinline__ float vsel(u64 m, float a, float b) { return __builtin_amdgcn_inverse_ballot_w64(m) ? a : b; }
__device__ __forceinline__ void attention(const Ctx& c, const AtPre& a) {
  const float sc = 0.17677669529663687f * 1.4426950408889634f;
  const f32x4 zero = {0.f, 0.f, 0.f, 0.f};
  bf16x8 qf[2], kf[2][2], vf[2][2];
#pragma unroll
  for (int par = 0; par < 2; ++par) {
    kf[par][0] = lds_ld<bf16x8>(a.k0 + par * AT_BLK); qf[par] = lds_ld<bf16x8>(a.q + par * AT_BLK); kf[par][1] = lds_ld<bf16x8>(a.k1 + par * AT_BLK);
  }
  bf16x4 r[8];
  asm volatile("ds_read_b64_tr_b16 %0, %8\n\tds_read_b64_tr_b16 %1, %9\n\tds_read_b64_tr_b16 %2, %8 offset:32\n\tds_read_b64_tr_b16 %3, %9 offset:32\n\t"
               "ds_read_b64_tr_b16 %4, %8 offset:%10\n\tds_read_b64_tr_b16 %5, %9 offset:%10\n\tds_read_b64_tr_b16 %6, %8 offset:%11\n\tds_read_b64_tr_b16 %7, %9 offset:%11"
               : "=&v"(r[0]), "=&v"(r[1]), "=&v"(r[2]), "=&v"(r[3]), "=&v"(r[4]), "=&v"(r[5]), "=&v"(r[6]), "=&v"(r[7])
               : "v"(a.v0), "v"(a.v1), "i"(AT_BLK), "i"(AT_BLK + 32));
  MK_SB();
  f32x4 s[2][2];
#pragma unroll
  for (int par = 0; par < 2; ++par)
#pragma unroll
    for (int k2 = 0; k2 < 2; ++k2) s[par][k2] = __builtin_amdgcn_mfma_f32_16x16x32_bf16(kf[par][k2], qf[par], zero, 0, 0, 0);
  float e[2][8], mx[2];
  const float ninf = -3.0e38f;
#pragma unroll
  for (int par = 0; par < 2; ++par) {
#pragma unroll
    for (int j = 0; j < 8; ++j) { const float t = s[par][j >> 2][j & 3]; e[par][j] = vsel(a.ok[j], t, ninf); }
    mx[par] = vmax3(vmax3(e[par][0], e[par][1], e[par][2]), vmax3(e[par][3], e[par][4], e[par][5]), vmax(e[par][6], e[par][7]));
  }
  {
    auto p = __builtin_amdgcn_permlane16_swap(asu(mx[0]), asu(mx[1]), false, false);
    const float m = vmax(asf((u32)p[0]), asf((u32)p[1]));
    auto q = __builtin_amdgcn_permlane32_swap(asu(m), asu(m), false, false);
    const float t = vmax(asf((u32)q[0]), asf((u32)q[1]));
    auto u = __builtin_amdgcn_permlane16_swap(asu(t), asu(t), false, false);
    mx[0] = asf((u32)u[0]) * -sc; mx[1] = asf((u32)u[1]) * -sc;
  }
  asm volatile("s_waitcnt lgkmcnt(0)" : "+v"(r[0]), "+v"(r[1]), "+v"(r[2]), "+v"(r[3]), "+v"(r[4]), "+v"(r[5]), "+v"(r[6]), "+v"(r[7]));
  const bf16x8 ones = __builtin_bit_cast(bf16x8, (u32x4){0x3f803f80u, 0x3f803f80u, 0x3f803f80u, 0x3f803f80u});
  f32x4 o[2][2], sum[2];
#pragma unroll
  for (int par = 0; par < 2; ++par) {
    vf[par][0] = __builtin_shufflevector(r[4 * par], r[4 * par + 1], 0, 1, 2, 3, 4, 5, 6, 7);
    vf[par][1] = __builtin_shufflevector(r[4 * par + 2], r[4 * par + 3], 0, 1, 2, 3, 4, 5, 6, 7);
#pragma unroll
    for (int j = 0; j < 8; ++j) e[par][j] = __builtin_amdgcn_exp2f(__builtin_fmaf(e[par][j], sc, mx[par]));
    const bf16x8 pf = __builtin_bit_cast(bf16x8, (u32x4){pk_bf16(e[par][0], e[par][1]), pk_bf16(e[par][2], e[par][3]), pk_bf16(e[par][4], e[par][5]), pk_bf16(e[par][6], e[par][7])});
    sum[par] = __builtin_amdgcn_mfma_f32_16x16x32_bf16(ones, pf, zero, 0, 0, 0);
    o[par][0] = __builtin_amdgcn_mfma_f32_16x16x32_bf16(vf[par][0], pf, zero, 0, 0, 0);
    o[par][1] = __builtin_amdgcn_mfma_f32_16x16x32_bf16(vf[par][1], pf, zero, 0, 0, 0);
  }
#pragma unroll
  for (int par = 0; par < 2; ++par) {
    const float inv = __builtin_amdgcn_rcpf(sum[par][0]);
    const f32x4 o0 = o[par][0] * inv, o1 = o[par][1] * inv;
    st128<SC1>(pair_swap((u32x2){pk_bf16(o0[0], o0[1]), pk_bf16(o0[2], o0[3])}, (u32x2){pk_bf16(o1[0], o1[1]), pk_bf16(o1[2], o1[3])}),
                                           c.rws, (int)a.o[par], 0);
  }
  publish_last(c, FO, 0, 8);
}

__device__ __forceinline__ float red8(const float (&v)[8], int r16) {
  const bool b2 = r16 & 4, b1 = r16 & 2, b0 = r16 & 1;
  float u[4], t[2];
#pragma unroll
  for (int i = 0; i < 4; ++i) u[i] = (b2 ? v[4 + i] : v[i]) + dpp<0x141>(b2 ? v[i] : v[4 + i]);
#pragma unroll
  for (int i = 0; i < 2; ++i) t[i] = (b1 ? u[2 + i] : u[i]) + dpp<0x4E>(b1 ? u[i] : u[2 + i]);
  const float x = (b0 ? t[1] : t[0]) + dpp<0xB1>(b0 ? t[0] : t[1]);
  return x + dpp<0x128>(x);
}

__device__ __forceinline__ void consumer(const Params& P, Ctx& c) {
  refresh(c);
  const int w = c.wid;
  const rsrc_t rx = mk_rsrc(P.x, 2048u * 2048u);
  const AtPre ap = attention_pre(c);
  {
    if (c.lane < 12) lds_st<u32>(c.lds + (u32)((c.lane >> 2) * P1_ST + P1_ONES + 4 * (c.lane & 3)), 0x3f803f80u);
    AdP1 ad;
#pragma unroll
    for (int ks = 0; ks < 2; ++ks) {
      const u32 a = (u32)(c.r16 * 128 + (((4 * ks + c.g) ^ (c.r16 >> 1)) * 16));
      ad.a[ks] = mk_win(c.lds + a);
      ad.a0[ks] = mk_win(c.lds + (c.r16 == 0 ? (u32)P1_ONES : a));
    }
#pragma unroll
    for (int n = 0; n < 3; ++n) ad.b[n] = mk_win(c.lds + tr64_lane(c, 3 * w + n));
    f32x4 acc[9][3];
#pragma unroll
    for (int m = 0; m < 9; ++m)
#pragma unroll
      for (int n = 0; n < 3; ++n) { acc[m][n] = (f32x4){0.f, 0.f, 0.f, 0.f}; asm volatile("" : "+v"(acc[m][n])); }
    u32 s0[3], s1[3];
#pragma unroll
    for (int n = 0; n < 3; ++n) {
      const int S = 3 * w + n;
      const u32 sb = c.lds + AT_BASE + (u32)((S >> 1) * 2 * AT_BLK + (S & 1) * 32 + 8 * c.g);
      s0[n] = sb + (u32)((c.r16 >> 3) * AT_BLK + (c.r16 & 7) * AT_RS); s1[n] = sb + (u32)((8 + c.r16) * AT_RS);
      asm volatile("" : "+v"(s0[n]), "+v"(s1[n]));
    }
    Set<1, 9, 3> X, Y;
    MK_LGKM0();
    bar_c();
    sfor<15>([&](auto kc) { rd_p1<0, 0, decltype(kc)::v>(ad, X); });
    MK_LGKM0();
#pragma unroll 1
    for (int it = 0; it < 5; ++it) { tile_p1<0, false>(acc, X, Y, ad); tile_p1<1, false>(acc, X, Y, ad); tile_p1<2, false>(acc, X, Y, ad); }
    part<1, 9, 3, true, 15>(acc, X, Gap1{}, [&](auto kc) { rd_p1<0, 1, decltype(kc)::v>(ad, Y); });
    bar_c();
    refresh(c);
    f32x2 xs[9];
    const u32 xa = opaque(c.lds + T_XST + (u32)(c.r16 * 8));
    part<1, 9, 3, true, 9>(acc, Y, Gap1{}, [&](auto kc) { constexpr int m = decltype(kc)::v; xs[m] = lds_ld<f32x2>(xa + m * 128); });
    mfma_out();
#pragma unroll
    for (int n = 0; n < 3; ++n) {
      f32x4 cs;
#pragma unroll
      for (int r = 0; r < 4; ++r) cs[r] = dpp<0x150>(acc[0][n][r]);
#pragma unroll
      for (int m = 0; m < 9; ++m) {
        const f32x4 v = (acc[m][n] - xs[m][0] * cs) * xs[m][1];
        lds_st<u32x2>(m == 0 ? s0[n] : s1[n] + (u32)(((m - 1) & 1) * AT_BLK + ((m - 1) >> 1) * 16 * AT_RS), (u32x2){pk_bf16(v[0], v[1]), pk_bf16(v[2], v[3])});
      }
    }
  }
  MK_LGKM0();
  bar_c();
  get_tag(c);
  int G = ((c.g & 1) << 1) | (c.g >> 1);
  u32x4 xr[2][2];
#pragma unroll
  for (int m = 0; m < 2; ++m)
#pragma unroll
    for (int pr = 0; pr < 2; ++pr)
      xr[m][pr] = __builtin_amdgcn_raw_buffer_load_b128(rx, (128 * c.rp + 32 * w + 2 * c.r16 + m) * 2048 + (64 * c.cj + 32 * pr + 8 * G) * 2, 0, 0);
  const u32 bspw = *(const u32*)(P.bsp + 128 * c.rp + 32 * w + 2 * c.r16);
  attention(c, ap);
  bar_c();
  u32x2 hb[2][4];
  {
    f32x4 acc[2][4];
#pragma unroll
    for (int m = 0; m < 2; ++m)
#pragma unroll
      for (int pr = 0; pr < 2; ++pr) {
        const u32x4 t = pair_swap((u32x2){xr[m][pr][0], xr[m][pr][1]}, (u32x2){xr[m][pr][2], xr[m][pr][3]});
        acc[m][2 * pr] = (f32x4){bf_lo(t[0]), bf_hi(t[0]), bf_lo(t[1]), bf_hi(t[1])};
        acc[m][2 * pr + 1] = (f32x4){bf_lo(t[2]), bf_hi(t[2]), bf_lo(t[3]), bf_hi(t[3])};
        asm volatile("" : "+v"(acc[m][2 * pr]), "+v"(acc[m][2 * pr + 1]));
      }
    u32 ho[2];
#pragma unroll
    for (int m = 0; m < 2; ++m) { ho[m] = WS_H + (u32)(c.rp * 32 + 2 * c.cj) * GA + frag_off(c, 2 * w + m, 16 * G + c.r16); asm volatile("" : "+v"(ho[m])); }
    bar_c();
    gemm_g2<16, true>(acc, c);
#pragma unroll
    for (int m = 0; m < 2; ++m) {
#pragma unroll
      for (int n = 0; n < 4; ++n) hb[m][n] = (u32x2){pk_bf16(acc[m][n][0], acc[m][n][1]), pk_bf16(acc[m][n][2], acc[m][n][3])};
#pragma unroll
      for (int pr = 0; pr < 2; ++pr) st128<SC1>(pair_swap(hb[m][2 * pr], hb[m][2 * pr + 1]), c.rws, (int)ho[m], pr * GA);
    }
    bar_c();
    float s[2], q[2];
#pragma unroll
    for (int m = 0; m < 2; ++m) {
      f32x2 s2 = {0.f, 0.f}, q2 = {0.f, 0.f};
#pragma unroll
      for (int n = 0; n < 4; ++n)
#pragma unroll
        for (int d = 0; d < 2; ++d) { const f32x2 a = {bf_lo(hb[m][n][d]), bf_hi(hb[m][n][d])}; s2 += a; q2 += a * a; }
      s[m] = sumg(s2[0] + s2[1]); q[m] = sumg(q2[0] + q2[1]);
    }
    float sv = c.g ? s[1] : s[0], qv = c.g ? q[1] : q[0];
    asm volatile("" : "+v"(sv), "+v"(qv));
    publish_last(c, FHH, 1, 4);
    if (c.g < 2) __builtin_amdgcn_raw_buffer_store_b64((u32x2){asu(sv), asu(qv)}, c.rws,
        (int)(WS_HSTAT + (u32)(((c.rp * 16 + c.cj) * 128 + 32 * w + c.lane) * 8)), 0, SC1);
  }
  {
    AdG3 ad;
    ad.af = mk_win(c.lds + (u32)(w * 2048 + c.lane * 16));
#pragma unroll
    for (int ks = 0; ks < 2; ++ks) ad.ar[ks] = mk_win(c.lds + (u32)((32 * w + c.r16) * 128 + (((4 * ks + c.g) ^ (c.r16 >> 1)) * 16)));
    ad.b = mk_win(c.lds + (u32)(c.lane * 16));
    float bsp[2] = {bf_lo(bspw), bf_hi(bspw)};
    f32x4 acc[2][7];
#pragma unroll
    for (int m = 0; m < 2; ++m)
#pragma unroll
      for (int n = 0; n < 7; ++n) { acc[m][n] = (f32x4){0.f, 0.f, 0.f, 0.f}; asm volatile("" : "+v"(acc[m][n])); }
    asm volatile("" : "+v"(bsp[0]), "+v"(bsp[1]));
    const u32 tg = opaque(c.lds + TAB + (u32)((32 * w + 4 * c.g) * 4));
    const u32 tr = opaque(c.lds + TAB + (u32)((32 * w + c.r16) * 4));
    Set<1, 2, 7> X, Y;
    bar_c();
    bar_c();
    sfor<9>([&](auto kc) { rd_g3<0, 0, decltype(kc)::v>(ad, X); });
    MK_LGKM0();
    sfor<16>([&](auto tc) { tile_g3<decltype(tc)::v>(acc, X, Y, ad, [&] { if constexpr (decltype(tc)::v == 3) MK_WAITV(0); }); });
    f32x4 hm[2], hr[2], c1[7];
    float c2[7], nm[2];
#pragma unroll
    for (int m = 0; m < 2; ++m) {
      hm[m] = lds_ld<f32x4>(tg + (T_HM - TAB) + 64 * m); hr[m] = lds_ld<f32x4>(tg + (T_HR - TAB) + 64 * m);
      nm[m] = lds_ld<float>(tr + (T_HM - TAB) + 64 * m);
    }
#pragma unroll
    for (int n = 0; n < 7; ++n) c2[n] = lds_ld<float>(c.lds + T_CS + (u32)((16 * n + c.r16) * 4));
    mfma_out();
    f32x4 z2[2][7];
    {
      f32x4 sm[2] = {{0.f, 0.f, 0.f, 0.f}, {0.f, 0.f, 0.f, 0.f}}, sq[2] = {{0.f, 0.f, 0.f, 0.f}, {0.f, 0.f, 0.f, 0.f}}, ga[2], gb[2];
#pragma unroll
      for (int m = 0; m < 2; ++m) { ga[m] = hr[m] * -2.30220820f; gb[m] = hr[m] * hr[m] * hr[m] * -0.10294324f; }
#pragma unroll
      for (int n = 0; n < 7; ++n) {
#pragma unroll
        for (int m = 0; m < 2; ++m) {
          const f32x4 t = acc[m][n] - hm[m] * c2[n];
          f32x4 y = t * (t * t * gb[m] + ga[m]);
          asm("" : "+v"(y));
          f32x4 e;
#pragma unroll
          for (int i = 0; i < 4; ++i) e[i] = __builtin_amdgcn_exp2f(y[i]);
          e += 1.0f;
#pragma unroll
          for (int i = 0; i < 4; ++i) e[i] = __builtin_amdgcn_rcpf(e[i]);
          const f32x4 z = t * e;
          z2[m][n] = z; sm[m] += z; sq[m] += z * z;
        }
      }
#pragma unroll
      for (int m = 0; m < 2; ++m) { sm[m] *= hr[m]; sq[m] *= hr[m] * hr[m]; }
      MK_SB();
      const u32 ca = c.lds + T_CS + (u32)((112 + 4 * c.g) * 4);
#pragma unroll
      for (int n = 0; n < 7; ++n) c1[n] = lds_ld<f32x4>(ca + 64 * n);
      const float sv[8] = {sm[0][0], sm[0][1], sm[0][2], sm[0][3], sm[1][0], sm[1][1], sm[1][2], sm[1][3]};
      const float qv[8] = {sq[0][0], sq[0][1], sq[0][2], sq[0][3], sq[1][0], sq[1][1], sq[1][2], sq[1][3]};
      const float s = red8(sv, c.r16), q = red8(qv, c.r16);
      if (c.r16 < 8) st128<SC1>(tag_pair(c, s, q), c.rws,
          (int)(WS_ZSTAT + (u32)(((c.rp * 16 + c.cj) * 128 + 32 * w + 16 * (c.r16 >> 2) + 4 * c.g + (c.r16 & 3)) * 16)), 0);
#pragma unroll
      for (int m = 0; m < 2; ++m)
#pragma unroll
        for (int n = 0; n < 7; ++n) { acc[m][n] = c1[n] * -nm[m]; asm volatile("" : "+v"(acc[m][n])); }
    }
    mfma_in();
    sfor<16>([&](auto tc) {
      constexpr int t = 16 + decltype(tc)::v;
      tile_g3<t>(acc, X, Y, ad, [&] { if constexpr (t == FLAG_T) MK_WAITV(0); });
      if constexpr (t == NORM_T) {
        refresh(c);
        f32x4 zr[2], zo[2];
#pragma unroll
        for (int m = 0; m < 2; ++m) {
          zr[m] = lds_ld<f32x4>(tg + (T_ZR - TAB) + 64 * m) * lds_ld<f32x4>(tg + (T_HR - TAB) + 64 * m);
          zo[m] = lds_ld<f32x4>(tg + (T_ZM - TAB) + 64 * m);
        }
#pragma unroll
        for (int n = 0; n < 7; ++n) {
          const f32x4 a = z2[0][n] * zr[0] + zo[0], b = z2[1][n] * zr[1] + zo[1];
          const u32x4 zf = {pk_bf16(a[0], b[0]), pk_bf16(a[1], b[1]), pk_bf16(a[2], b[2]), pk_bf16(a[3], b[3])};
          st128<SC1>(zf, c.rws, (int)frag_off(c, n, c.lane), (int)(WS_Z2N + (u32)(c.cj * 64 + 4 * c.rp + w) * GB));
        }
      }
    });
    mfma_out();
    f32x4 z1[2][7];
    float hr1[2];
#pragma unroll
    for (int m = 0; m < 2; ++m) {
      hr1[m] = lds_ld<float>(tr + (T_HR - TAB) + 64 * m);
#pragma unroll
      for (int n = 0; n < 7; ++n) {
        z1[m][n] = acc[m][n];
        acc[m][n] = (f32x4){bsp[m], bsp[m], bsp[m], bsp[m]};
        asm volatile("" : "+v"(acc[m][n]));
      }
    }
    u32 up[2], us[2];
    mfma_in();
    sfor<32>([&](auto tc) {
      constexpr int i = decltype(tc)::v;
      tile_g3<32 + i>(acc, X, Y, ad, [&] {
        if constexpr (i < 28) {
          constexpr int m = i / 14, n = (i % 14) / 2, h = 2 * (i & 1);
          const f32x2 x = (f32x2){z1[m][n][h], z1[m][n][h + 1]} * hr1[m];
          f32x2 y = x * (x * x * -0.10294324f + -2.30220820f);
          asm("" : "+v"(y));
          f32x2 e = {__builtin_amdgcn_exp2f(y[0]), __builtin_amdgcn_exp2f(y[1])};
          e += 1.0f;
          f32x2 z = x * (f32x2){__builtin_amdgcn_rcpf(e[0]), __builtin_amdgcn_rcpf(e[1])};
          asm volatile("" : "+v"(z));
          z1[m][n][h] = z[0]; z1[m][n][h + 1] = z[1];
        } else if constexpr (i < 30) {
          constexpr int m = i - 28;
          refresh(c);
          up[m] = frag_off(c, 2 * w + m, c.lane);
          us[m] = frag_off(c, 2 * w + m, 16 * (2 * (c.cj & 1) + (c.g >> 1)) + c.r16) + (u32)(8 * (c.g & 1));
          asm volatile("" : "+v"(up[m]), "+v"(us[m]));
        }
      });
    });
    mfma_out();
    {
      const int odd = c.cj & 1, kp = (7 * c.cj + 1) >> 1, ksg = (7 * c.cj + (odd ? -1 : 6)) >> 1;
      const u32 gp = WS_U + (u32)(c.rp * 56 + kp) * GA, gs = WS_U + (u32)(c.rp * 56 + ksg) * GA;
#pragma unroll
      for (int m = 0; m < 2; ++m) {
        u32x2 uv[7];
#pragma unroll
        for (int n = 0; n < 7; ++n) {
          const f32x4 u = z1[m][n] * acc[m][n];
          uv[n] = (u32x2){pk_bf16(u[0], u[1]), pk_bf16(u[2], u[3])};
          if (n & 1) st128<SC1>((u32x4){uv[n - 1][0], uv[n - 1][1], uv[n][0], uv[n][1]}, c.rws, (int)up[m], (int)(gp + (n >> 1) * GA));
        }
        __builtin_amdgcn_raw_buffer_store_b64(uv[6], c.rws, (int)us[m], (int)gs, SC1);
      }
      bar_c();
      publish_last(c, FU, 2, 4);
    }
  }
  {
    refresh(c);
    G = ((c.g & 1) << 1) | (c.g >> 1);
    f32x4 acc[2][4];
#pragma unroll
    for (int m = 0; m < 2; ++m)
#pragma unroll
      for (int n = 0; n < 4; ++n) {
        acc[m][n] = (f32x4){bf_lo(hb[m][n][0]), bf_hi(hb[m][n][0]), bf_lo(hb[m][n][1]), bf_hi(hb[m][n][1])};
        asm volatile("" : "+v"(acc[m][n]));
      }
    bar_c();
    gemm_g2<28, false>(acc, c);
    const rsrc_t ro = mk_rsrc(P.out, 2048u * 2048u);
#pragma unroll
    for (int m = 0; m < 2; ++m)
#pragma unroll
      for (int pr = 0; pr < 2; ++pr) {
        const f32x4 a = acc[m][2 * pr], b = acc[m][2 * pr + 1];
        st128<SC1 | 1>(pair_swap((u32x2){pk_bf16(a[0], a[1]), pk_bf16(a[2], a[3])}, (u32x2){pk_bf16(b[0], b[1]), pk_bf16(b[2], b[3])}), ro,
            (128 * c.rp + 32 * w + 2 * c.r16 + m) * 2048 + (64 * c.cj + 32 * pr + 8 * G) * 2, 0);
      }
    if (c.rp == 0 && c.cj == 0 && w == 0 && c.lane == 0)
      __builtin_amdgcn_raw_buffer_store_b64((u32x2){c.tag_lo, c.tag_hi}, c.rws, (int)WS_EPOCH, 0, SC1);
  }
  asm volatile(".Lmk_end_c:");
}

template <int N, int AUX> __device__ __forceinline__ void issue_v(rsrc_t r, u32 dst, const u32 (&vo)[N], u32 so) {
  sfor<N>([&](auto ic) { constexpr int s = decltype(ic)::v; glds<(s & 3) * 1024, AUX>(r, dst + (s >> 2) * 4096, vo[s], so); });
}
template <int N, int AUX> __device__ __forceinline__ void issue_v2(rsrc_t r, u32 dst, const u32 (&vo)[N], u32 so) {
  sfor<N>([&](auto ic) { constexpr int s = decltype(ic)::v; glds<(s & 1) * 2048, AUX>(r, dst + (s >> 1) * 4096, vo[s], so); });
}
template <int N> __device__ __forceinline__ void issue_f(rsrc_t r, u32 dst, const u32 (&lp)[8], u32 so) {
  sfor<N>([&](auto ic) { constexpr int s = decltype(ic)::v; glds<(s & 3) * 1024, SC1>(r, dst + (s >> 2) * 4096, lp[s], so); });
}
template <int LO, int HI, int NA_, class FA, class FB> __device__ __forceinline__ void issue_rng(FA&& fa, FB&& fb) {
  sfor<HI - LO>([&](auto ic) { constexpr int s = LO + decltype(ic)::v; if constexpr (s < NA_) fa(IC<s>{}); else fb(IC<s - NA_>{}); });
}
struct Off { u32 da, db, sa, sb; };
template <int NT_, int NA_, int SPLIT, int W, class FA, class FB, class NX>
__device__ __forceinline__ void steady(int n, Off& o, FA&& fa, FB&& fb, NX&& next) {
  bar_n();
  issue_rng<0, SPLIT, NA_>([&](auto ic) { fa(ic, o); }, [&](auto ic) { fb(ic, o); });
#pragma unroll 1
  for (int i = 1; i < n; ++i) {
    Off q = next(o);
    asm volatile("" : "+s"(q.da), "+s"(q.db), "+s"(q.sa), "+s"(q.sb));
    issue_rng<SPLIT, NT_, NA_>([&](auto ic) { fa(ic, o); }, [&](auto ic) { fb(ic, o); });
    MK_WAITV(W);
    bar_n();
    issue_rng<0, SPLIT, NA_>([&](auto ic) { fa(ic, q); }, [&](auto ic) { fb(ic, q); });
    o = q;
  }
  issue_rng<SPLIT, NT_, NA_>([&](auto ic) { fa(ic, o); }, [&](auto ic) { fb(ic, o); });
  MK_WAITV(W);
}
template <int NT_, int NA_, int W, class FA, class FB, class NX>
__device__ __forceinline__ void steady2(int lw, int n, Off o, FA&& fa, FB&& fb, NX&& next) {
  if (lw == 0) steady<NT_, NA_, NT_ / 4, W>(n, o, fa, fb, next); else steady<NT_, NA_, NT_ - NT_ / 4, W>(n, o, fa, fb, next);
}
#define MK_PIN2(a, b) asm volatile("" : "+s"(a), "+s"(b))
__device__ __forceinline__ u32 tr64_src(int lane, int lw, int ldb_bytes) {
  const int f = ((lane >> 4) & 1) | (lw << 1);
  return (u32)((8 * lw + (lane >> 3)) * ldb_bytes + (((lane & 7) ^ (2 * f)) * 16));
}
template <int T, bool LASTBAR>
__device__ __forceinline__ u32x2 load_g2(const Ctx& c, int lw, const u32 (&lp)[8], u32 abase, rsrc_t rb, const u32 (&vb)[4], u32 fo) {
  constexpr u32 bstep = 64 * 2048;
  const u32 sa = abase + (u32)lw * GA, da = c.lds + G2_A + (u32)(lw * 8192), db = c.lds + G2_B + (u32)(lw * 1024);
  auto A = [&](int t) { issue_f<8>(c.rws, da + (u32)((t & 3) * 16384), lp, sa + (u32)t * 2 * GA); };
  auto B = [&](int t) { issue_v2<4, 0>(rb, db + (u32)((t % G2_NB) * 8192), vb, (u32)t * bstep); };
  A(0); A(1); MK_WAITV(8); bar_n();
  A(2); A(3); MK_WAITV(16); bar_n();
  A(4); B(11); MK_WAITV(20); bar_n();
  A(5); B(12);
  MK_WAITV(24);
  steady2<12, 8, 28>(lw, T - 13, Off{da + 2 * 16384, db + 2 * 8192, sa + 12 * GA, 13 * bstep},
      [&](auto ic, const Off& o) { constexpr int s = decltype(ic)::v; glds<(s & 3) * 1024, SC1>(c.rws, o.da + (s >> 2) * 4096, lp[s], o.sa); },
      [&](auto ic, const Off& o) { constexpr int s = decltype(ic)::v; glds<(s & 1) * 2048, 0>(rb, o.db + (s >> 1) * 4096, vb[s], o.sb); },
      [&](const Off& o) { return Off{o.da == da + 3 * 16384 ? da : o.da + 16384, o.db == db + (G2_NB - 1) * 8192 ? db : o.db + 8192, o.sa + 2 * GA, o.sb + bstep}; });
  bar_n(); A(T - 7); MK_WAITV(24);
  bar_n(); A(T - 6); MK_WAITV(20);
  bar_n(); A(T - 5); MK_WAITV(16);
  bar_n(); A(T - 4); MK_WAITV(16);
  bar_n(); A(T - 3); MK_WAITV(16);
  bar_n(); A(T - 2); MK_WAITV(16);
  bar_n(); A(T - 1); MK_WAITV(16);
  bar_n(); MK_WAITV(8);
  bar_n(); MK_WAITV(0);
  bar_n();
  u32x2 fv = {0, 0};
  if (LASTBAR) { fv = poll_issue(c, fo); bar_n(); }
  return fv;
}
__device__ __forceinline__ void pre_g2(const Ctx& c, int lw, rsrc_t rb, const u32 (&vb)[4], int t0, int t1) {
#pragma unroll 1
  for (int t = t0; t < t1; ++t) issue_v2<4, 0>(rb, c.lds + G2_B + (u32)(lw * 1024 + t * 8192), vb, (u32)(t * 64 * 2048));
}
__device__ __forceinline__ void src_g2(const Ctx& c, int lw, u32 (&vb)[4]) {
  const u32 b0 = tr64_src(c.lane, lw, 2048) + (u32)(128 * c.cj + 4096);
#pragma unroll
  for (int s = 0; s < 4; ++s) vb[s] = b0 + (u32)(s * 16 * 2048 - (s & 1) * 2048);
}

__device__ __forceinline__ void loader(const Params& P, Ctx& c) {
  const int lw = c.wid - 4, lane = c.lane;
  {
    const rsrc_t rx = mk_rsrc((const char*)P.x - 4096, 2048u * 2048u + 4096u);
    const rsrc_t rw = mk_rsrc((const char*)P.wqkv - 4096, 1024u * 6144u + 4096u);
    u32 va[9], vb[12];
    const u32 ch = (u32)((((lane & 7) ^ (4 * lw + (lane >> 4))) * 16) + 4096);
    va[0] = (u32)(max(128 * c.rp - 16 + 2 * (lane >> 3) + lw, 0) * 2048) + ch;
    const u32 a1 = (u32)((128 * c.rp + 16 * lw + 2 * (lane >> 3)) * 2048) + ch;
#pragma unroll
    for (int m = 1; m < 9; ++m) va[m] = a1 + (u32)((32 * ((m - 1) >> 1) + ((m - 1) & 1)) * 2048 - (m & 1) * 2048);
    const u32 b0 = tr64_src(lane, lw, 6144) + (u32)(128 * c.cj + 4096);
#pragma unroll
    for (int s = 0; s < 12; ++s) vb[s] = b0 + (u32)((s >> 2) * 2048 + (s & 3) * 16 * 6144 - (s & 1) * 2048);
    auto I = [&](int t, int st) {
      const u32 sb = c.lds + (u32)(st * P1_ST + lw * 1024);
      issue_v2<9, 0>(rx, sb, va, (u32)(t * 128));
      issue_v2<12, 0>(rw, sb + P1_A, vb, (u32)(t * 64 * 6144));
    };
    I(0, 0); MK_WAITV(0); bar_n();
    I(1, 1); I(2, 2);
    const u32 d0 = c.lds + (u32)(lw * 1024);
    MK_WAITV(21);
    steady2<21, 9, 21>(lw, 13, Off{d0, d0 + P1_A, 3 * 128, 3 * 64 * 6144},
        [&](auto ic, const Off& o) { constexpr int s = decltype(ic)::v; glds<(s & 1) * 2048, 0>(rx, o.da + (s >> 1) * 4096, va[s], o.sa); },
        [&](auto ic, const Off& o) { constexpr int s = decltype(ic)::v; glds<(s & 1) * 2048, 0>(rw, o.db + (s >> 1) * 4096, vb[s], o.sb); },
        [&](const Off& o) { const u32 d = o.da == d0 + 2 * P1_ST ? d0 : o.da + P1_ST; return Off{d, d + P1_A, o.sa + 128, o.sb + 64 * 6144}; });
  }
  const u32 fwo = flag_off(FW, 0, 0) + (u32)((((lane >> 1) & 15) * 16 + c.cj) * 128 + 8 * (lane & 1));
  u32x2 fwv;
  u32 lp[8];
#pragma unroll
  for (int f = 0; f < 8; ++f) lp[f] = frag_off(c, f, lane) - (u32)((f & 3) * 1024);
  {
    const rsrc_t rb = mk_rsrc((const char*)P.wo - 4096, 1024u * 2048u + 4096u);
    u32 vb[4];
    src_g2(c, lw, vb);
    static_assert(6 * 8192 >= P1_ST && G2_PRE * 8192 <= 2 * P1_ST, "B slots 6.. lie in the stage P1 frees first");
    bar_n();
    pre_g2(c, lw, rb, vb, 6, G2_PRE);
    MK_WAITV(4 * (G2_PRE - 6)); bar_n();
    bar_n();
    pre_g2(c, lw, rb, vb, 0, 6);
    get_tag(c);
    const AtPre ap = attention_pre(c);
    bar_h();
    attention(c, ap);
    bar_n();
    pre_g2(c, lw, rb, vb, G2_PRE, G2_NB);
    if (lw == 0) await_panel<9>(c, FO, 0, 8);
    bar_h();
    fwv = load_g2<16, true>(c, lw, lp, WS_O + (u32)(c.rp * 32) * GA, rb, vb, fwo);
  }
  {
    const rsrc_t rs = mk_rsrc((const char*)P.wsp - 4096, 2048u * 4096u + 4096u);
    const u32 bh = WS_H + (u32)(c.rp * 32 + lw) * GA, bw = WS_WINP + (u32)(c.cj * 64 + lw) * GB, bz = WS_Z2N + (u32)(c.cj * 64 + lw) * GB;
    const u32 da = c.lds + (u32)(lw * 8192), db = c.lds + (u32)(G3_B + lw * 7168);
    auto A3 = [&](int t) { issue_f<8>(c.rws, da + (u32)((t % 6) * 16384), lp, bh + (u32)(t < 16 ? t : t - 22) * 2 * GA); };
    auto B3 = [&](int t) { issue_f<7>(c.rws, db + (u32)((t & 3) * 14336), lp, bw + (u32)(t < 16 ? t : 16 + ((t + 10) & 15)) * 2 * GB); };
    u32 vs[8];
    const u32 s0 = (u32)((128 * c.rp + 16 * lw + 2 * (lane >> 3)) * 4096 + (((lane & 7) ^ (4 * lw + (lane >> 4))) * 16) + 4096);
#pragma unroll
    for (int m = 0; m < 8; ++m) vs[m] = s0 + (u32)((32 * (m >> 1) + (m & 1)) * 4096 - (m & 1) * 2048);
    bar_h();
    poll_check(c, fwo, fwv);
    B3(0); B3(1); B3(2); B3(3);
    bar_h();
    A3(0); A3(1); MK_WAITV(8); bar_n();
    A3(2); A3(3); MK_WAITV(16); bar_n();
    A3(4); A3(5); A3(6); B3(4); MK_WAITV(39);
    bar_n(); A3(7); B3(5);
    auto fF = [&](auto ic, const Off& o) { constexpr int s = decltype(ic)::v; glds<(s & 3) * 1024, SC1>(c.rws, o.da + (s >> 2) * 4096, lp[s], o.sa); };
    auto fB = [&](auto ic, const Off& o) { constexpr int s = decltype(ic)::v; glds<(s & 3) * 1024, SC1>(c.rws, o.db + (s >> 2) * 4096, lp[s], o.sb); };
    auto n3 = [&](const Off& o) { return Off{o.da == da + 5 * 16384 ? da : o.da + 16384, o.db == db + 3 * 14336 ? db : o.db + 14336, o.sa + 2 * GA, o.sb + 2 * GB}; };
    MK_WAITV(46);
    steady2<15, 8, 30>(lw, 8, Off{da + 2 * 16384, db + 2 * 14336, bh + 16 * GA, bw + 12 * GB}, fF, fB, n3);
    bar_n(); B3(14); MK_WAITV(22);
    bar_n(); B3(15); MK_WAITV(14);
    bar_n(); B3(16); MK_WAITV(14);
    bar_n(); B3(17); MK_WAITV(14);
    bar_n(); B3(18); MK_WAITV(14);
    bar_n(); B3(19); MK_WAITV(14);
    bar_n(); A3(22); B3(20); MK_WAITV(22);
    bar_n(); A3(23); B3(21);
    MK_WAITV(30);
    steady2<15, 8, 30>(lw, 8, Off{da, db + 2 * 14336, bh + 4 * GA, bw + 32 * GB}, fF, fB, n3);
    bar_n(); B3(30); MK_WAITV(22);
    bar_n(); B3(31); MK_WAITV(14);
    const u32 dr = c.lds + (u32)(lw * 1024);
    auto A4 = [&](int t) { issue_v2<8, 0>(rs, dr + (u32)(((t + 2) & 3) * 16384), vs, (u32)(t * 128)); };
    auto B4 = [&](int t) { issue_f<7>(c.rws, db + (u32)((t & 3) * 14336), lp, bz + (u32)t * 2 * GB); };
    bar_n(); A4(0); MK_WAITV(15);
    bar_n(); B4(0); A4(1); B4(1);
    MK_WAITV(30);
    steady2<15, 8, 30>(lw, 30, Off{dr, db + 2 * 14336, 2 * 128, bz + 4 * GB},
        [&](auto ic, const Off& o) { constexpr int s = decltype(ic)::v; glds<(s & 1) * 2048, 0>(rs, o.da + (s >> 1) * 4096, vs[s], o.sa); }, fB,
        [&](const Off& o) { return Off{o.da == dr + 3 * 16384 ? dr : o.da + 16384, o.db == db + 3 * 14336 ? db : o.db + 14336, o.sa + 128, o.sb + 2 * GB}; });
    bar_n();
    MK_WAITV(15); bar_n(); MK_WAITV(0); bar_n();
    bar_n();
  }
  {
    const rsrc_t rb = mk_rsrc((const char*)P.wout - 4096, 1792u * 2048u + 4096u);
    u32 vb[4];
    src_g2(c, lw, vb);
    bar_h();
    pre_g2(c, lw, rb, vb, 0, G2_NB);
    bar_h();
    load_g2<28, false>(c, lw, lp, WS_U + (u32)(c.rp * 56) * GA, rb, vb, 0);
  }
  asm volatile(".Lmk_end_l:");
}

__device__ __forceinline__ void bars(int n) {
#pragma unroll 1
  for (int i = 0; i < n; ++i) bar_n();
}
__device__ __forceinline__ void sumsq2(float& s, float& q, u32 u) {
  float a, b;
  asm("v_lshlrev_b32 %2, 16, %4\n\tv_and_b32 %3, 0xffff0000, %4\n\tv_add_f32 %0, %0, %2\n\tv_fmac_f32 %1, %2, %2\n\tv_add_f32 %0, %0, %3\n\tv_fmac_f32 %1, %3, %3"
      : "+v"(s), "+v"(q), "=&v"(a), "=&v"(b) : "v"(u));
}
__device__ __forceinline__ void warm_slow(const Params& P, const Ctx& c, int row0, int nrow) {
  const int cslow = (c.cj & 8) + ((3 - (int)((size_t)P.wqkv >> 7)) & 7), i = c.lane / 3, g = c.lane - 3 * i;
  if (32 * i < nrow) touch(c, mk_rsrc(P.wqkv, 1024u * 6144u), (u32)((row0 + (blockIdx.x >> 3) + 32 * i) * 6144 + g * 2048 + cslow * 128), 0);
}
__device__ __forceinline__ void warm_x(const Params& P, const Ctx& c, int h, u32x4 (&xr)[4]) {
  const int v = c.wid - 6, b = blockIdx.x;
  if (v == 1) {
    const rsrc_t rq = mk_rsrc(P.wqkv, 1024u * 6144u);
    const int r = c.lane / 48;
    touch(c, rq, (u32)(((b & 63) + 64 * (4 * (2 * h + r) + (b >> 6))) * 6144 + (c.lane - 48 * r) * 128), 0);
    if (c.lane < 32) touch(c, rq, (u32)(((b & 63) + 64 * (4 * (2 * h + 1) + (b >> 6))) * 6144 + (16 + c.lane) * 128), 0);
  }
  const rsrc_t rx = mk_rsrc(P.x, 2048u * 2048u);
#pragma unroll
  for (int i = 0; i < 4; ++i) xr[i] = __builtin_amdgcn_raw_buffer_load_b128(rx, (128 * c.rp + 8 * c.cj + 4 * v + i) * 2048 + c.lane * 16, h * 1024, 0);
}
__device__ __forceinline__ void worker(const Params& P, Ctx& c, u32x4 (&xr0)[4]) {
  refresh(c);
  const int v = c.wid - 6, lane = c.lane;
  const u32 stg = c.lds + WSTG + (u32)(v * 14336);
  u32x4 wr[14];
  {
    const int b = blockIdx.x;
    u32x4 xr1[4];
    const u32x2 ep = __builtin_amdgcn_raw_buffer_load_b64(c.rws, (int)WS_EPOCH, 0, SC1);
    bar_n();
    { const u64 t = (((u64)ep[1] << 32) | ep[0]) + 1;
      c.tag_lo = __builtin_amdgcn_readfirstlane((u32)t); c.tag_hi = __builtin_amdgcn_readfirstlane((u32)(t >> 32));
      if (v == 1 && lane < 5) lds_st<u32>(c.lds + T_TAG + (u32)(4 * lane), lane == 0 ? c.tag_lo : lane == 1 ? c.tag_hi : 0u); }
    bar_n();
    warm_x(P, c, 1, xr1);
    bars(4);
    float sm = 0.f, sr = 0.f;
#pragma unroll
    for (int i = 0; i < 4; ++i) {
      float s = 0.f, q = 0.f;
#pragma unroll
      for (int h = 0; h < 2; ++h)
#pragma unroll
        for (int d = 0; d < 4; ++d) { u32 u = h ? xr1[i][d] : xr0[i][d]; sumsq2(s, q, u); }
      const float mean = sumg(sum16(s)) * (1.0f / 1024.0f);
      const float rs = __builtin_amdgcn_rsqf(fmaxf(sumg(sum16(q)) * (1.0f / 1024.0f) - mean * mean, 0.f) + 1e-5f);
      sm = lane == i ? mean : sm; sr = lane == i ? rs : sr;
      asm volatile("" : "+v"(sm), "+v"(sr));
      if (i == 3) { if (lane < 4) st128<SC1>(tag_pair(c, sm, sr), c.rws, (int)(WS_XSTAT + (u32)((128 * c.rp + 8 * c.cj + 4 * v + lane) * 16)), 0); }
      if (i == 0 && v == 1) warm_slow(P, c, 448, 320);
      bar_n();
    }
    if (v == 0) warm_slow(P, c, 768, 256);
    else { touch(c, mk_rsrc(P.wo, 1024u * 2048u), (u32)(lane * 128), b * 8192u); touch_code(c); }
    bars(2);
    u32x4 xg[2];
    u32 xo[2];
#pragma unroll
    for (int i = 0; i < 2; ++i) {
      const int rho = min(72 * v + 64 * i + lane, 72 * v + 71), m = rho >> 4, sl = rho & 15;
      const int il = m == 0 ? (2 * (sl & 7) + (sl >> 3)) : 16 + 32 * ((m - 1) >> 1) + 2 * sl + ((m - 1) & 1);
      xo[i] = WS_XSTAT + (u32)(max(128 * c.rp - 16 + il, 0) * 16);
      xg[i] = __builtin_amdgcn_raw_buffer_load_b128(c.rws, (int)xo[i], 0, SC1);
    }
    bars(2);
    if (!__all((tag_bad(c, xg[0]) | tag_bad(c, xg[1])) == 0)) {
#pragma unroll 1
      for (int sp = 0; sp < SPIN_CAP; ++sp) {
        asm volatile("" ::: "memory");
#pragma unroll
        for (int i = 0; i < 2; ++i) xg[i] = __builtin_amdgcn_raw_buffer_load_b128(c.rws, (int)xo[i], 0, SC1);
        if (__all((tag_bad(c, xg[0]) | tag_bad(c, xg[1])) == 0)) break;
      }
    }
#pragma unroll
    for (int i = 0; i < 2; ++i) lds_st<u32x2>(c.lds + T_XST + (u32)(min(72 * v + 64 * i + lane, 72 * v + 71) * 8), (u32x2){xg[i][0], xg[i][2]});
    bar_h();
    {
      const rsrc_t rw = mk_rsrc(P.win, 1024u * 7168u);
      int k = lane / 14, ch = lane - 14 * k;
      u32 vo = (u32)((64 * c.rp + k) * 7168 + ((v == 0 ? 1792 : 0) + 112 * c.cj) * 2 + ch * 16);
#pragma unroll
      for (int j = 0; j < 14; ++j) {
        wr[j] = __builtin_amdgcn_raw_buffer_load_b128(rw, (int)vo, 0, 0);
        ch += 8; const bool wrap = ch >= 14; ch -= wrap ? 14 : 0; vo += wrap ? (u32)(5 * 7168 - 96) : (u32)(4 * 7168 + 128);
        if (j == 6) bar_n();
      }
    }
    bar_n();
  }
  const AtPre ap = attention_pre(c);
  bar_n();
  attention(c, ap);
  bar_n();
#pragma unroll
  for (int j = 0; j < 14; ++j) lds_st<u32x4>(stg + (u32)(j * 1024 + lane * 16), wr[j]);
  MK_LGKM0();
  {
    const int odd = c.cj & 1;
    const u32 ta = stg + (u32)((8 * c.g + c.q) * 224);
    const u32 tp = ta + (u32)(((odd ? 16 : 0) + 8 * c.p) * 2), ts = ta + (u32)(((odd ? 0 : 96) + 4 * c.p) * 2);
    const u32 wo_ = WS_WINP + (u32)((c.cj * 2 + v) * 32 + 2 * c.rp) * GB;
    float cs = 0.f;
    sfor<7>([&](auto nc) {
      constexpr int n = decltype(nc)::v, o = n < 6 ? 64 * (n >> 1) + 8 * (n & 1) : 0;
      bf16x8 f2[2];
      lds_tr4<o, o + 896, o + 7168, o + 7168 + 896>(n < 6 ? tp : ts, n < 6 ? tp : ts, f2[0], f2[1]);
      f32x2 s2 = {0.f, 0.f};
#pragma unroll
      for (int kl = 0; kl < 2; ++kl) {
        const u32x4 fr = __builtin_bit_cast(u32x4, f2[kl]);
        st128<SC1>(fr, c.rws, (int)frag_off(c, n, lane), (int)(wo_ + kl * GB));
#pragma unroll
        for (int d = 0; d < 4; ++d) s2 += (f32x2){bf_lo(fr[d]), bf_hi(fr[d])};
      }
      const float t = sumg(s2[0] + s2[1]);
      if constexpr (n < 4) cs = (c.g == n) ? t : cs;
      if constexpr (n == 3) __builtin_amdgcn_raw_buffer_store_b32(asu(cs), c.rws, (int)(WS_CS + (u32)(((c.rp * 16 + c.cj) * 256 + 112 * v + lane) * 4)), 0, SC1);
      if constexpr (n >= 4) cs = (c.g == n - 4) ? t : cs;
    });
    if (lane < 48) __builtin_amdgcn_raw_buffer_store_b32(asu(cs), c.rws, (int)(WS_CS + (u32)(((c.rp * 16 + c.cj) * 256 + 112 * v + 64 + lane) * 4)), 0, SC1);
  }
  bar_h();
  bars(4);
  publish(c, FW, v);
  bars(6);
  if (v == 1) touch_code(c);
  bars(7);
  bar_n();
  const int hi = lane >> 5, l5 = lane & 31;
  u32x4 cp[16];
  if (v == 0) await_panel<14>(c, FHH, 1, 4);
  else {
    poll64(c, flag_off(FW, 0, 0) + (u32)((((lane >> 1) & 15) * 16 + c.cj) * 128 + 8 * (lane & 1)));
#pragma unroll
    for (int j = 0; j < 16; ++j)
      cp[j] = __builtin_amdgcn_raw_buffer_load_b128(c.rws, (int)(WS_CS + (u32)(((hi * 16 + c.cj) * 256 + 112 * (j >> 3) + 4 * min(l5, 27)) * 4)), (j & 7) * 32768, SC1);
  }
  bar_n();
  {
    auto fold = [](float x) { const u32 u = asu(x); auto r = __builtin_amdgcn_permlane32_swap(u, u, false, false); return asf((u32)r[0]) + asf((u32)r[1]); };
    const u32 fso = flag_off(FHS, 0, 0) + (u32)((c.rp * 16 + (lane & 15)) * 128);
    bars(5);
    if (v == 1 && lane == 0) __builtin_amdgcn_raw_buffer_store_b64((u32x2){c.tag_lo, c.tag_hi}, c.rws, (int)flag_off(FHS, c.rp, c.cj), 0, SC1);
    bars(3);
    const u32x2 fsv = poll_issue(c, fso);
    bars(2);
    poll_check(c, fso, fsv);
    u32x4 hp[8];
#pragma unroll
    for (int j = 0; j < 8; ++j) {
      hp[j] = __builtin_amdgcn_raw_buffer_load_b128(c.rws, (int)(WS_HSTAT + (u32)(((c.rp * 16 + hi) * 128 + 64 * v + 2 * l5) * 8)), j * 2048, SC1);
    }
#pragma unroll
    for (int h = 0; h < 2; ++h) {
      bar_n();
      if (v == 1) {
        float c0 = 0.f, c1 = 0.f, c2 = 0.f, c3 = 0.f;
#pragma unroll
        for (int j = 8 * h; j < 8 * h + 8; ++j) { add2(c0, c1, cp[j][0], cp[j][1]); add2(c2, c3, cp[j][2], cp[j][3]); }
        lds_st<f32x4>(c.lds + T_CS + (u32)((112 * h + 4 * min(l5, 27)) * 4), (f32x4){fold(c0), fold(c1), fold(c2), fold(c3)});
      }
    }
    if (v == 0) touch_code(c);
    bars(2);
    float s0 = 0.f, q0 = 0.f, s1 = 0.f, q1 = 0.f;
#pragma unroll
    for (int j = 0; j < 8; ++j) { add2(s0, q0, hp[j][0], hp[j][1]); add2(s1, q1, hp[j][2], hp[j][3]); }
    {
      s0 = fold(s0); q0 = fold(q0); s1 = fold(s1); q1 = fold(q1);
      const float sx = hi ? s1 : s0, qx = hi ? q1 : q0;
      const float mean = sx * (1.0f / 1024.0f);
      const float rs = __builtin_amdgcn_rsqf(fmaxf(qx * (1.0f / 1024.0f) - mean * mean, 0.f) + 1e-5f);
      lds_st<float>(c.lds + T_HM + (u32)((64 * v + 2 * l5 + hi) * 4), mean);
      lds_st<float>(c.lds + T_HR + (u32)((64 * v + 2 * l5 + hi) * 4), rs);
    }
    bar_n();
    bar_h(); bar_n();
#pragma unroll
    for (int h = 0; h < 2; ++h)
      touch(c, mk_rsrc(P.wsp, 2048u * 4096u), (u32)((lane >> 4) * 4096 + (lane & 15) * 128), blockIdx.x * 32768u + v * 16384u + h * 2048u);
    bars(SWEEP_T - 16);
    u32x4 zp[16];
    auto sweep = [&]() {
#pragma unroll
      for (int j = 0; j < 16; ++j) zp[j] = __builtin_amdgcn_raw_buffer_load_b128(c.rws, (int)(WS_ZSTAT + (u32)((c.rp * 2048 + 64 * v + lane) * 16)), j * 2048, SC1);
    };
    sweep();
    bars(NORM_T - 1 - SWEEP_T);
    float zs = 0.f, zq = 0.f;
    u32 bad = 0;
    auto half = [&](int j0) {
#pragma unroll
      for (int j = j0; j < j0 + 8; ++j) {
        u32 t1, t2;
        asm("v_bitop3_b32 %3, %5, %6, %8 bitop3:0x96\n\tv_bitop3_b32 %4, %7, %9, %8 bitop3:0x69\n\tv_add_f32 %0, %0, %9\n\tv_add_f32 %1, %1, %6\n\tv_or3_b32 %2, %2, %3, %4"
            : "+v"(zs), "+v"(zq), "+v"(bad), "=&v"(t1), "=&v"(t2) : "v"(zp[j][1]), "v"(zp[j][2]), "v"(zp[j][3]), "s"(c.tag_lo), "v"(zp[j][0]));
      }
    };
    half(0);
    asm volatile("" : "+v"(zs), "+v"(zq), "+v"(bad));
    bar_n();
    half(8);
    if (!__all(bad == 0)) {
#pragma unroll 1
      for (int sp = 0; sp < SPIN_CAP; ++sp) {
        asm volatile("" ::: "memory");
        sweep();
        zs = 0.f; zq = 0.f; bad = 0;
        half(0); half(8);
        if (__all(bad == 0)) break;
      }
    }
    const float zm = zs * (1.0f / 1792.0f);
    const float zr = __builtin_amdgcn_rsqf(fmaxf(zq * (1.0f / 1792.0f) - zm * zm, 0.f) + 1e-5f);
    lds_st<float>(c.lds + T_ZM + (u32)((64 * v + lane) * 4), -zm * zr);
    lds_st<float>(c.lds + T_ZR + (u32)((64 * v + lane) * 4), zr);
    bar_h();
    bars(FLAG_T - NORM_T);
    if (v == 1 && lane == 0) __builtin_amdgcn_raw_buffer_store_b64((u32x2){c.tag_lo, c.tag_hi}, c.rws, (int)flag_off(FZ, c.rp, c.cj), 0, SC1);
    bars(26 - FLAG_T);
    const u32 fo = flag_off(FZ, 0, 0) + (u32)(((lane & 15) * 16 + c.cj) * 128);
    u32x2 fv = {0, 0};
    if (v == 0) fv = poll_issue(c, fo);
    bars(2);
    if (v == 0) poll_check(c, fo, fv);
    bars(10);
    if (v == 1) {
      const rsrc_t r3 = mk_rsrc(P.wout, 1792u * 2048u);
      touch(c, r3, (u32)(lane * 128), blockIdx.x * 14336u);
      if (lane < 48) touch(c, r3, (u32)(lane * 128), blockIdx.x * 14336u + 8192u);
    }
    bars(14);
    if (v == 1) touch_code(c);
    if (v == 0) {
      const int cs5 = (c.cj & 8) + ((3 - (int)((size_t)P.wout >> 7)) & 7);
      if (lane < 56) touch(c, mk_rsrc(P.wout, 1792u * 2048u), (u32)(((blockIdx.x >> 3) + 32 * lane) * 2048 + cs5 * 128), 0);
    }
    bars(11);
  }
  bar_n();
  if (v == 0) await_panel<16>(c, FU, 2, 4);
  bar_n();
  asm volatile(".Lmk_end_w:");
}

__global__ __launch_bounds__(512, 2) void fused_block(Params P) {
  extern __shared__ __attribute__((aligned(16))) char smem[];
  asm volatile(".Lmk_code_begin:");
  Ctx c;
  c.lane = threadIdx.x & 63;
  c.wid = __builtin_amdgcn_readfirstlane(threadIdx.x >> 6);
  const int xcd = blockIdx.x & 7, idx = blockIdx.x >> 3;
  c.rp = (xcd & 3) * 4 + (idx & 3);
  c.cj = (xcd >> 2) * 8 + (idx >> 2);
  c.r16 = c.lane & 15; c.g = c.lane >> 4; c.q = (c.lane >> 2) & 3; c.p = c.lane & 3;
  c.lds = (u32)(size_t)(LAS char*)smem;
  c.rws = mk_rsrc(P.ws, WS_END);
  c.skip = 7 - ((3 - (int)((size_t)P.ws >> 7)) & 7);
  c.tag_lo = 0; c.tag_hi = 0;
  u32x4 xr0[4];
  if (c.wid == 7) touch_code(c);
  if (c.wid >= 6) {
    __builtin_amdgcn_s_sleep(5);
    if (c.wid == 6) warm_slow(P, c, 64, 384);
    warm_x(P, c, 0, xr0);
  }
  if (__builtin_expect((c.wid >> 1) == 2, 1)) loader(P, c); else if (c.wid < 4) consumer(P, c); else worker(P, c, xr0);
}

}

extern "C" void kernel_launch(void* const* d_in, const int* in_sizes, int n_in, void* d_out, int out_size, void* d_ws, size_t ws_size,
                              hipStream_t stream) {
  (void)in_sizes; (void)n_in; (void)out_size; (void)ws_size;
  mk::Params P;
  P.x = (const mk::u16*)d_in[0]; P.wqkv = (const mk::u16*)d_in[1]; P.wo = (const mk::u16*)d_in[2];
  P.win = (const mk::u16*)d_in[4]; P.wsp = (const mk::u16*)d_in[5]; P.bsp = (const mk::u16*)d_in[6]; P.wout = (const mk::u16*)d_in[7];
  P.out = (mk::u16*)d_out; P.ws = (char*)d_ws;
  (void)hipFuncSetAttribute((const void*)mk::fused_block, hipFuncAttributeMaxDynamicSharedMemorySize, mk::LDS_BYTES);
  hipLaunchKernelGGL(mk::fused_block, dim3(256), dim3(512), mk::LDS_BYTES, stream, P);
}
```

```cpp
#include <hip/hip_runtime.h>

namespace mk {

#define LAS __attribute__((address_space(3)))
typedef LAS void* lds_vp;
typedef unsigned short u16;
typedef unsigned int u32;
typedef unsigned long long u64;
typedef short bf16x8 __attribute__((ext_vector_type(8)));
typedef short bf16x4 __attribute__((ext_vector_type(4)));
typedef float f32x2 __attribute__((ext_vector_type(2)));
typedef float f32x4 __attribute__((ext_vector_type(4)));
typedef u32 u32x2 __attribute__((ext_vector_type(2)));
typedef u32 u32x4 __attribute__((ext_vector_type(4)));
typedef __bf16 bf16x2n __attribute__((ext_vector_type(2)));
typedef __amdgpu_buffer_rsrc_t rsrc_t;

constexpr u32 WS_EPOCH = 0;
constexpr u32 WS_FLAGS = 4096;
enum { FHS = 0, FW = 1, FO = 2, FHH = 3, FZ = 4, FU = 5 };
constexpr u32 WS_XSTAT = 256 * 1024;
constexpr u32 WS_HSTAT = 512 * 1024;
constexpr u32 WS_CS    = 768 * 1024;
constexpr u32 WS_ZSTAT = 1024 * 1024;
constexpr u32 GA = 10240, GB = 8192;
constexpr u32 WS_O     = 2u << 20;
constexpr u32 WS_H     = 8u << 20;
constexpr u32 WS_WINP  = 14u << 20;
constexpr u32 WS_Z2N   = 23u << 20;
constexpr u32 WS_U     = 32u << 20;
constexpr u32 WS_END   = 42u << 20;
__host__ __device__ constexpr u32 flag_off(int kind, int rp, int cj) { return WS_FLAGS + (u32)((kind * 256 + rp * 16 + cj) * 128); }

constexpr int P1_A = 144 * 128, P1_B = 192 * 128, P1_ST = P1_A + P1_B + 16;
constexpr int P1_ONES = P1_A + P1_B;
constexpr int G2_NB = 11, G2_B = 0, G2_A = G2_NB * 8192;
constexpr int WSTG = G2_A;
constexpr int G2_PRE = 10, AT_BASE = G2_PRE * 8192, AT_RS = 80, AT_BLK = 72 * AT_RS;
constexpr int G3_B = 6 * 16384;
__host__ __device__ constexpr int g3_aslot(int t) { return t < 32 ? t % 6 : (t + 2) % 4; }
constexpr int TAB = 156 * 1024;
constexpr int T_XST = TAB;
constexpr int T_ZM = TAB, T_ZR = TAB + 512, T_HM = TAB + 1152, T_HR = T_HM + 512, T_CS = T_HR + 512;
constexpr int T_TAG = T_CS + 896, T_CNT = T_TAG + 8, T_DUMMY = T_TAG + 32;
constexpr int LDS_BYTES = 160 * 1024;
static_assert(3 * P1_ST <= TAB && WSTG + 2 * 14336 <= TAB, "lds");
static_assert(AT_BASE + 12 * AT_BLK <= TAB, "lds");
static_assert(G2_A + 4 * 16384 <= TAB, "lds");
static_assert(G3_B + 4 * 14336 <= TAB, "lds");
static_assert(T_DUMMY + 256 <= LDS_BYTES, "lds");

constexpr int SWEEP_T = 18;
constexpr int NORM_T = 21;
constexpr int FLAG_T = 24;

struct Params {
  const u16* x; const u16* wqkv; const u16* wo; const u16* win; const u16* wsp; const u16* bsp; const u16* wout;
  u16* out; char* ws;
};

template <int I> struct IC { static constexpr int v = I; };
template <int N, int I = 0, class F> __device__ __forceinline__ void sfor(F&& f) { if constexpr (I < N) { f(IC<I>{}); sfor<N, I + 1>(f); } }
#define MK_WAITV(n) asm volatile("s_waitcnt vmcnt(%0)" ::"i"(n) : "memory")
#define MK_LGKM0() __builtin_amdgcn_s_waitcnt(0xC07F)
#define MK_SB() __builtin_amdgcn_sched_barrier(0)
__device__ __forceinline__ void bar_h() { asm volatile("s_waitcnt lgkmcnt(0)\n\ts_barrier" ::: "memory"); }
__device__ __forceinline__ void bar_n() { asm volatile("s_barrier" ::: "memory"); }
__device__ __forceinline__ void bar_c() { asm volatile("" ::: "memory"); __builtin_amdgcn_s_barrier(); asm volatile("" ::: "memory"); }

__device__ __forceinline__ rsrc_t mk_rsrc(const void* p, u32 bytes) { return __builtin_amdgcn_make_buffer_rsrc((void*)p, 0, bytes, 0x00020000); }
template <int OFF, int AUX> __device__ __forceinline__ void glds(rsrc_t r, u32 ldsdst, u32 voff, u32 soff) {
  __builtin_amdgcn_raw_ptr_buffer_load_lds(r, (lds_vp)(size_t)ldsdst, 16, (int)voff, (int)soff, OFF, AUX);
}
constexpr int SC1 = 16;
template <int AUX> __device__ __forceinline__ void st128(u32x4 v, rsrc_t r, int voff, int soff) {
  __builtin_amdgcn_raw_buffer_store_b128(v, r, voff, soff, AUX);
  asm volatile("s_nop 1" ::"v"(v) : "memory");
}
__device__ __forceinline__ u32 pk_bf16(float lo, float hi) { f32x2 v = {lo, hi}; return __builtin_bit_cast(u32, __builtin_convertvector(v, bf16x2n)); }
__device__ __forceinline__ float asf(u32 u) { return __builtin_bit_cast(float, u); }
__device__ __forceinline__ u32 asu(float f) { return __builtin_bit_cast(u32, f); }
__device__ __forceinline__ float bf_lo(u32 u) { return __builtin_bit_cast(float, u << 16); }
__device__ __forceinline__ float bf_hi(u32 u) { return __builtin_bit_cast(float, u & 0xffff0000u); }
template <int CTRL> __device__ __forceinline__ float dpp(float x) {
  return __builtin_bit_cast(float, __builtin_amdgcn_update_dpp(0, __builtin_bit_cast(int, x), CTRL, 0xf, 0xf, true));
}
__device__ __forceinline__ float sum16(float x) { x += dpp<0xB1>(x); x += dpp<0x4E>(x); x += dpp<0x141>(x); x += dpp<0x140>(x); return x; }
__device__ __forceinline__ float sumg(float x) {
  u32 u = __builtin_bit_cast(u32, x);
  auto a = __builtin_amdgcn_permlane16_swap(u, u, false, false);
  x = __builtin_bit_cast(float, (u32)a[0]) + __builtin_bit_cast(float, (u32)a[1]);
  u = __builtin_bit_cast(u32, x);
  auto b = __builtin_amdgcn_permlane32_swap(u, u, false, false);
  return __builtin_bit_cast(float, (u32)b[0]) + __builtin_bit_cast(float, (u32)b[1]);
}
__device__ __forceinline__ u32x4 pair_swap(u32x2 a, u32x2 b) {
  auto r0 = __builtin_amdgcn_permlane16_swap(a[0], b[0], false, false);
  auto r1 = __builtin_amdgcn_permlane16_swap(a[1], b[1], false, false);
  return (u32x4){(u32)r0[0], (u32)r1[0], (u32)r0[1], (u32)r1[1]};
}
__device__ __forceinline__ void add2(float& s, float& q, u32 a, u32 b) {
  asm("v_add_f32 %0, %0, %2\n\tv_add_f32 %1, %1, %3" : "+v"(s), "+v"(q) : "v"(a), "v"(b));
}
template <class T> __device__ __forceinline__ T lds_ld(u32 a) { return *(const LAS T*)(size_t)a; }
template <class T> __device__ __forceinline__ void lds_st(u32 a, T v) { *(LAS T*)(size_t)a = v; }
__device__ __forceinline__ bf16x4 lds_tr(u32 a) { return __builtin_amdgcn_ds_read_tr16_b64_v4i16((LAS bf16x4*)(size_t)a); }
template <int O0, int O1, int O2, int O3> __device__ __forceinline__ void lds_tr4(u32 a, u32 b, bf16x8& f0, bf16x8& f1) {
  bf16x4 r0, r1, r2, r3;
  asm volatile("ds_read_b64_tr_b16 %0, %4 offset:%6\n\tds_read_b64_tr_b16 %1, %5 offset:%7\n\tds_read_b64_tr_b16 %2, %4 offset:%8\n\tds_read_b64_tr_b16 %3, %5 offset:%9\n\ts_waitcnt lgkmcnt(0)"
               : "=&v"(r0), "=&v"(r1), "=&v"(r2), "=&v"(r3) : "v"(a), "v"(b), "i"(O0), "i"(O1), "i"(O2), "i"(O3) : "memory");
  f0 = __builtin_shufflevector(r0, r1, 0, 1, 2, 3, 4, 5, 6, 7); f1 = __builtin_shufflevector(r2, r3, 0, 1, 2, 3, 4, 5, 6, 7);
}
__device__ __forceinline__ u32 opaque(u32 x) { asm("" : "+v"(x)); return x; }

struct Ctx {
  int lane, wid, rp, cj, r16, g, q, p;
  u32 lds;
  u32 tag_lo, tag_hi;
  int skip;
  rsrc_t rws;
};
__device__ __forceinline__ u32 frag_off(const Ctx& c, int f, int p) {
  const u32 l = (u32)(8 * f + (p >> 3));
  return (l + (((l + (u32)c.skip) * 37u) >> 8)) * 128u + (u32)((p & 7) * 16);
}
__device__ __forceinline__ void refresh(Ctx& c) {
  u32 t = threadIdx.x;
  asm volatile("" : "+v"(t));
  c.lane = t & 63; c.r16 = t & 15; c.g = (t >> 4) & 3; c.q = (t >> 2) & 3; c.p = t & 3;
}
constexpr int SPIN_CAP = 1 << 17;

__device__ __forceinline__ void get_tag(Ctx& c) {
  const u32x2 tg = lds_ld<u32x2>(c.lds + T_TAG);
  c.tag_lo = __builtin_amdgcn_readfirstlane(tg[0]); c.tag_hi = __builtin_amdgcn_readfirstlane(tg[1]);
}
__device__ __forceinline__ void publish(const Ctx& c, int kind, int slot) {
  asm volatile("s_waitcnt vmcnt(0)" ::: "memory");
  if (c.lane == 0) __builtin_amdgcn_raw_buffer_store_b64((u32x2){c.tag_lo, c.tag_hi}, c.rws, (int)(flag_off(kind, c.rp, c.cj) + 8 * slot), 0, SC1);
}
template <int PRE = 0> __device__ __forceinline__ void poll64(const Ctx& c, u32 voff) {
  if (PRE) __builtin_amdgcn_s_sleep(PRE);
#pragma unroll 1
  for (int s = 0; s < SPIN_CAP; ++s) {
    asm volatile("" ::: "memory");
    const u32x2 v = __builtin_amdgcn_raw_buffer_load_b64(c.rws, (int)voff, 0, SC1);
    if (__all(v[0] == c.tag_lo && v[1] == c.tag_hi)) break;
    __builtin_amdgcn_s_sleep(1);
  }
  asm volatile("" ::: "memory");
}
__device__ __forceinline__ void publish_last(const Ctx& c, int kind, int idx, u32 n) {
  MK_WAITV(0);
  if (c.lane == 0) {
    u32 old;
    asm volatile("ds_add_rtn_u32 %0, %1, %2\n\ts_waitcnt lgkmcnt(0)" : "=&v"(old) : "v"(c.lds + T_CNT + 4 * idx), "v"(1u) : "memory");
    if (old == n - 1) __builtin_amdgcn_raw_buffer_store_b64((u32x2){c.tag_lo, c.tag_hi}, c.rws, (int)flag_off(kind, c.rp, c.cj), 0, SC1);
  }
}
template <int PRE> __device__ __forceinline__ void await_panel(const Ctx& c, int kind, int idx, u32 n) {
#pragma unroll 1
  for (int s = 0; s < (1 << 22); ++s) {
    if (__builtin_amdgcn_readfirstlane(__hip_atomic_load((LAS u32*)(size_t)(c.lds + T_CNT + 4 * idx), __ATOMIC_RELAXED, __HIP_MEMORY_SCOPE_WORKGROUP)) >= n) break;
    __builtin_amdgcn_s_sleep(1);
  }
  poll64<PRE>(c, flag_off(kind, 0, 0) + (u32)((c.rp * 16 + (c.lane & 15)) * 128));
}
__device__ __forceinline__ u32x2 poll_issue(const Ctx& c, u32 voff) { return __builtin_amdgcn_raw_buffer_load_b64(c.rws, (int)voff, 0, SC1); }
__device__ __forceinline__ void poll_check(const Ctx& c, u32 voff, u32x2 v) {
  if (!__all(v[0] == c.tag_lo && v[1] == c.tag_hi)) poll64(c, voff);
  asm volatile("" ::: "memory");
}
__device__ __forceinline__ u32x4 tag_pair(const Ctx& c, float a, float b) {
  return (u32x4){asu(a), c.tag_lo ^ asu(b), asu(b), ~c.tag_lo ^ asu(a)};
}
__device__ __forceinline__ u32 tag_bad(const Ctx& c, u32x4 v) { return (v[1] ^ v[2] ^ c.tag_lo) | (v[3] ^ v[0] ^ ~c.tag_lo); }
__device__ __forceinline__ void touch(const Ctx& c, rsrc_t r, u32 voff, u32 soff) {
  __builtin_amdgcn_raw_ptr_buffer_load_lds(r, (lds_vp)(size_t)(c.lds + T_DUMMY), 4, (int)voff, (int)soff, 0, 0);
}
__device__ __forceinline__ void touch_code(const Ctx& c) {
  {
    u64 pc0; u32 back, e0, e1, e2;
    asm volatile("s_getpc_b64 %0\n.Lmk_pc%=:\n\ts_mov_b32 %1, .Lmk_pc%=-.Lmk_code_begin\n\ts_mov_b32 %2, .Lmk_end_c-.Lmk_code_begin\n\t"
                 "s_mov_b32 %3, .Lmk_end_l-.Lmk_code_begin\n\ts_mov_b32 %4, .Lmk_end_w-.Lmk_code_begin" : "=s"(pc0), "=s"(back), "=s"(e0), "=s"(e1), "=s"(e2));
    const rsrc_t rc = mk_rsrc((const void*)((pc0 - back) & ~127ull), max(e0, max(e1, e2)));
    if (c.lane < 24) touch(c, rc, (u32)(((blockIdx.x >> 3) + 32 * c.lane) * 128), 0);
  }
}

struct Win { u32 b[3]; };
__device__ __forceinline__ Win mk_win(u32 base) { Win w; w.b[0] = opaque(base); w.b[1] = opaque(base + 65536u); w.b[2] = opaque(base + 131072u); return w; }
template <int OFF> __device__ __forceinline__ u32 wad(const Win& w) { return w.b[OFF >> 16] + (u32)(OFF & 0xffff); }

template <int KS, int MT, int NT> struct Set { bf16x8 a[KS][MT]; bf16x8 b[KS][NT]; bf16x4 t[KS][NT]; };

__device__ __forceinline__ void mfma_acc(f32x4& c, const bf16x8& a, const bf16x8& b) {
  asm volatile("v_mfma_f32_16x16x32_bf16 %0, %1, %2, %0" : "+v"(c) : "v"(a), "v"(b));
}
__device__ __forceinline__ void mfma_in() { MK_SB(); asm volatile("s_nop 1"); MK_SB(); }
__device__ __forceinline__ void mfma_out() { MK_SB(); asm volatile("s_nop 7\n\ts_nop 1"); MK_SB(); }
template <int KS, int MT, int NT, bool SWAP, int NRD, class GAP, class RD>
__device__ __forceinline__ void part(f32x4 (&acc)[MT][NT], const Set<KS, MT, NT>& X, GAP, RD&& rd) {
  constexpr int NMF = KS * MT * NT;
  MK_SB();
  sfor<NMF>([&](auto ic) {
    constexpr int i = decltype(ic)::v, ks = i / (MT * NT), m = (i / NT) % MT, n = i % NT;
    if constexpr (SWAP) mfma_acc(acc[m][n], X.b[ks][n], X.a[ks][m]); else mfma_acc(acc[m][n], X.a[ks][m], X.b[ks][n]);
    MK_SB();
    sfor<NRD>([&](auto kc) { if constexpr (GAP::gap(decltype(kc)::v) == i) rd(kc); });
    if constexpr (i == NMF - 2) MK_LGKM0();
    MK_SB();
  });
}
struct Gap1 { static constexpr int gap(int k) { return k; } };
struct GapG2 { static constexpr int gap(int k) { return k < 2 ? k : (k < 10 ? 2 + (k - 2) / 2 : (k < 12 ? k - 4 : 8 + (k - 12) / 2)); } };

struct AdP1 { Win a[2], a0[2], b[3]; };
template <int ST, int KSI, int K> __device__ __forceinline__ void rd_p1(const AdP1& ad, Set<1, 9, 3>& Y) {
  constexpr int SB = ST * P1_ST;
  if constexpr (K == 0) Y.a[0][0] = lds_ld<bf16x8>(wad<SB>(ad.a0[KSI]));
  else if constexpr (K <= 6) {
    constexpr int n = (K - 1) >> 1, h = (K - 1) & 1;
    const bf16x4 v = lds_tr(wad<SB + P1_A + 4096 * KSI + 512 * h>(ad.b[n]));
    if constexpr (h == 0) Y.t[0][n] = v; else Y.b[0][n] = __builtin_shufflevector(Y.t[0][n], v, 0, 1, 2, 3, 4, 5, 6, 7);
  } else { constexpr int m = K - 6; Y.a[0][m] = lds_ld<bf16x8>(wad<SB + m * 2048>(ad.a[KSI])); }
}
struct AdG3 { Win af, ar[2], b; };
template <int T, int KSI, int K> __device__ __forceinline__ void rd_g3(const AdG3& ad, Set<1, 2, 7>& Y) {
  constexpr int SA = g3_aslot(T) * 16384, SB = G3_B + (T % 4) * 14336;
  if constexpr (K == 0 || K == 8) {
    constexpr int m = K == 0 ? 0 : 1;
    if constexpr (T >= 32) Y.a[0][m] = lds_ld<bf16x8>(wad<SA + m * 2048>(ad.ar[KSI]));
    else Y.a[0][m] = lds_ld<bf16x8>(wad<SA + KSI * 8192 + m * 1024>(ad.af));
  } else { constexpr int n = K - 1; Y.b[0][n] = lds_ld<bf16x8>(wad<SB + (KSI * 7 + n) * 1024>(ad.b)); }
}
struct AdG2 { Win a, b[4]; };
template <int T, int K> __device__ __forceinline__ void rd_g2(const AdG2& ad, Set<2, 2, 4>& Y) {
  constexpr int ks = K / 10, k = K % 10;
  if constexpr (k < 2) Y.a[ks][k] = lds_ld<bf16x8>(wad<G2_A + (T % 4) * 16384 + ks * 8192 + k * 1024>(ad.a));
  else {
    constexpr int n = (k - 2) >> 1, h = (k - 2) & 1;
    const bf16x4 v = lds_tr(wad<G2_B + (T % G2_NB) * 8192 + 4096 * ks + 512 * h>(ad.b[n]));
    if constexpr (h == 0) Y.t[ks][n] = v; else Y.b[ks][n] = __builtin_shufflevector(Y.t[ks][n], v, 0, 1, 2, 3, 4, 5, 6, 7);
  }
}
__device__ __forceinline__ u32 tr64_lane(const Ctx& c, int strip) {
  const int f = (c.q >> 1) | ((c.g & 1) << 1);
  return (u32)((strip >> 2) * 8192 + 128 * (8 * c.g + c.q) + 16 * ((2 * (strip & 3) + (c.p >> 1)) ^ (2 * f)) + 8 * (c.p & 1));
}

template <int ST, bool LAST> __device__ __forceinline__ void tile_p1(f32x4 (&acc)[9][3], Set<1, 9, 3>& X, Set<1, 9, 3>& Y, const AdP1& ad) {
  part<1, 9, 3, true, 15>(acc, X, Gap1{}, [&](auto kc) { rd_p1<ST, 1, decltype(kc)::v>(ad, Y); });
  bar_c();
  part<1, 9, 3, true, LAST ? 0 : 15>(acc, Y, Gap1{}, [&](auto kc) { rd_p1<(ST + 1) % 3, 0, decltype(kc)::v>(ad, X); });
}
template <int T, class H>
__device__ __forceinline__ void tile_g3(f32x4 (&acc)[2][7], Set<1, 2, 7>& X, Set<1, 2, 7>& Y, const AdG3& ad, H&& hook) {
  part<1, 2, 7, (T >= 16), 9>(acc, X, Gap1{}, [&](auto kc) { rd_g3<T, 1, decltype(kc)::v>(ad, Y); });
  hook();
  MK_SB();
  bar_c();
  part<1, 2, 7, (T >= 16), T < 63 ? 9 : 0>(acc, Y, Gap1{}, [&](auto kc) { rd_g3<T + 1, 0, decltype(kc)::v>(ad, X); });
}
template <int NT, bool LASTBAR> __device__ __forceinline__ void gemm_g2(f32x4 (&acc)[2][4], const Ctx& c) {
  AdG2 ad;
  ad.a = mk_win(c.lds + (u32)(c.wid * 2048 + c.lane * 16));
#pragma unroll
  for (int n = 0; n < 4; ++n) ad.b[n] = mk_win(c.lds + tr64_lane(c, n));
  Set<2, 2, 4> X, Y;
  bar_c();
  sfor<20>([&](auto kc) { rd_g2<0, decltype(kc)::v>(ad, X); });
  MK_LGKM0();
  sfor<NT>([&](auto tc) {
    constexpr int t = decltype(tc)::v;
    if constexpr (t + 1 < NT || LASTBAR) bar_c();
    part<2, 2, 4, true, (t + 1 < NT) ? 20 : 0>(acc, (t & 1) ? Y : X, GapG2{}, [&](auto kc) { rd_g2<t + 1, decltype(kc)::v>(ad, (t & 1) ? X : Y); });
  });
  mfma_out();
}

struct AtPre { u32 q, k0, k1, v0, v1, o[2]; u64 ok[8]; };
__device__ __forceinline__ AtPre attention_pre(const Ctx& c) {
  const int hh = c.wid & 1, T = c.wid >> 1;
  const int lo = (c.rp == 0 && T == 0) ? 8 : 0;
  const u32 bh = c.lds + AT_BASE + (u32)(hh * 2 * AT_BLK);
  AtPre a;
  a.q = bh + (u32)((8 + 16 * T + c.r16) * AT_RS + 16 * c.g);
  a.k0 = bh + (u32)(4 * AT_BLK + (16 * T + c.r16) * AT_RS + 16 * c.g);
  a.k1 = bh + (u32)(4 * AT_BLK + min(16 * T + 16 + c.r16, 71) * AT_RS + 16 * c.g);
  const int v0 = 16 * T + 4 * c.g + c.q;
  a.v0 = bh + (u32)(8 * AT_BLK + v0 * AT_RS + 8 * c.p);
  a.v1 = bh + (u32)(8 * AT_BLK + min(v0 + 16, 71) * AT_RS + 8 * c.p);
  const int G = ((c.g & 1) << 1) | (c.g >> 1);
#pragma unroll
  for (int par = 0; par < 2; ++par) a.o[par] = WS_O + (u32)(c.rp * 32 + 2 * c.cj + hh) * GA + frag_off(c, 2 * T + par, 16 * G + c.r16);
#pragma unroll
  for (int j = 0; j < 8; ++j) {
    const int ak = 16 * (j >> 2) + 4 * c.g + (j & 3), w = 8 + c.r16 - ak;
    a.ok[j] = __ballot((w >= 0) && (w <= 7) && (ak >= lo));
  }
  asm volatile("" : "+v"(a.q), "+v"(a.k0), "+v"(a.k1), "+v"(a.v0), "+v"(a.v1), "+v"(a.o[0]), "+v"(a.o[1]));
  asm volatile("" : "+s"(a.ok[0]), "+s"(a.ok[1]), "+s"(a.ok[2]), "+s"(a.ok[3]), "+s"(a.ok[4]), "+s"(a.ok[5]), "+s"(a.ok[6]), "+s"(a.ok[7]));
  return a;
}
__device__ __forceinline__ float vmax(float a, float b) { return __builtin_elementwise_maximum(a, b); }
__device__ __forceinline__ float vmax3(float a, float b, float d) { return vmax(vmax(a, b), d); }
__device__ __forceinline__ float vsel(u64 m, float a, float b) { return __builtin_amdgcn_inverse_ballot_w64(m) ? a : b; }
__device__ __forceinline__ void attention(const Ctx& c, const AtPre& a) {
  const float sc = 0.17677669529663687f * 1.4426950408889634f;
  const f32x4 zero = {0.f, 0.f, 0.f, 0.f};
  bf16x8 qf[2], kf[2][2], vf[2][2];
#pragma unroll
  for (int par = 0; par < 2; ++par) {
    kf[par][0] = lds_ld<bf16x8>(a.k0 + par * AT_BLK); qf[par] = lds_ld<bf16x8>(a.q + par * AT_BLK); kf[par][1] = lds_ld<bf16x8>(a.k1 + par * AT_BLK);
  }
  bf16x4 r[8];
  asm volatile("ds_read_b64_tr_b16 %0, %8\n\tds_read_b64_tr_b16 %1, %9\n\tds_read_b64_tr_b16 %2, %8 offset:32\n\tds_read_b64_tr_b16 %3, %9 offset:32\n\t"
               "ds_read_b64_tr_b16 %4, %8 offset:%10\n\tds_read_b64_tr_b16 %5, %9 offset:%10\n\tds_read_b64_tr_b16 %6, %8 offset:%11\n\tds_read_b64_tr_b16 %7, %9 offset:%11"
               : "=&v"(r[0]), "=&v"(r[1]), "=&v"(r[2]), "=&v"(r[3]), "=&v"(r[4]), "=&v"(r[5]), "=&v"(r[6]), "=&v"(r[7])
               : "v"(a.v0), "v"(a.v1), "i"(AT_BLK), "i"(AT_BLK + 32));
  MK_SB();
  f32x4 s[2][2];
#pragma unroll
  for (int par = 0; par < 2; ++par)
#pragma unroll
    for (int k2 = 0; k2 < 2; ++k2) s[par][k2] = __builtin_amdgcn_mfma_f32_16x16x32_bf16(kf[par][k2], qf[par], zero, 0, 0, 0);
  float e[2][8], mx[2];
  const float ninf = -3.0e38f;
#pragma unroll
  for (int par = 0; par < 2; ++par) {
#pragma unroll
    for (int j = 0; j < 8; ++j) { const float t = s[par][j >> 2][j & 3]; e[par][j] = vsel(a.ok[j], t, ninf); }
    mx[par] = vmax3(vmax3(e[par][0], e[par][1], e[par][2]), vmax3(e[par][3], e[par][4], e[par][5]), vmax(e[par][6], e[par][7]));
  }
  {
    auto p = __builtin_amdgcn_permlane16_swap(asu(mx[0]), asu(mx[1]), false, false);
    const float m = vmax(asf((u32)p[0]), asf((u32)p[1]));
    auto q = __builtin_amdgcn_permlane32_swap(asu(m), asu(m), false, false);
    const float t = vmax(asf((u32)q[0]), asf((u32)q[1]));
    auto u = __builtin_amdgcn_permlane16_swap(asu(t), asu(t), false, false);
    mx[0] = asf((u32)u[0]) * -sc; mx[1] = asf((u32)u[1]) * -sc;
  }
  asm volatile("s_waitcnt lgkmcnt(0)" : "+v"(r[0]), "+v"(r[1]), "+v"(r[2]), "+v"(r[3]), "+v"(r[4]), "+v"(r[5]), "+v"(r[6]), "+v"(r[7]));
  const bf16x8 ones = __builtin_bit_cast(bf16x8, (u32x4){0x3f803f80u, 0x3f803f80u, 0x3f803f80u, 0x3f803f80u});
  f32x4 o[2][2], sum[2];
#pragma unroll
  for (int par = 0; par < 2; ++par) {
    vf[par][0] = __builtin_shufflevector(r[4 * par], r[4 * par + 1], 0, 1, 2, 3, 4, 5, 6, 7);
    vf[par][1] = __builtin_shufflevector(r[4 * par + 2], r[4 * par + 3], 0, 1, 2, 3, 4, 5, 6, 7);
#pragma unroll
    for (int j = 0; j < 8; ++j) e[par][j] = __builtin_amdgcn_exp2f(__builtin_fmaf(e[par][j], sc, mx[par]));
    const bf16x8 pf = __builtin_bit_cast(bf16x8, (u32x4){pk_bf16(e[par][0], e[par][1]), pk_bf16(e[par][2], e[par][3]), pk_bf16(e[par][4], e[par][5]), pk_bf16(e[par][6], e[par][7])});
    sum[par] = __builtin_amdgcn_mfma_f32_16x16x32_bf16(ones, pf, zero, 0, 0, 0);
    o[par][0] = __builtin_amdgcn_mfma_f32_16x16x32_bf16(vf[par][0], pf, zero, 0, 0, 0);
    o[par][1] = __builtin_amdgcn_mfma_f32_16x16x32_bf16(vf[par][1], pf, zero, 0, 0, 0);
  }
#pragma unroll
  for (int par = 0; par < 2; ++par) {
    const float inv = __builtin_amdgcn_rcpf(sum[par][0]);
    const f32x4 o0 = o[par][0] * inv, o1 = o[par][1] * inv;
    st128<SC1>(pair_swap((u32x2){pk_bf16(o0[0], o0[1]), pk_bf16(o0[2], o0[3])}, (u32x2){pk_bf16(o1[0], o1[1]), pk_bf16(o1[2], o1[3])}),
                                           c.rws, (int)a.o[par], 0);
  }
  publish_last(c, FO, 0, 8);
}

__device__ __forceinline__ float red8(const float (&v)[8], int r16) {
  const bool b2 = r16 & 4, b1 = r16 & 2, b0 = r16 & 1;
  float u[4], t[2];
#pragma unroll
  for (int i = 0; i < 4; ++i) u[i] = (b2 ? v[4 + i] : v[i]) + dpp<0x141>(b2 ? v[i] : v[4 + i]);
#pragma unroll
  for (int i = 0; i < 2; ++i) t[i] = (b1 ? u[2 + i] : u[i]) + dpp<0x4E>(b1 ? u[i] : u[2 + i]);
  const float x = (b0 ? t[1] : t[0]) + dpp<0xB1>(b0 ? t[0] : t[1]);
  return x + dpp<0x128>(x);
}

__device__ __forceinline__ void consumer(const Params& P, Ctx& c) {
  refresh(c);
  const int w = c.wid;
  const rsrc_t rx = mk_rsrc(P.x, 2048u * 2048u);
  const AtPre ap = attention_pre(c);
  {
    if (c.lane < 12) lds_st<u32>(c.lds + (u32)((c.lane >> 2) * P1_ST + P1_ONES + 4 * (c.lane & 3)), 0x3f803f80u);
    AdP1 ad;
#pragma unroll
    for (int ks = 0; ks < 2; ++ks) {
      const u32 a = (u32)(c.r16 * 128 + (((4 * ks + c.g) ^ (c.r16 >> 1)) * 16));
      ad.a[ks] = mk_win(c.lds + a);
      ad.a0[ks] = mk_win(c.lds + (c.r16 == 0 ? (u32)P1_ONES : a));
    }
#pragma unroll
    for (int n = 0; n < 3; ++n) ad.b[n] = mk_win(c.lds + tr64_lane(c, 3 * w + n));
    f32x4 acc[9][3];
#pragma unroll
    for (int m = 0; m < 9; ++m)
#pragma unroll
      for (int n = 0; n < 3; ++n) { acc[m][n] = (f32x4){0.f, 0.f, 0.f, 0.f}; asm volatile("" : "+v"(acc[m][n])); }
    u32 s0[3], s1[3];
#pragma unroll
    for (int n = 0; n < 3; ++n) {
      const int S = 3 * w + n;
      const u32 sb = c.lds + AT_BASE + (u32)((S >> 1) * 2 * AT_BLK + (S & 1) * 32 + 8 * c.g);
      s0[n] = sb + (u32)((c.r16 >> 3) * AT_BLK + (c.r16 & 7) * AT_RS); s1[n] = sb + (u32)((8 + c.r16) * AT_RS);
      asm volatile("" : "+v"(s0[n]), "+v"(s1[n]));
    }
    Set<1, 9, 3> X, Y;
    MK_LGKM0();
    bar_c();
    sfor<15>([&](auto kc) { rd_p1<0, 0, decltype(kc)::v>(ad, X); });
    MK_LGKM0();
#pragma unroll 1
    for (int it = 0; it < 5; ++it) { tile_p1<0, false>(acc, X, Y, ad); tile_p1<1, false>(acc, X, Y, ad); tile_p1<2, false>(acc, X, Y, ad); }
    part<1, 9, 3, true, 15>(acc, X, Gap1{}, [&](auto kc) { rd_p1<0, 1, decltype(kc)::v>(ad, Y); });
    bar_c();
    refresh(c);
    f32x2 xs[9];
    const u32 xa = opaque(c.lds + T_XST + (u32)(c.r16 * 8));
    part<1, 9, 3, true, 9>(acc, Y, Gap1{}, [&](auto kc) { constexpr int m = decltype(kc)::v; xs[m] = lds_ld<f32x2>(xa + m * 128); });
    mfma_out();
#pragma unroll
    for (int n = 0; n < 3; ++n) {
      f32x4 cs;
#pragma unroll
      for (int r = 0; r < 4; ++r) cs[r] = dpp<0x150>(acc[0][n][r]);
#pragma unroll
      for (int m = 0; m < 9; ++m) {
        const f32x4 v = (acc[m][n] - xs[m][0] * cs) * xs[m][1];
        lds_st<u32x2>(m == 0 ? s0[n] : s1[n] + (u32)(((m - 1) & 1) * AT_BLK + ((m - 1) >> 1) * 16 * AT_RS), (u32x2){pk_bf16(v[0], v[1]), pk_bf16(v[2], v[3])});
      }
    }
  }
  MK_LGKM0();
  bar_c();
  get_tag(c);
  int G = ((c.g & 1) << 1) | (c.g >> 1);
  u32x4 xr[2][2];
#pragma unroll
  for (int m = 0; m < 2; ++m)
#pragma unroll
    for (int pr = 0; pr < 2; ++pr)
      xr[m][pr] = __builtin_amdgcn_raw_buffer_load_b128(rx, (128 * c.rp + 32 * w + 2 * c.r16 + m) * 2048 + (64 * c.cj + 32 * pr + 8 * G) * 2, 0, 0);
  const u32 bspw = *(const u32*)(P.bsp + 128 * c.rp + 32 * w + 2 * c.r16);
  attention(c, ap);
  bar_c();
  u32x2 hb[2][4];
  {
    f32x4 acc[2][4];
#pragma unroll
    for (int m = 0; m < 2; ++m)
#pragma unroll
      for (int pr = 0; pr < 2; ++pr) {
        const u32x4 t = pair_swap((u32x2){xr[m][pr][0], xr[m][pr][1]}, (u32x2){xr[m][pr][2], xr[m][pr][3]});
        acc[m][2 * pr] = (f32x4){bf_lo(t[0]), bf_hi(t[0]), bf_lo(t[1]), bf_hi(t[1])};
        acc[m][2 * pr + 1] = (f32x4){bf_lo(t[2]), bf_hi(t[2]), bf_lo(t[3]), bf_hi(t[3])};
        asm volatile("" : "+v"(acc[m][2 * pr]), "+v"(acc[m][2 * pr + 1]));
      }
    u32 ho[2];
#pragma unroll
    for (int m = 0; m < 2; ++m) { ho[m] = WS_H + (u32)(c.rp * 32 + 2 * c.cj) * GA + frag_off(c, 2 * w + m, 16 * G + c.r16); asm volatile("" : "+v"(ho[m])); }
    bar_c();
    gemm_g2<16, true>(acc, c);
#pragma unroll
    for (int m = 0; m < 2; ++m) {
#pragma unroll
      for (int n = 0; n < 4; ++n) hb[m][n] = (u32x2){pk_bf16(acc[m][n][0], acc[m][n][1]), pk_bf16(acc[m][n][2], acc[m][n][3])};
#pragma unroll
      for (int pr = 0; pr < 2; ++pr) st128<SC1>(pair_swap(hb[m][2 * pr], hb[m][2 * pr + 1]), c.rws, (int)ho[m], pr * GA);
    }
    bar_c();
    float s[2], q[2];
#pragma unroll
    for (int m = 0; m < 2; ++m) {
      f32x2 s2 = {0.f, 0.f}, q2 = {0.f, 0.f};
#pragma unroll
      for (int n = 0; n < 4; ++n)
#pragma unroll
        for (int d = 0; d < 2; ++d) { const f32x2 a = {bf_lo(hb[m][n][d]), bf_hi(hb[m][n][d])}; s2 += a; q2 += a * a; }
      s[m] = sumg(s2[0] + s2[1]); q[m] = sumg(q2[0] + q2[1]);
    }
    float sv = c.g ? s[1] : s[0], qv = c.g ? q[1] : q[0];
    asm volatile("" : "+v"(sv), "+v"(qv));
    publish_last(c, FHH, 1, 4);
    if (c.g < 2) __builtin_amdgcn_raw_buffer_store_b64((u32x2){asu(sv), asu(qv)}, c.rws,
        (int)(WS_HSTAT + (u32)(((c.rp * 16 + c.cj) * 128 + 32 * w + c.lane) * 8)), 0, SC1);
  }
  {
    AdG3 ad;
    ad.af = mk_win(c.lds + (u32)(w * 2048 + c.lane * 16));
#pragma unroll
    for (int ks = 0; ks < 2; ++ks) ad.ar[ks] = mk_win(c.lds + (u32)((32 * w + c.r16) * 128 + (((4 * ks + c.g) ^ (c.r16 >> 1)) * 16)));
    ad.b = mk_win(c.lds + (u32)(c.lane * 16));
    float bsp[2] = {bf_lo(bspw), bf_hi(bspw)};
    f32x4 acc[2][7];
#pragma unroll
    for (int m = 0; m < 2; ++m)
#pragma unroll
      for (int n = 0; n < 7; ++n) { acc[m][n] = (f32x4){0.f, 0.f, 0.f, 0.f}; asm volatile("" : "+v"(acc[m][n])); }
    asm volatile("" : "+v"(bsp[0]), "+v"(bsp[1]));
    const u32 tg = opaque(c.lds + TAB + (u32)((32 * w + 4 * c.g) * 4));
    const u32 tr = opaque(c.lds + TAB + (u32)((32 * w + c.r16) * 4));
    Set<1, 2, 7> X, Y;
    bar_c();
    bar_c();
    sfor<9>([&](auto kc) { rd_g3<0, 0, decltype(kc)::v>(ad, X); });
    MK_LGKM0();
    sfor<16>([&](auto tc) { tile_g3<decltype(tc)::v>(acc, X, Y, ad, [&] { if constexpr (decltype(tc)::v == 3) MK_WAITV(0); }); });
    f32x4 hm[2], hr[2], c1[7];
    float c2[7], nm[2];
#pragma unroll
    for (int m = 0; m < 2; ++m) {
      hm[m] = lds_ld<f32x4>(tg + (T_HM - TAB) + 64 * m); hr[m] = lds_ld<f32x4>(tg + (T_HR - TAB) + 64 * m);
      nm[m] = lds_ld<float>(tr + (T_HM - TAB) + 64 * m);
    }
#pragma unroll
    for (int n = 0; n < 7; ++n) c2[n] = lds_ld<float>(c.lds + T_CS + (u32)((16 * n + c.r16) * 4));
    mfma_out();
    f32x4 z2[2][7];
    {
      f32x4 sm[2] = {{0.f, 0.f, 0.f, 0.f}, {0.f, 0.f, 0.f, 0.f}}, sq[2] = {{0.f, 0.f, 0.f, 0.f}, {0.f, 0.f, 0.f, 0.f}}, ga[2], gb[2];
#pragma unroll
      for (int m = 0; m < 2; ++m) { ga[m] = hr[m] * -2.30220820f; gb[m] = hr[m] * hr[m] * hr[m] * -0.10294324f; }
#pragma unroll
      for (int n = 0; n < 7; ++n) {
#pragma unroll
        for (int m = 0; m < 2; ++m) {
          const f32x4 t = acc[m][n] - hm[m] * c2[n];
          f32x4 y = t * (t * t * gb[m] + ga[m]);
          asm("" : "+v"(y));
          f32x4 e;
#pragma unroll
          for (int i = 0; i < 4; ++i) e[i] = __builtin_amdgcn_exp2f(y[i]);
          e += 1.0f;
#pragma unroll
          for (int i = 0; i < 4; ++i) e[i] = __builtin_amdgcn_rcpf(e[i]);
          const f32x4 z = t * e;
          z2[m][n] = z; sm[m] += z; sq[m] += z * z;
        }
      }
#pragma unroll
      for (int m = 0; m < 2; ++m) { sm[m] *= hr[m]; sq[m] *= hr[m] * hr[m]; }
      MK_SB();
      const u32 ca = c.lds + T_CS + (u32)((112 + 4 * c.g) * 4);
#pragma unroll
      for (int n = 0; n < 7; ++n) c1[n] = lds_ld<f32x4>(ca + 64 * n);
      const float sv[8] = {sm[0][0], sm[0][1], sm[0][2], sm[0][3], sm[1][0], sm[1][1], sm[1][2], sm[1][3]};
      const float qv[8] = {sq[0][0], sq[0][1], sq[0][2], sq[0][3], sq[1][0], sq[1][1], sq[1][2], sq[1][3]};
      const float s = red8(sv, c.r16), q = red8(qv, c.r16);
      if (c.r16 < 8) st128<SC1>(tag_pair(c, s, q), c.rws,
          (int)(WS_ZSTAT + (u32)(((c.rp * 16 + c.cj) * 128 + 32 * w + 16 * (c.r16 >> 2) + 4 * c.g + (c.r16 & 3)) * 16)), 0);
#pragma unroll
      for (int m = 0; m < 2; ++m)
#pragma unroll
        for (int n = 0; n < 7; ++n) { acc[m][n] = c1[n] * -nm[m]; asm volatile("" : "+v"(acc[m][n])); }
    }
    mfma_in();
    sfor<16>([&](auto tc) {
      constexpr int t = 16 + decltype(tc)::v;
      tile_g3<t>(acc, X, Y, ad, [&] { if constexpr (t == FLAG_T) MK_WAITV(0); });
      if constexpr (t == NORM_T) {
        refresh(c);
        f32x4 zr[2], zo[2];
#pragma unroll
        for (int m = 0; m < 2; ++m) {
          zr[m] = lds_ld<f32x4>(tg + (T_ZR - TAB) + 64 * m) * lds_ld<f32x4>(tg + (T_HR - TAB) + 64 * m);
          zo[m] = lds_ld<f32x4>(tg + (T_ZM - TAB) + 64 * m);
        }
#pragma unroll
        for (int n = 0; n < 7; ++n) {
          const f32x4 a = z2[0][n] * zr[0] + zo[0], b = z2[1][n] * zr[1] + zo[1];
          const u32x4 zf = {pk_bf16(a[0], b[0]), pk_bf16(a[1], b[1]), pk_bf16(a[2], b[2]), pk_bf16(a[3], b[3])};
          st128<SC1>(zf, c.rws, (int)frag_off(c, n, c.lane), (int)(WS_Z2N + (u32)(c.cj * 64 + 4 * c.rp + w) * GB));
        }
      }
    });
    mfma_out();
    f32x4 z1[2][7];
    float hr1[2];
#pragma unroll
    for (int m = 0; m < 2; ++m) {
      hr1[m] = lds_ld<float>(tr + (T_HR - TAB) + 64 * m);
#pragma unroll
      for (int n = 0; n < 7; ++n) {
        z1[m][n] = acc[m][n];
        acc[m][n] = (f32x4){bsp[m], bsp[m], bsp[m], bsp[m]};
        asm volatile("" : "+v"(acc[m][n]));
      }
    }
    u32 up[2], us[2];
    mfma_in();
    sfor<32>([&](auto tc) {
      constexpr int i = decltype(tc)::v;
      tile_g3<32 + i>(acc, X, Y, ad, [&] {
        if constexpr (i < 28) {
          constexpr int m = i / 14, n = (i % 14) / 2, h = 2 * (i & 1);
          const f32x2 x = (f32x2){z1[m][n][h], z1[m][n][h + 1]} * hr1[m];
          f32x2 y = x * (x * x * -0.10294324f + -2.30220820f);
          asm("" : "+v"(y));
          f32x2 e = {__builtin_amdgcn_exp2f(y[0]), __builtin_amdgcn_exp2f(y[1])};
          e += 1.0f;
          f32x2 z = x * (f32x2){__builtin_amdgcn_rcpf(e[0]), __builtin_amdgcn_rcpf(e[1])};
          asm volatile("" : "+v"(z));
          z1[m][n][h] = z[0]; z1[m][n][h + 1] = z[1];
        } else if constexpr (i < 30) {
          constexpr int m = i - 28;
          refresh(c);
          up[m] = frag_off(c, 2 * w + m, c.lane);
          us[m] = frag_off(c, 2 * w + m, 16 * (2 * (c.cj & 1) + (c.g >> 1)) + c.r16) + (u32)(8 * (c.g & 1));
          asm volatile("" : "+v"(up[m]), "+v"(us[m]));
        }
      });
    });
    mfma_out();
    {
      const int odd = c.cj & 1, kp = (7 * c.cj + 1) >> 1, ksg = (7 * c.cj + (odd ? -1 : 6)) >> 1;
      const u32 gp = WS_U + (u32)(c.rp * 56 + kp) * GA, gs = WS_U + (u32)(c.rp * 56 + ksg) * GA;
#pragma unroll
      for (int m = 0; m < 2; ++m) {
        u32x2 uv[7];
#pragma unroll
        for (int n = 0; n < 7; ++n) {
          const f32x4 u = z1[m][n] * acc[m][n];
          uv[n] = (u32x2){pk_bf16(u[0], u[1]), pk_bf16(u[2], u[3])};
          if (n & 1) st128<SC1>((u32x4){uv[n - 1][0], uv[n - 1][1], uv[n][0], uv[n][1]}, c.rws, (int)up[m], (int)(gp + (n >> 1) * GA));
        }
        __builtin_amdgcn_raw_buffer_store_b64(uv[6], c.rws, (int)us[m], (int)gs, SC1);
      }
      bar_c();
      publish_last(c, FU, 2, 4);
    }
  }
  {
    refresh(c);
    G = ((c.g & 1) << 1) | (c.g >> 1);
    f32x4 acc[2][4];
#pragma unroll
    for (int m = 0; m < 2; ++m)
#pragma unroll
      for (int n = 0; n < 4; ++n) {
        acc[m][n] = (f32x4){bf_lo(hb[m][n][0]), bf_hi(hb[m][n][0]), bf_lo(hb[m][n][1]), bf_hi(hb[m][n][1])};
        asm volatile("" : "+v"(acc[m][n]));
      }
    bar_c();
    gemm_g2<28, false>(acc, c);
    const rsrc_t ro = mk_rsrc(P.out, 2048u * 2048u);
#pragma unroll
    for (int m = 0; m < 2; ++m)
#pragma unroll
      for (int pr = 0; pr < 2; ++pr) {
        const f32x4 a = acc[m][2 * pr], b = acc[m][2 * pr + 1];
        st128<SC1 | 1>(pair_swap((u32x2){pk_bf16(a[0], a[1]), pk_bf16(a[2], a[3])}, (u32x2){pk_bf16(b[0], b[1]), pk_bf16(b[2], b[3])}), ro,
            (128 * c.rp + 32 * w + 2 * c.r16 + m) * 2048 + (64 * c.cj + 32 * pr + 8 * G) * 2, 0);
      }
    if (c.rp == 0 && c.cj == 0 && w == 0 && c.lane == 0)
      __builtin_amdgcn_raw_buffer_store_b64((u32x2){c.tag_lo, c.tag_hi}, c.rws, (int)WS_EPOCH, 0, SC1);
  }
  asm volatile(".Lmk_end_c:");
}

template <int N, int AUX> __device__ __forceinline__ void issue_v2(rsrc_t r, u32 dst, const u32 (&vo)[N], u32 so) {
  sfor<N>([&](auto ic) { constexpr int s = decltype(ic)::v; glds<(s & 1) * 2048, AUX>(r, dst + (s >> 1) * 4096, vo[s], so); });
}
template <int N> __device__ __forceinline__ void issue_f(rsrc_t r, u32 dst, const u32 (&lp)[8], u32 so) {
  sfor<N>([&](auto ic) { constexpr int s = decltype(ic)::v; glds<(s & 3) * 1024, SC1>(r, dst + (s >> 2) * 4096, lp[s], so); });
}
template <int LO, int HI, int NA_, class FA, class FB> __device__ __forceinline__ void issue_rng(FA&& fa, FB&& fb) {
  sfor<HI - LO>([&](auto ic) { constexpr int s = LO + decltype(ic)::v; if constexpr (s < NA_) fa(IC<s>{}); else fb(IC<s - NA_>{}); });
}
struct Off { u32 da, db, sa, sb; };
template <int NT_, int NA_, int SPLIT, int W, class FA, class FB, class NX>
__device__ __forceinline__ void steady(int n, Off& o, FA&& fa, FB&& fb, NX&& next) {
  bar_n();
  issue_rng<0, SPLIT, NA_>([&](auto ic) { fa(ic, o); }, [&](auto ic) { fb(ic, o); });
#pragma unroll 1
  for (int i = 1; i < n; ++i) {
    Off q = next(o);
    asm volatile("" : "+s"(q.da), "+s"(q.db), "+s"(q.sa), "+s"(q.sb));
    issue_rng<SPLIT, NT_, NA_>([&](auto ic) { fa(ic, o); }, [&](auto ic) { fb(ic, o); });
    MK_WAITV(W);
    bar_n();
    issue_rng<0, SPLIT, NA_>([&](auto ic) { fa(ic, q); }, [&](auto ic) { fb(ic, q); });
    o = q;
  }
  issue_rng<SPLIT, NT_, NA_>([&](auto ic) { fa(ic, o); }, [&](auto ic) { fb(ic, o); });
  MK_WAITV(W);
}
template <int NT_, int NA_, int W, class FA, class FB, class NX>
__device__ __forceinline__ void steady2(int lw, int n, Off o, FA&& fa, FB&& fb, NX&& next) {
  if (lw == 0) steady<NT_, NA_, NT_ / 4, W>(n, o, fa, fb, next); else steady<NT_, NA_, NT_ - NT_ / 4, W>(n, o, fa, fb, next);
}
__device__ __forceinline__ u32 tr64_src(int lane, int lw, int ldb_bytes) {
  const int f = ((lane >> 4) & 1) | (lw << 1);
  return (u32)((8 * lw + (lane >> 3)) * ldb_bytes + (((lane & 7) ^ (2 * f)) * 16));
}
template <int T, bool LASTBAR>
__device__ __forceinline__ u32x2 load_g2(const Ctx& c, int lw, const u32 (&lp)[8], u32 abase, rsrc_t rb, const u32 (&vb)[4], u32 fo) {
  constexpr u32 bstep = 64 * 2048;
  const u32 sa = abase + (u32)lw * GA, da = c.lds + G2_A + (u32)(lw * 8192), db = c.lds + G2_B + (u32)(lw * 1024);
  const int ko = (T / 4) * (c.rp >> 2);
  auto kt = [&](int t) { const int k = t + ko; return (u32)(k >= T ? k - T : k); };
  auto A = [&](int t) { issue_f<8>(c.rws, da + (u32)((t & 3) * 16384), lp, sa + kt(t) * 2 * GA); };
  auto B = [&](int t) { issue_v2<4, 0>(rb, db + (u32)((t % G2_NB) * 8192), vb, kt(t) * bstep); };
  A(0); A(1); MK_WAITV(8); bar_n();
  A(2); A(3); MK_WAITV(16); bar_n();
  A(4); B(11); MK_WAITV(20); bar_n();
  A(5); B(12);
  MK_WAITV(24);
  steady2<12, 8, 28>(lw, T - 13, Off{da + 2 * 16384, db + 2 * 8192, sa + kt(6) * 2 * GA, kt(13) * bstep},
      [&](auto ic, const Off& o) { constexpr int s = decltype(ic)::v; glds<(s & 3) * 1024, SC1>(c.rws, o.da + (s >> 2) * 4096, lp[s], o.sa); },
      [&](auto ic, const Off& o) { constexpr int s = decltype(ic)::v; glds<(s & 1) * 2048, 0>(rb, o.db + (s >> 1) * 4096, vb[s], o.sb); },
      [&](const Off& o) { return Off{o.da == da + 3 * 16384 ? da : o.da + 16384, o.db == db + (G2_NB - 1) * 8192 ? db : o.db + 8192,
                                      o.sa == sa + (T - 1) * 2 * GA ? sa : o.sa + 2 * GA, o.sb == (T - 1) * bstep ? 0 : o.sb + bstep}; });
  bar_n(); A(T - 7); MK_WAITV(24);
  bar_n(); A(T - 6); MK_WAITV(20);
  bar_n(); A(T - 5); MK_WAITV(16);
  bar_n(); A(T - 4); MK_WAITV(16);
  bar_n(); A(T - 3); MK_WAITV(16);
  bar_n(); A(T - 2); MK_WAITV(16);
  bar_n(); A(T - 1); MK_WAITV(16);
  bar_n(); MK_WAITV(8);
  bar_n(); MK_WAITV(0);
  bar_n();
  u32x2 fv = {0, 0};
  if (LASTBAR) { fv = poll_issue(c, fo); bar_n(); }
  return fv;
}
template <int T> __device__ __forceinline__ void pre_g2(const Ctx& c, int lw, rsrc_t rb, const u32 (&vb)[4], int t0, int t1) {
  const int ko = (T / 4) * (c.rp >> 2);
#pragma unroll 1
  for (int t = t0; t < t1; ++t) { const int k = t + ko; issue_v2<4, 0>(rb, c.lds + G2_B + (u32)(lw * 1024 + t * 8192), vb, (u32)((k >= T ? k - T : k) * 64 * 2048)); }
}
__device__ __forceinline__ void src_g2(const Ctx& c, int lw, u32 (&vb)[4]) {
  const u32 b0 = tr64_src(c.lane, lw, 2048) + (u32)(128 * c.cj + 4096);
#pragma unroll
  for (int s = 0; s < 4; ++s) vb[s] = b0 + (u32)(s * 16 * 2048 - (s & 1) * 2048);
}

__device__ __forceinline__ void loader(const Params& P, Ctx& c) {
  const int lw = c.wid - 4, lane = c.lane;
  {
    const rsrc_t rx = mk_rsrc((const char*)P.x - 4096, 2048u * 2048u + 4096u);
    const rsrc_t rw = mk_rsrc((const char*)P.wqkv - 4096, 1024u * 6144u + 4096u);
    u32 va[9], vb[12];
    const u32 ch = (u32)((((lane & 7) ^ (4 * lw + (lane >> 4))) * 16) + 4096);
    va[0] = (u32)(max(128 * c.rp - 16 + 2 * (lane >> 3) + lw, 0) * 2048) + ch;
    const u32 a1 = (u32)((128 * c.rp + 16 * lw + 2 * (lane >> 3)) * 2048) + ch;
#pragma unroll
    for (int m = 1; m < 9; ++m) va[m] = a1 + (u32)((32 * ((m - 1) >> 1) + ((m - 1) & 1)) * 2048 - (m & 1) * 2048);
    const u32 b0 = tr64_src(lane, lw, 6144) + (u32)(128 * c.cj + 4096);
#pragma unroll
    for (int s = 0; s < 12; ++s) vb[s] = b0 + (u32)((s >> 2) * 2048 + (s & 3) * 16 * 6144 - (s & 1) * 2048);
    const int ko = 4 * (c.rp >> 2);
    auto I = [&](int t, int st) {
      const u32 sb = c.lds + (u32)(st * P1_ST + lw * 1024);
      const int k = (t + ko) & 15;
      issue_v2<9, 0>(rx, sb, va, (u32)(k * 128));
      issue_v2<12, 0>(rw, sb + P1_A, vb, (u32)(k * 64 * 6144));
    };
    I(0, 0); MK_WAITV(0); bar_n();
    I(1, 1); I(2, 2);
    const u32 d0 = c.lds + (u32)(lw * 1024);
    MK_WAITV(21);
    steady2<21, 9, 21>(lw, 13, Off{d0, d0 + P1_A, (u32)((3 + ko) * 128), (u32)((3 + ko) * 64 * 6144)},
        [&](auto ic, const Off& o) { constexpr int s = decltype(ic)::v; glds<(s & 1) * 2048, 0>(rx, o.da + (s >> 1) * 4096, va[s], o.sa); },
        [&](auto ic, const Off& o) { constexpr int s = decltype(ic)::v; glds<(s & 1) * 2048, 0>(rw, o.db + (s >> 1) * 4096, vb[s], o.sb); },
        [&](const Off& o) { const u32 d = o.da == d0 + 2 * P1_ST ? d0 : o.da + P1_ST; return Off{d, d + P1_A, (o.sa + 128) & 2047, o.sa == 1920 ? 0 : o.sb + 64 * 6144}; });
  }
  const u32 fwo = flag_off(FW, 0, 0) + (u32)((((lane >> 1) & 15) * 16 + c.cj) * 128 + 8 * (lane & 1));
  u32x2 fwv;
  u32 lp[8];
#pragma unroll
  for (int f = 0; f < 8; ++f) lp[f] = frag_off(c, f, lane) - (u32)((f & 3) * 1024);
  {
    const rsrc_t rb = mk_rsrc((const char*)P.wo - 4096, 1024u * 2048u + 4096u);
    u32 vb[4];
    src_g2(c, lw, vb);
    static_assert(6 * 8192 >= P1_ST && G2_PRE * 8192 <= 2 * P1_ST, "B slots 6.. lie in the stage P1 frees first");
    bar_n();
    pre_g2<16>(c, lw, rb, vb, 6, G2_PRE);
    MK_WAITV(4 * (G2_PRE - 6)); bar_n();
    bar_n();
    pre_g2<16>(c, lw, rb, vb, 0, 6);
    get_tag(c);
    const AtPre ap = attention_pre(c);
    bar_h();
    attention(c, ap);
    bar_n();
    pre_g2<16>(c, lw, rb, vb, G2_PRE, G2_NB);
    if (lw == 0) await_panel<9>(c, FO, 0, 8);
    bar_h();
    fwv = load_g2<16, true>(c, lw, lp, WS_O + (u32)(c.rp * 32) * GA, rb, vb, fwo);
  }
  {
    const rsrc_t rs = mk_rsrc((const char*)P.wsp - 4096, 2048u * 4096u + 4096u);
    const u32 bh = WS_H + (u32)(c.rp * 32 + lw) * GA, bw = WS_WINP + (u32)(c.cj * 64 + lw) * GB, bz = WS_Z2N + (u32)(c.cj * 64 + lw) * GB;
    const u32 da = c.lds + (u32)(lw * 8192), db = c.lds + (u32)(G3_B + lw * 7168);
    auto A3 = [&](int t) { issue_f<8>(c.rws, da + (u32)((t % 6) * 16384), lp, bh + (u32)(t < 16 ? t : t - 22) * 2 * GA); };
    auto B3 = [&](int t) { issue_f<7>(c.rws, db + (u32)((t & 3) * 14336), lp, bw + (u32)(t < 16 ? t : 16 + ((t + 10) & 15)) * 2 * GB); };
    u32 vs[8];
    const u32 s0 = (u32)((128 * c.rp + 16 * lw + 2 * (lane >> 3)) * 4096 + (((lane & 7) ^ (4 * lw + (lane >> 4))) * 16) + 4096);
#pragma unroll
    for (int m = 0; m < 8; ++m) vs[m] = s0 + (u32)((32 * (m >> 1) + (m & 1)) * 4096 - (m & 1) * 2048);
    bar_h();
    poll_check(c, fwo, fwv);
    B3(0); B3(1); B3(2); B3(3);
    bar_h();
    A3(0); A3(1); MK_WAITV(8); bar_n();
    A3(2); A3(3); MK_WAITV(16); bar_n();
    A3(4); A3(5); A3(6); B3(4); MK_WAITV(39);
    bar_n(); A3(7); B3(5);
    auto fF = [&](auto ic, const Off& o) { constexpr int s = decltype(ic)::v; glds<(s & 3) * 1024, SC1>(c.rws, o.da + (s >> 2) * 4096, lp[s], o.sa); };
    auto fB = [&](auto ic, const Off& o) { constexpr int s = decltype(ic)::v; glds<(s & 3) * 1024, SC1>(c.rws, o.db + (s >> 2) * 4096, lp[s], o.sb); };
    auto n3 = [&](const Off& o) { return Off{o.da == da + 5 * 16384 ? da : o.da + 16384, o.db == db + 3 * 14336 ? db : o.db + 14336, o.sa + 2 * GA, o.sb + 2 * GB}; };
    MK_WAITV(46);
    steady2<15, 8, 30>(lw, 8, Off{da + 2 * 16384, db + 2 * 14336, bh + 16 * GA, bw + 12 * GB}, fF, fB, n3);
    bar_n(); B3(14); MK_WAITV(22);
    bar_n(); B3(15); MK_WAITV(14);
    bar_n(); B3(16); MK_WAITV(14);
    bar_n(); B3(17); MK_WAITV(14);
    bar_n(); B3(18); MK_WAITV(14);
    bar_n(); B3(19); MK_WAITV(14);
    bar_n(); A3(22); B3(20); MK_WAITV(22);
    bar_n(); A3(23); B3(21);
    MK_WAITV(30);
    steady2<15, 8, 30>(lw, 8, Off{da, db + 2 * 14336, bh + 4 * GA, bw + 32 * GB}, fF, fB, n3);
    bar_n(); B3(30); MK_WAITV(22);
    bar_n(); B3(31); MK_WAITV(14);
    const u32 dr = c.lds + (u32)(lw * 1024);
    auto A4 = [&](int t) { issue_v2<8, 0>(rs, dr + (u32)(((t + 2) & 3) * 16384), vs, (u32)(t * 128)); };
    auto B4 = [&](int t) { issue_f<7>(c.rws, db + (u32)((t & 3) * 14336), lp, bz + (u32)t * 2 * GB); };
    bar_n(); A4(0); MK_WAITV(15);
    bar_n(); B4(0); A4(1); B4(1);
    MK_WAITV(30);
    steady2<15, 8, 30>(lw, 30, Off{dr, db + 2 * 14336, 2 * 128, bz + 4 * GB},
        [&](auto ic, const Off& o) { constexpr int s = decltype(ic)::v; glds<(s & 1) * 2048, 0>(rs, o.da + (s >> 1) * 4096, vs[s], o.sa); }, fB,
        [&](const Off& o) { return Off{o.da == dr + 3 * 16384 ? dr : o.da + 16384, o.db == db + 3 * 14336 ? db : o.db + 14336, o.sa + 128, o.sb + 2 * GB}; });
    bar_n();
    MK_WAITV(15); bar_n(); MK_WAITV(0); bar_n();
    bar_n();
  }
  {
    const rsrc_t rb = mk_rsrc((const char*)P.wout - 4096, 1792u * 2048u + 4096u);
    u32 vb[4];
    src_g2(c, lw, vb);
    bar_h();
    pre_g2<28>(c, lw, rb, vb, 0, G2_NB);
    bar_h();
    load_g2<28, false>(c, lw, lp, WS_U + (u32)(c.rp * 56) * GA, rb, vb, 0);
  }
  asm volatile(".Lmk_end_l:");
}

__device__ __forceinline__ void bars(int n) {
#pragma unroll 1
  for (int i = 0; i < n; ++i) bar_n();
}
__device__ __forceinline__ void sumsq2(float& s, float& q, u32 u) {
  float a, b;
  asm("v_lshlrev_b32 %2, 16, %4\n\tv_and_b32 %3, 0xffff0000, %4\n\tv_add_f32 %0, %0, %2\n\tv_fmac_f32 %1, %2, %2\n\tv_add_f32 %0, %0, %3\n\tv_fmac_f32 %1, %3, %3"
      : "+v"(s), "+v"(q), "=&v"(a), "=&v"(b) : "v"(u));
}
__device__ __forceinline__ void warm_slow(const Params& P, const Ctx& c, int row0, int nrow) {
  const int cslow = (c.cj & 8) + ((3 - (int)((size_t)P.wqkv >> 7)) & 7), i = c.lane / 3, g = c.lane - 3 * i;
  if (32 * i < nrow) touch(c, mk_rsrc(P.wqkv, 1024u * 6144u), (u32)(((row0 + 256 * (c.rp >> 2) + (blockIdx.x >> 3) + 32 * i) & 1023) * 6144 + g * 2048 + cslow * 128), 0);
}
__device__ __forceinline__ void load_x(const Params& P, const Ctx& c, int h, u32x4 (&xr)[4]) {
  const rsrc_t rx = mk_rsrc(P.x, 2048u * 2048u);
#pragma unroll
  for (int i = 0; i < 4; ++i)
    xr[i] = __builtin_amdgcn_raw_buffer_load_b128(rx, (128 * c.rp + 8 * c.cj + 4 * (c.wid - 6) + i) * 2048 + ((c.lane * 16 + h * 1024 + 512 * (c.rp >> 2)) & 2047), 0, 0);
}
__device__ __forceinline__ void worker(const Params& P, Ctx& c, u32x4 (&xr0)[4]) {
  refresh(c);
  const int v = c.wid - 6, lane = c.lane;
  const u32 stg = c.lds + WSTG + (u32)(v * 14336);
  u32x4 wr[14];
  {
    u32x4 xr1[4];
    const u32x2 ep = __builtin_amdgcn_raw_buffer_load_b64(c.rws, (int)WS_EPOCH, 0, SC1);
    bar_n();
    { const u64 t = (((u64)ep[1] << 32) | ep[0]) + 1;
      c.tag_lo = __builtin_amdgcn_readfirstlane((u32)t); c.tag_hi = __builtin_amdgcn_readfirstlane((u32)(t >> 32));
      if (v == 1 && lane < 5) lds_st<u32>(c.lds + T_TAG + (u32)(4 * lane), lane == 0 ? c.tag_lo : lane == 1 ? c.tag_hi : 0u); }
    bars(2);
    load_x(P, c, 1, xr1);
    bars(3);
    float sm = 0.f, sr = 0.f;
#pragma unroll
    for (int i = 0; i < 4; ++i) {
      float s = 0.f, q = 0.f;
#pragma unroll
      for (int h = 0; h < 2; ++h)
#pragma unroll
        for (int d = 0; d < 4; ++d) { u32 u = h ? xr1[i][d] : xr0[i][d]; sumsq2(s, q, u); }
      const float mean = sumg(sum16(s)) * (1.0f / 1024.0f);
      const float rs = __builtin_amdgcn_rsqf(fmaxf(sumg(sum16(q)) * (1.0f / 1024.0f) - mean * mean, 0.f) + 1e-5f);
      sm = lane == i ? mean : sm; sr = lane == i ? rs : sr;
      asm volatile("" : "+v"(sm), "+v"(sr));
      if (i == 3) { if (lane < 4) st128<SC1>(tag_pair(c, sm, sr), c.rws, (int)(WS_XSTAT + (u32)((128 * c.rp + 8 * c.cj + 4 * v + lane) * 16)), 0); }
      if (i == 0 && v == 1) warm_slow(P, c, 448, 320);
      bar_n();
    }
    if (v == 0) warm_slow(P, c, 768, 256);
    else touch_code(c);
    bars(2);
    u32x4 xg[2];
    u32 xo[2];
#pragma unroll
    for (int i = 0; i < 2; ++i) {
      const int rho = min(72 * v + 64 * i + lane, 72 * v + 71), m = rho >> 4, sl = rho & 15;
      const int il = m == 0 ? (2 * (sl & 7) + (sl >> 3)) : 16 + 32 * ((m - 1) >> 1) + 2 * sl + ((m - 1) & 1);
      xo[i] = WS_XSTAT + (u32)(max(128 * c.rp - 16 + il, 0) * 16);
      xg[i] = __builtin_amdgcn_raw_buffer_load_b128(c.rws, (int)xo[i], 0, SC1);
    }
    bars(2);
    if (!__all((tag_bad(c, xg[0]) | tag_bad(c, xg[1])) == 0)) {
#pragma unroll 1
      for (int sp = 0; sp < SPIN_CAP; ++sp) {
        asm volatile("" ::: "memory");
#pragma unroll
        for (int i = 0; i < 2; ++i) xg[i] = __builtin_amdgcn_raw_buffer_load_b128(c.rws, (int)xo[i], 0, SC1);
        if (__all((tag_bad(c, xg[0]) | tag_bad(c, xg[1])) == 0)) break;
      }
    }
#pragma unroll
    for (int i = 0; i < 2; ++i) lds_st<u32x2>(c.lds + T_XST + (u32)(min(72 * v + 64 * i + lane, 72 * v + 71) * 8), (u32x2){xg[i][0], xg[i][2]});
    bar_h();
    {
      const rsrc_t rw = mk_rsrc(P.win, 1024u * 7168u);
      int k = lane / 14, ch = lane - 14 * k;
      u32 vo = (u32)((64 * c.rp + k) * 7168 + ((v == 0 ? 1792 : 0) + 112 * c.cj) * 2 + ch * 16);
#pragma unroll
      for (int j = 0; j < 14; ++j) {
        wr[j] = __builtin_amdgcn_raw_buffer_load_b128(rw, (int)vo, 0, 0);
        ch += 8; const bool wrap = ch >= 14; ch -= wrap ? 14 : 0; vo += wrap ? (u32)(5 * 7168 - 96) : (u32)(4 * 7168 + 128);
        if (j == 6) bar_n();
      }
    }
    bar_n();
  }
  const AtPre ap = attention_pre(c);
  bar_n();
  attention(c, ap);
  bar_n();
#pragma unroll
  for (int j = 0; j < 14; ++j) lds_st<u32x4>(stg + (u32)(j * 1024 + lane * 16), wr[j]);
  MK_LGKM0();
  {
    const int odd = c.cj & 1;
    const u32 ta = stg + (u32)((8 * c.g + c.q) * 224);
    const u32 tp = ta + (u32)(((odd ? 16 : 0) + 8 * c.p) * 2), ts = ta + (u32)(((odd ? 0 : 96) + 4 * c.p) * 2);
    const u32 wo_ = WS_WINP + (u32)((c.cj * 2 + v) * 32 + 2 * c.rp) * GB;
    float cs = 0.f;
    sfor<7>([&](auto nc) {
      constexpr int n = decltype(nc)::v, o = n < 6 ? 64 * (n >> 1) + 8 * (n & 1) : 0;
      bf16x8 f2[2];
      lds_tr4<o, o + 896, o + 7168, o + 7168 + 896>(n < 6 ? tp : ts, n < 6 ? tp : ts, f2[0], f2[1]);
      f32x2 s2 = {0.f, 0.f};
#pragma unroll
      for (int kl = 0; kl < 2; ++kl) {
        const u32x4 fr = __builtin_bit_cast(u32x4, f2[kl]);
        st128<SC1>(fr, c.rws, (int)frag_off(c, n, lane), (int)(wo_ + kl * GB));
#pragma unroll
        for (int d = 0; d < 4; ++d) s2 += (f32x2){bf_lo(fr[d]), bf_hi(fr[d])};
      }
      const float t = sumg(s2[0] + s2[1]);
      if constexpr (n < 4) cs = (c.g == n) ? t : cs;
      if constexpr (n == 3) __builtin_amdgcn_raw_buffer_store_b32(asu(cs), c.rws, (int)(WS_CS + (u32)(((c.rp * 16 + c.cj) * 256 + 112 * v + lane) * 4)), 0, SC1);
      if constexpr (n >= 4) cs = (c.g == n - 4) ? t : cs;
    });
    if (lane < 48) __builtin_amdgcn_raw_buffer_store_b32(asu(cs), c.rws, (int)(WS_CS + (u32)(((c.rp * 16 + c.cj) * 256 + 112 * v + 64 + lane) * 4)), 0, SC1);
  }
  bar_h();
  bars(4);
  publish(c, FW, v);
  bars(6);
  if (v == 1) touch_code(c);
  bars(7);
  bar_n();
  const int hi = lane >> 5, l5 = lane & 31;
  u32x4 cp[16];
  if (v == 0) await_panel<14>(c, FHH, 1, 4);
  else {
    poll64(c, flag_off(FW, 0, 0) + (u32)((((lane >> 1) & 15) * 16 + c.cj) * 128 + 8 * (lane & 1)));
#pragma unroll
    for (int j = 0; j < 16; ++j)
      cp[j] = __builtin_amdgcn_raw_buffer_load_b128(c.rws, (int)(WS_CS + (u32)(((hi * 16 + c.cj) * 256 + 112 * (j >> 3) + 4 * min(l5, 27)) * 4)), (j & 7) * 32768, SC1);
  }
  bar_n();
  {
    auto fold = [](float x) { const u32 u = asu(x); auto r = __builtin_amdgcn_permlane32_swap(u, u, false, false); return asf((u32)r[0]) + asf((u32)r[1]); };
    const u32 fso = flag_off(FHS, 0, 0) + (u32)((c.rp * 16 + (lane & 15)) * 128);
    bars(5);
    if (v == 1 && lane == 0) __builtin_amdgcn_raw_buffer_store_b64((u32x2){c.tag_lo, c.tag_hi}, c.rws, (int)flag_off(FHS, c.rp, c.cj), 0, SC1);
    bars(3);
    const u32x2 fsv = poll_issue(c, fso);
    bars(2);
    poll_check(c, fso, fsv);
    u32x4 hp[8];
#pragma unroll
    for (int j = 0; j < 8; ++j) {
      hp[j] = __builtin_amdgcn_raw_buffer_load_b128(c.rws, (int)(WS_HSTAT + (u32)(((c.rp * 16 + hi) * 128 + 64 * v + 2 * l5) * 8)), j * 2048, SC1);
    }
#pragma unroll
    for (int h = 0; h < 2; ++h) {
      bar_n();
      if (v == 1) {
        float c0 = 0.f, c1 = 0.f, c2 = 0.f, c3 = 0.f;
#pragma unroll
        for (int j = 8 * h; j < 8 * h + 8; ++j) { add2(c0, c1, cp[j][0], cp[j][1]); add2(c2, c3, cp[j][2], cp[j][3]); }
        lds_st<f32x4>(c.lds + T_CS + (u32)((112 * h + 4 * min(l5, 27)) * 4), (f32x4){fold(c0), fold(c1), fold(c2), fold(c3)});
      }
    }
    if (v == 0) touch_code(c);
    bars(2);
    float s0 = 0.f, q0 = 0.f, s1 = 0.f, q1 = 0.f;
#pragma unroll
    for (int j = 0; j < 8; ++j) { add2(s0, q0, hp[j][0], hp[j][1]); add2(s1, q1, hp[j][2], hp[j][3]); }
    {
      s0 = fold(s0); q0 = fold(q0); s1 = fold(s1); q1 = fold(q1);
      const float sx = hi ? s1 : s0, qx = hi ? q1 : q0;
      const float mean = sx * (1.0f / 1024.0f);
      const float rs = __builtin_amdgcn_rsqf(fmaxf(qx * (1.0f / 1024.0f) - mean * mean, 0.f) + 1e-5f);
      lds_st<float>(c.lds + T_HM + (u32)((64 * v + 2 * l5 + hi) * 4), mean);
      lds_st<float>(c.lds + T_HR + (u32)((64 * v + 2 * l5 + hi) * 4), rs);
    }
    bar_n();
    bar_h(); bar_n();
#pragma unroll
    for (int h = 0; h < 2; ++h)
      touch(c, mk_rsrc(P.wsp, 2048u * 4096u), (u32)((lane >> 4) * 4096 + (lane & 15) * 128), blockIdx.x * 32768u + v * 16384u + h * 2048u);
    bars(SWEEP_T - 16);
    u32x4 zp[16];
    auto sweep = [&]() {
#pragma unroll
      for (int j = 0; j < 16; ++j) zp[j] = __builtin_amdgcn_raw_buffer_load_b128(c.rws, (int)(WS_ZSTAT + (u32)((c.rp * 2048 + 64 * v + lane) * 16)), j * 2048, SC1);
    };
    sweep();
    bars(NORM_T - 1 - SWEEP_T);
    float zs = 0.f, zq = 0.f;
    u32 bad = 0;
    auto half = [&](int j0) {
#pragma unroll
      for (int j = j0; j < j0 + 8; ++j) {
        u32 t1, t2;
        asm("v_bitop3_b32 %3, %5, %6, %8 bitop3:0x96\n\tv_bitop3_b32 %4, %7, %9, %8 bitop3:0x69\n\tv_add_f32 %0, %0, %9\n\tv_add_f32 %1, %1, %6\n\tv_or3_b32 %2, %2, %3, %4"
            : "+v"(zs), "+v"(zq), "+v"(bad), "=&v"(t1), "=&v"(t2) : "v"(zp[j][1]), "v"(zp[j][2]), "v"(zp[j][3]), "s"(c.tag_lo), "v"(zp[j][0]));
      }
    };
    half(0);
    asm volatile("" : "+v"(zs), "+v"(zq), "+v"(bad));
    bar_n();
    half(8);
    if (!__all(bad == 0)) {
#pragma unroll 1
      for (int sp = 0; sp < SPIN_CAP; ++sp) {
        asm volatile("" ::: "memory");
        sweep();
        zs = 0.f; zq = 0.f; bad = 0;
        half(0); half(8);
        if (__all(bad == 0)) break;
      }
    }
    const float zm = zs * (1.0f / 1792.0f);
    const float zr = __builtin_amdgcn_rsqf(fmaxf(zq * (1.0f / 1792.0f) - zm * zm, 0.f) + 1e-5f);
    lds_st<float>(c.lds + T_ZM + (u32)((64 * v + lane) * 4), -zm * zr);
    lds_st<float>(c.lds + T_ZR + (u32)((64 * v + lane) * 4), zr);
    bar_h();
    bars(FLAG_T - NORM_T);
    if (v == 1 && lane == 0) __builtin_amdgcn_raw_buffer_store_b64((u32x2){c.tag_lo, c.tag_hi}, c.rws, (int)flag_off(FZ, c.rp, c.cj), 0, SC1);
    bars(26 - FLAG_T);
    const u32 fo = flag_off(FZ, 0, 0) + (u32)(((lane & 15) * 16 + c.cj) * 128);
    u32x2 fv = {0, 0};
    if (v == 0) fv = poll_issue(c, fo);
    bars(2);
    if (v == 0) poll_check(c, fo, fv);
    bars(24);
    if (v == 1) touch_code(c);
    if (v == 0) {
      const int cs5 = (c.cj & 8) + ((3 - (int)((size_t)P.wout >> 7)) & 7);
      if (lane < 56) touch(c, mk_rsrc(P.wout, 1792u * 2048u), (u32)(((blockIdx.x >> 3) + 32 * lane) * 2048 + cs5 * 128), 0);
    }
    bars(11);
  }
  bar_n();
  if (v == 0) await_panel<16>(c, FU, 2, 4);
  bar_n();
  asm volatile(".Lmk_end_w:");
}

__global__ __launch_bounds__(512, 2) void fused_block(Params P) {
  extern __shared__ __attribute__((aligned(16))) char smem[];
  asm volatile(".Lmk_code_begin:");
  Ctx c;
  c.lane = threadIdx.x & 63;
  c.wid = __builtin_amdgcn_readfirstlane(threadIdx.x >> 6);
  const int xcd = blockIdx.x & 7, idx = blockIdx.x >> 3;
  c.rp = (xcd & 3) * 4 + (idx & 3);
  c.cj = (xcd >> 2) * 8 + (idx >> 2);
  c.r16 = c.lane & 15; c.g = c.lane >> 4; c.q = (c.lane >> 2) & 3; c.p = c.lane & 3;
  c.lds = (u32)(size_t)(LAS char*)smem;
  c.rws = mk_rsrc(P.ws, WS_END);
  c.skip = 7 - ((3 - (int)((size_t)P.ws >> 7)) & 7);
  c.tag_lo = 0; c.tag_hi = 0;
  u32x4 xr0[4];
  if (c.wid == 7) touch_code(c);
  if (c.wid >= 6) {
    __builtin_amdgcn_s_sleep(5);
    if (c.wid == 6) warm_slow(P, c, 64, 384);
    load_x(P, c, 0, xr0);
  }
  if (__builtin_expect((c.wid >> 1) == 2, 1)) loader(P, c); else if (c.wid < 4) consumer(P, c); else worker(P, c, xr0);
}

}

extern "C" void kernel_launch(void* const* d_in, const int* in_sizes, int n_in, void* d_out, int out_size, void* d_ws, size_t ws_size,
                              hipStream_t stream) {
  (void)in_sizes; (void)n_in; (void)out_size; (void)ws_size;
  mk::Params P;
  P.x = (const mk::u16*)d_in[0]; P.wqkv = (const mk::u16*)d_in[1]; P.wo = (const mk::u16*)d_in[2];
  P.win = (const mk::u16*)d_in[4]; P.wsp = (const mk::u16*)d_in[5]; P.bsp = (const mk::u16*)d_in[6]; P.wout = (const mk::u16*)d_in[7];
  P.out = (mk::u16*)d_out; P.ws = (char*)d_ws;
  (void)hipFuncSetAttribute((const void*)mk::fused_block, hipFuncAttributeMaxDynamicSharedMemorySize, mk::LDS_BYTES);
  hipLaunchKernelGGL(mk::fused_block, dim3(256), dim3(512), mk::LDS_BYTES, stream, P);
}
```

```cpp
#include <hip/hip_runtime.h>

namespace mk {

#define LAS __attribute__((address_space(3)))
typedef LAS void* lds_vp;
typedef unsigned short u16;
typedef unsigned int u32;
typedef unsigned long long u64;
typedef short bf16x8 __attribute__((ext_vector_type(8)));
typedef short bf16x4 __attribute__((ext_vector_type(4)));
typedef float f32x2 __attribute__((ext_vector_type(2)));
typedef float f32x4 __attribute__((ext_vector_type(4)));
typedef u32 u32x2 __attribute__((ext_vector_type(2)));
typedef u32 u32x4 __attribute__((ext_vector_type(4)));
typedef __bf16 bf16x2n __attribute__((ext_vector_type(2)));
typedef __amdgpu_buffer_rsrc_t rsrc_t;

constexpr u32 WS_EPOCH = 0;
constexpr u32 WS_FLAGS = 4096;
enum { FHS = 0, FW = 1, FO = 2, FHH = 3, FZ = 4, FU = 5 };
constexpr u32 WS_XSTAT = 256 * 1024;
constexpr u32 WS_HSTAT = 512 * 1024;
constexpr u32 WS_CS    = 768 * 1024;
constexpr u32 WS_ZSTAT = 1024 * 1024;
constexpr u32 GA = 10240, GB = 8192;
constexpr u32 WS_O     = 2u << 20;
constexpr u32 WS_H     = 8u << 20;
constexpr u32 WS_WINP  = 14u << 20;
constexpr u32 WS_Z2N   = 23u << 20;
constexpr u32 WS_U     = 32u << 20;
constexpr u32 WS_END   = 42u << 20;
__host__ __device__ constexpr u32 flag_off(int kind, int rp, int cj) { return WS_FLAGS + (u32)((kind * 256 + rp * 16 + cj) * 128); }

constexpr int P1_A = 144 * 128, P1_B = 192 * 128, P1_ST = P1_A + P1_B + 16;
constexpr int P1_ONES = P1_A + P1_B;
constexpr int G2_NB = 11, G2_B = 0, G2_A = G2_NB * 8192;
constexpr int WSTG = G2_A;
constexpr int G2_PRE = 10, AT_BASE = G2_PRE * 8192, AT_RS = 80, AT_BLK = 72 * AT_RS;
constexpr int G3_B = 6 * 16384;
__host__ __device__ constexpr int g3_a(int t) { return (t < 8 ? t % 4 : (t + 2) % 6) * 16384; }
__host__ __device__ constexpr int g3_b(int t) { return t == 4 || t == 5 ? t * 16384 : G3_B + (t % 4) * 14336; }
__host__ __device__ constexpr int g3_na(int p) { return p + 3 >= 16 && p + 3 < 22 ? 0 : 8; }
__host__ __device__ constexpr int g3_nb(int p) { return p + 3 < 6 || p == 29 ? 0 : p == 30 ? 14 : 7; }
constexpr int TAB = 156 * 1024;
constexpr int T_XST = TAB;
constexpr int T_ZM = TAB, T_ZR = TAB + 512, T_HM = TAB + 1152, T_HR = T_HM + 512, T_CS = T_HR + 512;
constexpr int T_TAG = T_CS + 896, T_CNT = T_TAG + 8, T_DUMMY = T_TAG + 32;
constexpr int LDS_BYTES = 160 * 1024;
static_assert(3 * P1_ST <= TAB && WSTG + 2 * 14336 <= TAB, "lds");
static_assert(AT_BASE + 12 * AT_BLK <= TAB, "lds");
static_assert(G2_A + 4 * 16384 <= TAB, "lds");
static_assert(G3_B + 4 * 14336 <= TAB, "lds");
static_assert(T_DUMMY + 256 <= LDS_BYTES, "lds");

constexpr int SWEEP_T = 18;
constexpr int NORM_T = 21;
constexpr int FLAG_T = 24;

struct Params {
  const u16* x; const u16* wqkv; const u16* wo; const u16* win; const u16* wsp; const u16* bsp; const u16* wout;
  u16* out; char* ws;
};

template <int I> struct IC { static constexpr int v = I; };
template <int N, int I = 0, class F> __device__ __forceinline__ void sfor(F&& f) { if constexpr (I < N) { f(IC<I>{}); sfor<N, I + 1>(f); } }
#define MK_WAITV(n) asm volatile("s_waitcnt vmcnt(%0)" ::"i"(n) : "memory")
#define MK_LGKM0() __builtin_amdgcn_s_waitcnt(0xC07F)
#define MK_SB() __builtin_amdgcn_sched_barrier(0)
__device__ __forceinline__ void bar_h() { asm volatile("s_waitcnt lgkmcnt(0)\n\ts_barrier" ::: "memory"); }
__device__ __forceinline__ void bar_n() { asm volatile("s_barrier" ::: "memory"); }
__device__ __forceinline__ void bar_c() { asm volatile("" ::: "memory"); __builtin_amdgcn_s_barrier(); asm volatile("" ::: "memory"); }

__device__ __forceinline__ rsrc_t mk_rsrc(const void* p, u32 bytes) { return __builtin_amdgcn_make_buffer_rsrc((void*)p, 0, bytes, 0x00020000); }
template <int OFF, int AUX> __device__ __forceinline__ void glds(rsrc_t r, u32 ldsdst, u32 voff, u32 soff) {
  __builtin_amdgcn_raw_ptr_buffer_load_lds(r, (lds_vp)(size_t)ldsdst, 16, (int)voff, (int)soff, OFF, AUX);
}
constexpr int SC1 = 16;
template <int AUX> __device__ __forceinline__ void st128(u32x4 v, rsrc_t r, int voff, int soff) {
  __builtin_amdgcn_raw_buffer_store_b128(v, r, voff, soff, AUX);
  asm volatile("s_nop 1" ::"v"(v) : "memory");
}
__device__ __forceinline__ u32 pk_bf16(float lo, float hi) { f32x2 v = {lo, hi}; return __builtin_bit_cast(u32, __builtin_convertvector(v, bf16x2n)); }
__device__ __forceinline__ float asf(u32 u) { return __builtin_bit_cast(float, u); }
__device__ __forceinline__ u32 asu(float f) { return __builtin_bit_cast(u32, f); }
__device__ __forceinline__ float bf_lo(u32 u) { return __builtin_bit_cast(float, u << 16); }
__device__ __forceinline__ float bf_hi(u32 u) { return __builtin_bit_cast(float, u & 0xffff0000u); }
template <int CTRL> __device__ __forceinline__ float dpp(float x) {
  return __builtin_bit_cast(float, __builtin_amdgcn_update_dpp(0, __builtin_bit_cast(int, x), CTRL, 0xf, 0xf, true));
}
__device__ __forceinline__ float sum16(float x) { x += dpp<0xB1>(x); x += dpp<0x4E>(x); x += dpp<0x141>(x); x += dpp<0x140>(x); return x; }
__device__ __forceinline__ float sumg(float x) {
  u32 u = __builtin_bit_cast(u32, x);
  auto a = __builtin_amdgcn_permlane16_swap(u, u, false, false);
  x = __builtin_bit_cast(float, (u32)a[0]) + __builtin_bit_cast(float, (u32)a[1]);
  u = __builtin_bit_cast(u32, x);
  auto b = __builtin_amdgcn_permlane32_swap(u, u, false, false);
  return __builtin_bit_cast(float, (u32)b[0]) + __builtin_bit_cast(float, (u32)b[1]);
}
__device__ __forceinline__ u32x4 pair_swap(u32x2 a, u32x2 b) {
  auto r0 = __builtin_amdgcn_permlane16_swap(a[0], b[0], false, false);
  auto r1 = __builtin_amdgcn_permlane16_swap(a[1], b[1], false, false);
  return (u32x4){(u32)r0[0], (u32)r1[0], (u32)r0[1], (u32)r1[1]};
}
__device__ __forceinline__ void add2(float& s, float& q, u32 a, u32 b) {
  asm("v_add_f32 %0, %0, %2\n\tv_add_f32 %1, %1, %3" : "+v"(s), "+v"(q) : "v"(a), "v"(b));
}
template <class T> __device__ __forceinline__ T lds_ld(u32 a) { return *(const LAS T*)(size_t)a; }
template <class T> __device__ __forceinline__ void lds_st(u32 a, T v) { *(LAS T*)(size_t)a = v; }
__device__ __forceinline__ bf16x4 lds_tr(u32 a) { return __builtin_amdgcn_ds_read_tr16_b64_v4i16((LAS bf16x4*)(size_t)a); }
template <int O0, int O1, int O2, int O3> __device__ __forceinline__ void lds_tr4(u32 a, u32 b, bf16x8& f0, bf16x8& f1) {
  bf16x4 r0, r1, r2, r3;
  asm volatile("ds_read_b64_tr_b16 %0, %4 offset:%6\n\tds_read_b64_tr_b16 %1, %5 offset:%7\n\tds_read_b64_tr_b16 %2, %4 offset:%8\n\tds_read_b64_tr_b16 %3, %5 offset:%9\n\ts_waitcnt lgkmcnt(0)"
               : "=&v"(r0), "=&v"(r1), "=&v"(r2), "=&v"(r3) : "v"(a), "v"(b), "i"(O0), "i"(O1), "i"(O2), "i"(O3) : "memory");
  f0 = __builtin_shufflevector(r0, r1, 0, 1, 2, 3, 4, 5, 6, 7); f1 = __builtin_shufflevector(r2, r3, 0, 1, 2, 3, 4, 5, 6, 7);
}
__device__ __forceinline__ u32 opaque(u32 x) { asm("" : "+v"(x)); return x; }

struct Ctx {
  int lane, wid, rp, cj, r16, g, q, p;
  u32 lds;
  u32 tag_lo, tag_hi;
  int skip;
  rsrc_t rws;
};
__device__ __forceinline__ u32 frag_off(const Ctx& c, int f, int p) {
  const u32 l = (u32)(8 * f + (p >> 3));
  return (l + (((l + (u32)c.skip) * 37u) >> 8)) * 128u + (u32)((p & 7) * 16);
}
__device__ __forceinline__ void refresh(Ctx& c) {
  u32 t = threadIdx.x;
  asm volatile("" : "+v"(t));
  c.lane = t & 63; c.r16 = t & 15; c.g = (t >> 4) & 3; c.q = (t >> 2) & 3; c.p = t & 3;
}
constexpr int SPIN_CAP = 1 << 17;

__device__ __forceinline__ void get_tag(Ctx& c) {
  const u32x2 tg = lds_ld<u32x2>(c.lds + T_TAG);
  c.tag_lo = __builtin_amdgcn_readfirstlane(tg[0]); c.tag_hi = __builtin_amdgcn_readfirstlane(tg[1]);
}
__device__ __forceinline__ void publish(const Ctx& c, int kind, int slot) {
  asm volatile("s_waitcnt vmcnt(0)" ::: "memory");
  if (c.lane == 0) __builtin_amdgcn_raw_buffer_store_b64((u32x2){c.tag_lo, c.tag_hi}, c.rws, (int)(flag_off(kind, c.rp, c.cj) + 8 * slot), 0, SC1);
}
template <int PRE = 0> __device__ __forceinline__ void poll64(const Ctx& c, u32 voff) {
  if (PRE) __builtin_amdgcn_s_sleep(PRE);
#pragma unroll 1
  for (int s = 0; s < SPIN_CAP; ++s) {
    asm volatile("" ::: "memory");
    const u32x2 v = __builtin_amdgcn_raw_buffer_load_b64(c.rws, (int)voff, 0, SC1);
    if (__all(v[0] == c.tag_lo && v[1] == c.tag_hi)) break;
    __builtin_amdgcn_s_sleep(1);
  }
  asm volatile("" ::: "memory");
}
__device__ __forceinline__ void publish_last(const Ctx& c, int kind, int idx, u32 n) {
  MK_WAITV(0);
  if (c.lane == 0) {
    u32 old;
    asm volatile("ds_add_rtn_u32 %0, %1, %2\n\ts_waitcnt lgkmcnt(0)" : "=&v"(old) : "v"(c.lds + T_CNT + 4 * idx), "v"(1u) : "memory");
    if (old == n - 1) __builtin_amdgcn_raw_buffer_store_b64((u32x2){c.tag_lo, c.tag_hi}, c.rws, (int)flag_off(kind, c.rp, c.cj), 0, SC1);
  }
}
template <int PRE> __device__ __forceinline__ void await_panel(const Ctx& c, int kind, int idx, u32 n) {
#pragma unroll 1
  for (int s = 0; s < (1 << 22); ++s) {
    if (__builtin_amdgcn_readfirstlane(__hip_atomic_load((LAS u32*)(size_t)(c.lds + T_CNT + 4 * idx), __ATOMIC_RELAXED, __HIP_MEMORY_SCOPE_WORKGROUP)) >= n) break;
    __builtin_amdgcn_s_sleep(1);
  }
  poll64<PRE>(c, flag_off(kind, 0, 0) + (u32)((c.rp * 16 + (c.lane & 15)) * 128));
}
__device__ __forceinline__ u32x2 poll_issue(const Ctx& c, u32 voff) { return __builtin_amdgcn_raw_buffer_load_b64(c.rws, (int)voff, 0, SC1); }
__device__ __forceinline__ void poll_check(const Ctx& c, u32 voff, u32x2 v) {
  if (!__all(v[0] == c.tag_lo && v[1] == c.tag_hi)) poll64(c, voff);
  asm volatile("" ::: "memory");
}
__device__ __forceinline__ u32x4 tag_pair(const Ctx& c, float a, float b) {
  return (u32x4){asu(a), c.tag_lo ^ asu(b), asu(b), ~c.tag_lo ^ asu(a)};
}
__device__ __forceinline__ u32 tag_bad(const Ctx& c, u32x4 v) { return (v[1] ^ v[2] ^ c.tag_lo) | (v[3] ^ v[0] ^ ~c.tag_lo); }
__device__ __forceinline__ void touch(const Ctx& c, rsrc_t r, u32 voff, u32 soff) {
  __builtin_amdgcn_raw_ptr_buffer_load_lds(r, (lds_vp)(size_t)(c.lds + T_DUMMY), 4, (int)voff, (int)soff, 0, 0);
}
__device__ __forceinline__ void touch_code(const Ctx& c) {
  {
    u64 pc0; u32 back, e0, e1, e2;
    asm volatile("s_getpc_b64 %0\n.Lmk_pc%=:\n\ts_mov_b32 %1, .Lmk_pc%=-.Lmk_code_begin\n\ts_mov_b32 %2, .Lmk_end_c-.Lmk_code_begin\n\t"
                 "s_mov_b32 %3, .Lmk_end_l-.Lmk_code_begin\n\ts_mov_b32 %4, .Lmk_end_w-.Lmk_code_begin" : "=s"(pc0), "=s"(back), "=s"(e0), "=s"(e1), "=s"(e2));
    const rsrc_t rc = mk_rsrc((const void*)((pc0 - back) & ~127ull), max(e0, max(e1, e2)));
    if (c.lane < 24) touch(c, rc, (u32)(((blockIdx.x >> 3) + 32 * c.lane) * 128), 0);
  }
}

struct Win { u32 b[3]; };
__device__ __forceinline__ Win mk_win(u32 base) { Win w; w.b[0] = opaque(base); w.b[1] = opaque(base + 65536u); w.b[2] = opaque(base + 131072u); return w; }
template <int OFF> __device__ __forceinline__ u32 wad(const Win& w) { return w.b[OFF >> 16] + (u32)(OFF & 0xffff); }

template <int KS, int MT, int NT> struct Set { bf16x8 a[KS][MT]; bf16x8 b[KS][NT]; bf16x4 t[KS][NT]; };

__device__ __forceinline__ void mfma_acc(f32x4& c, const bf16x8& a, const bf16x8& b) {
  asm volatile("v_mfma_f32_16x16x32_bf16 %0, %1, %2, %0" : "+v"(c) : "v"(a), "v"(b));
}
__device__ __forceinline__ void mfma_in() { MK_SB(); asm volatile("s_nop 1"); MK_SB(); }
__device__ __forceinline__ void mfma_out() { MK_SB(); asm volatile("s_nop 7\n\ts_nop 1"); MK_SB(); }
template <int KS, int MT, int NT, bool SWAP, int NRD, class GAP, class RD>
__device__ __forceinline__ void part(f32x4 (&acc)[MT][NT], const Set<KS, MT, NT>& X, GAP, RD&& rd) {
  constexpr int NMF = KS * MT * NT;
  MK_SB();
  sfor<NMF>([&](auto ic) {
    constexpr int i = decltype(ic)::v, ks = i / (MT * NT), m = (i / NT) % MT, n = i % NT;
    if constexpr (SWAP) mfma_acc(acc[m][n], X.b[ks][n], X.a[ks][m]); else mfma_acc(acc[m][n], X.a[ks][m], X.b[ks][n]);
    MK_SB();
    sfor<NRD>([&](auto kc) { if constexpr (GAP::gap(decltype(kc)::v) == i) rd(kc); });
    if constexpr (i == NMF - 2) MK_LGKM0();
    MK_SB();
  });
}
struct Gap1 { static constexpr int gap(int k) { return k; } };
struct GapG2 { static constexpr int gap(int k) { return k < 2 ? k : (k < 10 ? 2 + (k - 2) / 2 : (k < 12 ? k - 4 : 8 + (k - 12) / 2)); } };

struct AdP1 { Win a[2], a0[2], b[3]; };
template <int ST, int KSI, int K> __device__ __forceinline__ void rd_p1(const AdP1& ad, Set<1, 9, 3>& Y) {
  constexpr int SB = ST * P1_ST;
  if constexpr (K == 0) Y.a[0][0] = lds_ld<bf16x8>(wad<SB>(ad.a0[KSI]));
  else if constexpr (K <= 6) {
    constexpr int n = (K - 1) >> 1, h = (K - 1) & 1;
    const bf16x4 v = lds_tr(wad<SB + P1_A + 4096 * KSI + 512 * h>(ad.b[n]));
    if constexpr (h == 0) Y.t[0][n] = v; else Y.b[0][n] = __builtin_shufflevector(Y.t[0][n], v, 0, 1, 2, 3, 4, 5, 6, 7);
  } else { constexpr int m = K - 6; Y.a[0][m] = lds_ld<bf16x8>(wad<SB + m * 2048>(ad.a[KSI])); }
}
struct AdG3 { Win af, ar[2], b; };
template <int T, int KSI, int K> __device__ __forceinline__ void rd_g3(const AdG3& ad, Set<1, 2, 7>& Y) {
  constexpr int SA = g3_a(T), SB = g3_b(T);
  if constexpr (K == 0 || K == 8) {
    constexpr int m = K == 0 ? 0 : 1;
    if constexpr (T >= 32) Y.a[0][m] = lds_ld<bf16x8>(wad<SA + m * 2048>(ad.ar[KSI]));
    else Y.a[0][m] = lds_ld<bf16x8>(wad<SA + KSI * 8192 + m * 1024>(ad.af));
  } else { constexpr int n = K - 1; Y.b[0][n] = lds_ld<bf16x8>(wad<SB + (KSI * 7 + n) * 1024>(ad.b)); }
}
struct AdG2 { Win a, b[4]; };
template <int T, int K> __device__ __forceinline__ void rd_g2(const AdG2& ad, Set<2, 2, 4>& Y) {
  constexpr int ks = K / 10, k = K % 10;
  if constexpr (k < 2) Y.a[ks][k] = lds_ld<bf16x8>(wad<G2_A + (T % 4) * 16384 + ks * 8192 + k * 1024>(ad.a));
  else {
    constexpr int n = (k - 2) >> 1, h = (k - 2) & 1;
    const bf16x4 v = lds_tr(wad<G2_B + (T % G2_NB) * 8192 + 4096 * ks + 512 * h>(ad.b[n]));
    if constexpr (h == 0) Y.t[ks][n] = v; else Y.b[ks][n] = __builtin_shufflevector(Y.t[ks][n], v, 0, 1, 2, 3, 4, 5, 6, 7);
  }
}
__device__ __forceinline__ u32 tr64_lane(const Ctx& c, int strip) {
  const int f = (c.q >> 1) | ((c.g & 1) << 1);
  return (u32)((strip >> 2) * 8192 + 128 * (8 * c.g + c.q) + 16 * ((2 * (strip & 3) + (c.p >> 1)) ^ (2 * f)) + 8 * (c.p & 1));
}

template <int ST, bool LAST> __device__ __forceinline__ void tile_p1(f32x4 (&acc)[9][3], Set<1, 9, 3>& X, Set<1, 9, 3>& Y, const AdP1& ad) {
  part<1, 9, 3, true, 15>(acc, X, Gap1{}, [&](auto kc) { rd_p1<ST, 1, decltype(kc)::v>(ad, Y); });
  bar_c();
  part<1, 9, 3, true, LAST ? 0 : 15>(acc, Y, Gap1{}, [&](auto kc) { rd_p1<(ST + 1) % 3, 0, decltype(kc)::v>(ad, X); });
}
template <int T, class H>
__device__ __forceinline__ void tile_g3(f32x4 (&acc)[2][7], Set<1, 2, 7>& X, Set<1, 2, 7>& Y, const AdG3& ad, H&& hook) {
  part<1, 2, 7, (T >= 16), 9>(acc, X, Gap1{}, [&](auto kc) { rd_g3<T, 1, decltype(kc)::v>(ad, Y); });
  hook();
  MK_SB();
  bar_c();
  part<1, 2, 7, (T >= 16), T < 63 ? 9 : 0>(acc, Y, Gap1{}, [&](auto kc) { rd_g3<T + 1, 0, decltype(kc)::v>(ad, X); });
}
template <int NT, bool LASTBAR> __device__ __forceinline__ void gemm_g2(f32x4 (&acc)[2][4], const Ctx& c) {
  AdG2 ad;
  ad.a = mk_win(c.lds + (u32)(c.wid * 2048 + c.lane * 16));
#pragma unroll
  for (int n = 0; n < 4; ++n) ad.b[n] = mk_win(c.lds + tr64_lane(c, n));
  Set<2, 2, 4> X, Y;
  bar_c();
  sfor<20>([&](auto kc) { rd_g2<0, decltype(kc)::v>(ad, X); });
  MK_LGKM0();
  sfor<NT>([&](auto tc) {
    constexpr int t = decltype(tc)::v;
    if constexpr (t + 1 < NT || LASTBAR) bar_c();
    part<2, 2, 4, true, (t + 1 < NT) ? 20 : 0>(acc, (t & 1) ? Y : X, GapG2{}, [&](auto kc) { rd_g2<t + 1, decltype(kc)::v>(ad, (t & 1) ? X : Y); });
  });
  mfma_out();
}

struct AtPre { u32 q, k0, k1, v0, v1, o[2]; u64 ok[8]; };
__device__ __forceinline__ AtPre attention_pre(const Ctx& c) {
  const int hh = c.wid & 1, T = c.wid >> 1;
  const int lo = (c.rp == 0 && T == 0) ? 8 : 0;
  const u32 bh = c.lds + AT_BASE + (u32)(hh * 2 * AT_BLK);
  AtPre a;
  a.q = bh + (u32)((8 + 16 * T + c.r16) * AT_RS + 16 * c.g);
  a.k0 = bh + (u32)(4 * AT_BLK + (16 * T + c.r16) * AT_RS + 16 * c.g);
  a.k1 = bh + (u32)(4 * AT_BLK + min(16 * T + 16 + c.r16, 71) * AT_RS + 16 * c.g);
  const int v0 = 16 * T + 4 * c.g + c.q;
  a.v0 = bh + (u32)(8 * AT_BLK + v0 * AT_RS + 8 * c.p);
  a.v1 = bh + (u32)(8 * AT_BLK + min(v0 + 16, 71) * AT_RS + 8 * c.p);
  const int G = ((c.g & 1) << 1) | (c.g >> 1);
#pragma unroll
  for (int par = 0; par < 2; ++par) a.o[par] = WS_O + (u32)(c.rp * 32 + 2 * c.cj + hh) * GA + frag_off(c, 2 * T + par, 16 * G + c.r16);
#pragma unroll
  for (int j = 0; j < 8; ++j) {
    const int ak = 16 * (j >> 2) + 4 * c.g + (j & 3), w = 8 + c.r16 - ak;
    a.ok[j] = __ballot((w >= 0) && (w <= 7) && (ak >= lo));
  }
  asm volatile("" : "+v"(a.q), "+v"(a.k0), "+v"(a.k1), "+v"(a.v0), "+v"(a.v1), "+v"(a.o[0]), "+v"(a.o[1]));
  asm volatile("" : "+s"(a.ok[0]), "+s"(a.ok[1]), "+s"(a.ok[2]), "+s"(a.ok[3]), "+s"(a.ok[4]), "+s"(a.ok[5]), "+s"(a.ok[6]), "+s"(a.ok[7]));
  return a;
}
__device__ __forceinline__ float vmax(float a, float b) { return __builtin_elementwise_maximum(a, b); }
__device__ __forceinline__ float vmax3(float a, float b, float d) { return vmax(vmax(a, b), d); }
__device__ __forceinline__ float vsel(u64 m, float a, float b) { return __builtin_amdgcn_inverse_ballot_w64(m) ? a : b; }
__device__ __forceinline__ void attention(const Ctx& c, const AtPre& a) {
  const float sc = 0.17677669529663687f * 1.4426950408889634f;
  const f32x4 zero = {0.f, 0.f, 0.f, 0.f};
  bf16x8 qf[2], kf[2][2], vf[2][2];
#pragma unroll
  for (int par = 0; par < 2; ++par) {
    kf[par][0] = lds_ld<bf16x8>(a.k0 + par * AT_BLK); qf[par] = lds_ld<bf16x8>(a.q + par * AT_BLK); kf[par][1] = lds_ld<bf16x8>(a.k1 + par * AT_BLK);
  }
  bf16x4 r[8];
  asm volatile("ds_read_b64_tr_b16 %0, %8\n\tds_read_b64_tr_b16 %1, %9\n\tds_read_b64_tr_b16 %2, %8 offset:32\n\tds_read_b64_tr_b16 %3, %9 offset:32\n\t"
               "ds_read_b64_tr_b16 %4, %8 offset:%10\n\tds_read_b64_tr_b16 %5, %9 offset:%10\n\tds_read_b64_tr_b16 %6, %8 offset:%11\n\tds_read_b64_tr_b16 %7, %9 offset:%11"
               : "=&v"(r[0]), "=&v"(r[1]), "=&v"(r[2]), "=&v"(r[3]), "=&v"(r[4]), "=&v"(r[5]), "=&v"(r[6]), "=&v"(r[7])
               : "v"(a.v0), "v"(a.v1), "i"(AT_BLK), "i"(AT_BLK + 32));
  MK_SB();
  f32x4 s[2][2];
#pragma unroll
  for (int par = 0; par < 2; ++par)
#pragma unroll
    for (int k2 = 0; k2 < 2; ++k2) s[par][k2] = __builtin_amdgcn_mfma_f32_16x16x32_bf16(kf[par][k2], qf[par], zero, 0, 0, 0);
  float e[2][8], mx[2];
  const float ninf = -3.0e38f;
#pragma unroll
  for (int par = 0; par < 2; ++par) {
#pragma unroll
    for (int j = 0; j < 8; ++j) { const float t = s[par][j >> 2][j & 3]; e[par][j] = vsel(a.ok[j], t, ninf); }
    mx[par] = vmax3(vmax3(e[par][0], e[par][1], e[par][2]), vmax3(e[par][3], e[par][4], e[par][5]), vmax(e[par][6], e[par][7]));
  }
  {
    auto p = __builtin_amdgcn_permlane16_swap(asu(mx[0]), asu(mx[1]), false, false);
    const float m = vmax(asf((u32)p[0]), asf((u32)p[1]));
    auto q = __builtin_amdgcn_permlane32_swap(asu(m), asu(m), false, false);
    const float t = vmax(asf((u32)q[0]), asf((u32)q[1]));
    auto u = __builtin_amdgcn_permlane16_swap(asu(t), asu(t), false, false);
    mx[0] = asf((u32)u[0]) * -sc; mx[1] = asf((u32)u[1]) * -sc;
  }
  asm volatile("s_waitcnt lgkmcnt(0)" : "+v"(r[0]), "+v"(r[1]), "+v"(r[2]), "+v"(r[3]), "+v"(r[4]), "+v"(r[5]), "+v"(r[6]), "+v"(r[7]));
  const bf16x8 ones = __builtin_bit_cast(bf16x8, (u32x4){0x3f803f80u, 0x3f803f80u, 0x3f803f80u, 0x3f803f80u});
  f32x4 o[2][2], sum[2];
#pragma unroll
  for (int par = 0; par < 2; ++par) {
    vf[par][0] = __builtin_shufflevector(r[4 * par], r[4 * par + 1], 0, 1, 2, 3, 4, 5, 6, 7);
    vf[par][1] = __builtin_shufflevector(r[4 * par + 2], r[4 * par + 3], 0, 1, 2, 3, 4, 5, 6, 7);
#pragma unroll
    for (int j = 0; j < 8; ++j) e[par][j] = __builtin_amdgcn_exp2f(__builtin_fmaf(e[par][j], sc, mx[par]));
    const bf16x8 pf = __builtin_bit_cast(bf16x8, (u32x4){pk_bf16(e[par][0], e[par][1]), pk_bf16(e[par][2], e[par][3]), pk_bf16(e[par][4], e[par][5]), pk_bf16(e[par][6], e[par][7])});
    sum[par] = __builtin_amdgcn_mfma_f32_16x16x32_bf16(ones, pf, zero, 0, 0, 0);
    o[par][0] = __builtin_amdgcn_mfma_f32_16x16x32_bf16(vf[par][0], pf, zero, 0, 0, 0);
    o[par][1] = __builtin_amdgcn_mfma_f32_16x16x32_bf16(vf[par][1], pf, zero, 0, 0, 0);
  }
#pragma unroll
  for (int par = 0; par < 2; ++par) {
    const float inv = __builtin_amdgcn_rcpf(sum[par][0]);
    const f32x4 o0 = o[par][0] * inv, o1 = o[par][1] * inv;
    st128<SC1>(pair_swap((u32x2){pk_bf16(o0[0], o0[1]), pk_bf16(o0[2], o0[3])}, (u32x2){pk_bf16(o1[0], o1[1]), pk_bf16(o1[2], o1[3])}),
                                           c.rws, (int)a.o[par], 0);
  }
  publish_last(c, FO, 0, 8);
}

__device__ __forceinline__ float red8(const float (&v)[8], int r16) {
  const bool b2 = r16 & 4, b1 = r16 & 2, b0 = r16 & 1;
  float u[4], t[2];
#pragma unroll
  for (int i = 0; i < 4; ++i) u[i] = (b2 ? v[4 + i] : v[i]) + dpp<0x141>(b2 ? v[i] : v[4 + i]);
#pragma unroll
  for (int i = 0; i < 2; ++i) t[i] = (b1 ? u[2 + i] : u[i]) + dpp<0x4E>(b1 ? u[i] : u[2 + i]);
  const float x = (b0 ? t[1] : t[0]) + dpp<0xB1>(b0 ? t[0] : t[1]);
  return x + dpp<0x128>(x);
}

__device__ __forceinline__ void consumer(const Params& P, Ctx& c) {
  refresh(c);
  const int w = c.wid;
  const rsrc_t rx = mk_rsrc(P.x, 2048u * 2048u);
  const AtPre ap = attention_pre(c);
  {
    if (c.lane < 12) lds_st<u32>(c.lds + (u32)((c.lane >> 2) * P1_ST + P1_ONES + 4 * (c.lane & 3)), 0x3f803f80u);
    AdP1 ad;
#pragma unroll
    for (int ks = 0; ks < 2; ++ks) {
      const u32 a = (u32)(c.r16 * 128 + (((4 * ks + c.g) ^ (c.r16 >> 1)) * 16));
      ad.a[ks] = mk_win(c.lds + a);
      ad.a0[ks] = mk_win(c.lds + (c.r16 == 0 ? (u32)P1_ONES : a));
    }
#pragma unroll
    for (int n = 0; n < 3; ++n) ad.b[n] = mk_win(c.lds + tr64_lane(c, 3 * w + n));
    f32x4 acc[9][3];
#pragma unroll
    for (int m = 0; m < 9; ++m)
#pragma unroll
      for (int n = 0; n < 3; ++n) { acc[m][n] = (f32x4){0.f, 0.f, 0.f, 0.f}; asm volatile("" : "+v"(acc[m][n])); }
    u32 s0[3], s1[3];
#pragma unroll
    for (int n = 0; n < 3; ++n) {
      const int S = 3 * w + n;
      const u32 sb = c.lds + AT_BASE + (u32)((S >> 1) * 2 * AT_BLK + (S & 1) * 32 + 8 * c.g);
      s0[n] = sb + (u32)((c.r16 >> 3) * AT_BLK + (c.r16 & 7) * AT_RS); s1[n] = sb + (u32)((8 + c.r16) * AT_RS);
      asm volatile("" : "+v"(s0[n]), "+v"(s1[n]));
    }
    Set<1, 9, 3> X, Y;
    MK_LGKM0();
    bar_c();
    sfor<15>([&](auto kc) { rd_p1<0, 0, decltype(kc)::v>(ad, X); });
    MK_LGKM0();
#pragma unroll 1
    for (int it = 0; it < 5; ++it) { tile_p1<0, false>(acc, X, Y, ad); tile_p1<1, false>(acc, X, Y, ad); tile_p1<2, false>(acc, X, Y, ad); }
    part<1, 9, 3, true, 15>(acc, X, Gap1{}, [&](auto kc) { rd_p1<0, 1, decltype(kc)::v>(ad, Y); });
    bar_c();
    refresh(c);
    f32x2 xs[9];
    const u32 xa = opaque(c.lds + T_XST + (u32)(c.r16 * 8));
    part<1, 9, 3, true, 9>(acc, Y, Gap1{}, [&](auto kc) { constexpr int m = decltype(kc)::v; xs[m] = lds_ld<f32x2>(xa + m * 128); });
    mfma_out();
#pragma unroll
    for (int n = 0; n < 3; ++n) {
      f32x4 cs;
#pragma unroll
      for (int r = 0; r < 4; ++r) cs[r] = dpp<0x150>(acc[0][n][r]);
#pragma unroll
      for (int m = 0; m < 9; ++m) {
        const f32x4 v = (acc[m][n] - xs[m][0] * cs) * xs[m][1];
        lds_st<u32x2>(m == 0 ? s0[n] : s1[n] + (u32)(((m - 1) & 1) * AT_BLK + ((m - 1) >> 1) * 16 * AT_RS), (u32x2){pk_bf16(v[0], v[1]), pk_bf16(v[2], v[3])});
      }
    }
  }
  MK_LGKM0();
  bar_c();
  get_tag(c);
  int G = ((c.g & 1) << 1) | (c.g >> 1);
  u32x4 xr[2][2];
#pragma unroll
  for (int m = 0; m < 2; ++m)
#pragma unroll
    for (int pr = 0; pr < 2; ++pr)
      xr[m][pr] = __builtin_amdgcn_raw_buffer_load_b128(rx, (128 * c.rp + 32 * w + 2 * c.r16 + m) * 2048 + (64 * c.cj + 32 * pr + 8 * G) * 2, 0, 0);
  const u32 bspw = *(const u32*)(P.bsp + 128 * c.rp + 32 * w + 2 * c.r16);
  attention(c, ap);
  bar_c();
  u32x2 hb[2][4];
  {
    f32x4 acc[2][4];
#pragma unroll
    for (int m = 0; m < 2; ++m)
#pragma unroll
      for (int pr = 0; pr < 2; ++pr) {
        const u32x4 t = pair_swap((u32x2){xr[m][pr][0], xr[m][pr][1]}, (u32x2){xr[m][pr][2], xr[m][pr][3]});
        acc[m][2 * pr] = (f32x4){bf_lo(t[0]), bf_hi(t[0]), bf_lo(t[1]), bf_hi(t[1])};
        acc[m][2 * pr + 1] = (f32x4){bf_lo(t[2]), bf_hi(t[2]), bf_lo(t[3]), bf_hi(t[3])};
        asm volatile("" : "+v"(acc[m][2 * pr]), "+v"(acc[m][2 * pr + 1]));
      }
    u32 ho[2];
#pragma unroll
    for (int m = 0; m < 2; ++m) { ho[m] = WS_H + (u32)(c.rp * 32 + 2 * c.cj) * GA + frag_off(c, 2 * w + m, 16 * G + c.r16); asm volatile("" : "+v"(ho[m])); }
    bar_c();
    gemm_g2<16, true>(acc, c);
#pragma unroll
    for (int m = 0; m < 2; ++m) {
#pragma unroll
      for (int n = 0; n < 4; ++n) hb[m][n] = (u32x2){pk_bf16(acc[m][n][0], acc[m][n][1]), pk_bf16(acc[m][n][2], acc[m][n][3])};
#pragma unroll
      for (int pr = 0; pr < 2; ++pr) st128<SC1>(pair_swap(hb[m][2 * pr], hb[m][2 * pr + 1]), c.rws, (int)ho[m], pr * GA);
    }
    bar_c();
    float s[2], q[2];
#pragma unroll
    for (int m = 0; m < 2; ++m) {
      f32x2 s2 = {0.f, 0.f}, q2 = {0.f, 0.f};
#pragma unroll
      for (int n = 0; n < 4; ++n)
#pragma unroll
        for (int d = 0; d < 2; ++d) { const f32x2 a = {bf_lo(hb[m][n][d]), bf_hi(hb[m][n][d])}; s2 += a; q2 += a * a; }
      s[m] = sumg(s2[0] + s2[1]); q[m] = sumg(q2[0] + q2[1]);
    }
    float sv = c.g ? s[1] : s[0], qv = c.g ? q[1] : q[0];
    asm volatile("" : "+v"(sv), "+v"(qv));
    publish_last(c, FHH, 1, 4);
    if (c.g < 2) __builtin_amdgcn_raw_buffer_store_b64((u32x2){asu(sv), asu(qv)}, c.rws,
        (int)(WS_HSTAT + (u32)(((c.rp * 16 + c.cj) * 128 + 32 * w + c.lane) * 8)), 0, SC1);
  }
  {
    AdG3 ad;
    ad.af = mk_win(c.lds + (u32)(w * 2048 + c.lane * 16));
#pragma unroll
    for (int ks = 0; ks < 2; ++ks) ad.ar[ks] = mk_win(c.lds + (u32)((32 * w + c.r16) * 128 + (((4 * ks + c.g) ^ (c.r16 >> 1)) * 16)));
    ad.b = mk_win(c.lds + (u32)(c.lane * 16));
    float bsp[2] = {bf_lo(bspw), bf_hi(bspw)};
    f32x4 acc[2][7];
#pragma unroll
    for (int m = 0; m < 2; ++m)
#pragma unroll
      for (int n = 0; n < 7; ++n) { acc[m][n] = (f32x4){0.f, 0.f, 0.f, 0.f}; asm volatile("" : "+v"(acc[m][n])); }
    asm volatile("" : "+v"(bsp[0]), "+v"(bsp[1]));
    const u32 tg = opaque(c.lds + TAB + (u32)((32 * w + 4 * c.g) * 4));
    const u32 tr = opaque(c.lds + TAB + (u32)((32 * w + c.r16) * 4));
    Set<1, 2, 7> X, Y;
    bar_c();
    bar_c();
    sfor<9>([&](auto kc) { rd_g3<0, 0, decltype(kc)::v>(ad, X); });
    MK_LGKM0();
    sfor<16>([&](auto tc) { tile_g3<decltype(tc)::v>(acc, X, Y, ad, [&] { if constexpr (decltype(tc)::v == 3) MK_WAITV(0); }); });
    f32x4 hm[2], hr[2], c1[7];
    float c2[7], nm[2];
#pragma unroll
    for (int m = 0; m < 2; ++m) {
      hm[m] = lds_ld<f32x4>(tg + (T_HM - TAB) + 64 * m); hr[m] = lds_ld<f32x4>(tg + (T_HR - TAB) + 64 * m);
      nm[m] = lds_ld<float>(tr + (T_HM - TAB) + 64 * m);
    }
#pragma unroll
    for (int n = 0; n < 7; ++n) c2[n] = lds_ld<float>(c.lds + T_CS + (u32)((16 * n + c.r16) * 4));
    mfma_out();
    f32x4 z2[2][7];
    {
      f32x4 sm[2] = {{0.f, 0.f, 0.f, 0.f}, {0.f, 0.f, 0.f, 0.f}}, sq[2] = {{0.f, 0.f, 0.f, 0.f}, {0.f, 0.f, 0.f, 0.f}}, ga[2], gb[2];
#pragma unroll
      for (int m = 0; m < 2; ++m) { ga[m] = hr[m] * -2.30220820f; gb[m] = hr[m] * hr[m] * hr[m] * -0.10294324f; }
#pragma unroll
      for (int n = 0; n < 7; ++n) {
#pragma unroll
        for (int m = 0; m < 2; ++m) {
          const f32x4 t = acc[m][n] - hm[m] * c2[n];
          f32x4 y = t * (t * t * gb[m] + ga[m]);
          asm("" : "+v"(y));
          f32x4 e;
#pragma unroll
          for (int i = 0; i < 4; ++i) e[i] = __builtin_amdgcn_exp2f(y[i]);
          e += 1.0f;
#pragma unroll
          for (int i = 0; i < 4; ++i) e[i] = __builtin_amdgcn_rcpf(e[i]);
          const f32x4 z = t * e;
          z2[m][n] = z; sm[m] += z; sq[m] += z * z;
        }
      }
#pragma unroll
      for (int m = 0; m < 2; ++m) { sm[m] *= hr[m]; sq[m] *= hr[m] * hr[m]; }
      MK_SB();
      const u32 ca = c.lds + T_CS + (u32)((112 + 4 * c.g) * 4);
#pragma unroll
      for (int n = 0; n < 7; ++n) c1[n] = lds_ld<f32x4>(ca + 64 * n);
      const float sv[8] = {sm[0][0], sm[0][1], sm[0][2], sm[0][3], sm[1][0], sm[1][1], sm[1][2], sm[1][3]};
      const float qv[8] = {sq[0][0], sq[0][1], sq[0][2], sq[0][3], sq[1][0], sq[1][1], sq[1][2], sq[1][3]};
      const float s = red8(sv, c.r16), q = red8(qv, c.r16);
      if (c.r16 < 8) st128<SC1>(tag_pair(c, s, q), c.rws,
          (int)(WS_ZSTAT + (u32)(((c.rp * 16 + c.cj) * 128 + 32 * w + 16 * (c.r16 >> 2) + 4 * c.g + (c.r16 & 3)) * 16)), 0);
#pragma unroll
      for (int m = 0; m < 2; ++m)
#pragma unroll
        for (int n = 0; n < 7; ++n) { acc[m][n] = c1[n] * -nm[m]; asm volatile("" : "+v"(acc[m][n])); }
    }
    mfma_in();
    sfor<16>([&](auto tc) {
      constexpr int t = 16 + decltype(tc)::v;
      tile_g3<t>(acc, X, Y, ad, [&] { if constexpr (t == FLAG_T) MK_WAITV(0); });
      if constexpr (t == NORM_T) {
        refresh(c);
        f32x4 zr[2], zo[2];
#pragma unroll
        for (int m = 0; m < 2; ++m) {
          zr[m] = lds_ld<f32x4>(tg + (T_ZR - TAB) + 64 * m) * lds_ld<f32x4>(tg + (T_HR - TAB) + 64 * m);
          zo[m] = lds_ld<f32x4>(tg + (T_ZM - TAB) + 64 * m);
        }
#pragma unroll
        for (int n = 0; n < 7; ++n) {
          const f32x4 a = z2[0][n] * zr[0] + zo[0], b = z2[1][n] * zr[1] + zo[1];
          const u32x4 zf = {pk_bf16(a[0], b[0]), pk_bf16(a[1], b[1]), pk_bf16(a[2], b[2]), pk_bf16(a[3], b[3])};
          st128<SC1>(zf, c.rws, (int)frag_off(c, n, c.lane), (int)(WS_Z2N + (u32)(c.cj * 64 + 4 * c.rp + w) * GB));
        }
      }
    });
    mfma_out();
    f32x4 z1[2][7];
    float hr1[2];
#pragma unroll
    for (int m = 0; m < 2; ++m) {
      hr1[m] = lds_ld<float>(tr + (T_HR - TAB) + 64 * m);
#pragma unroll
      for (int n = 0; n < 7; ++n) {
        z1[m][n] = acc[m][n];
        acc[m][n] = (f32x4){bsp[m], bsp[m], bsp[m], bsp[m]};
        asm volatile("" : "+v"(acc[m][n]));
      }
    }
    u32 up[2], us[2];
    mfma_in();
    sfor<32>([&](auto tc) {
      constexpr int i = decltype(tc)::v;
      tile_g3<32 + i>(acc, X, Y, ad, [&] {
        if constexpr (i < 28) {
          constexpr int m = i / 14, n = (i % 14) / 2, h = 2 * (i & 1);
          const f32x2 x = (f32x2){z1[m][n][h], z1[m][n][h + 1]} * hr1[m];
          f32x2 y = x * (x * x * -0.10294324f + -2.30220820f);
          asm("" : "+v"(y));
          f32x2 e = {__builtin_amdgcn_exp2f(y[0]), __builtin_amdgcn_exp2f(y[1])};
          e += 1.0f;
          f32x2 z = x * (f32x2){__builtin_amdgcn_rcpf(e[0]), __builtin_amdgcn_rcpf(e[1])};
          asm volatile("" : "+v"(z));
          z1[m][n][h] = z[0]; z1[m][n][h + 1] = z[1];
        } else if constexpr (i < 30) {
          constexpr int m = i - 28;
          refresh(c);
          up[m] = frag_off(c, 2 * w + m, c.lane);
          us[m] = frag_off(c, 2 * w + m, 16 * (2 * (c.cj & 1) + (c.g >> 1)) + c.r16) + (u32)(8 * (c.g & 1));
          asm volatile("" : "+v"(up[m]), "+v"(us[m]));
        }
      });
    });
    mfma_out();
    {
      const int odd = c.cj & 1, kp = (7 * c.cj + 1) >> 1, ksg = (7 * c.cj + (odd ? -1 : 6)) >> 1;
      const u32 gp = WS_U + (u32)(c.rp * 56 + kp) * GA, gs = WS_U + (u32)(c.rp * 56 + ksg) * GA;
#pragma unroll
      for (int m = 0; m < 2; ++m) {
        u32x2 uv[7];
#pragma unroll
        for (int n = 0; n < 7; ++n) {
          const f32x4 u = z1[m][n] * acc[m][n];
          uv[n] = (u32x2){pk_bf16(u[0], u[1]), pk_bf16(u[2], u[3])};
          if (n & 1) st128<SC1>((u32x4){uv[n - 1][0], uv[n - 1][1], uv[n][0], uv[n][1]}, c.rws, (int)up[m], (int)(gp + (n >> 1) * GA));
        }
        __builtin_amdgcn_raw_buffer_store_b64(uv[6], c.rws, (int)us[m], (int)gs, SC1);
      }
      bar_c();
      publish_last(c, FU, 2, 4);
    }
  }
  {
    refresh(c);
    G = ((c.g & 1) << 1) | (c.g >> 1);
    f32x4 acc[2][4];
#pragma unroll
    for (int m = 0; m < 2; ++m)
#pragma unroll
      for (int n = 0; n < 4; ++n) {
        acc[m][n] = (f32x4){bf_lo(hb[m][n][0]), bf_hi(hb[m][n][0]), bf_lo(hb[m][n][1]), bf_hi(hb[m][n][1])};
        asm volatile("" : "+v"(acc[m][n]));
      }
    bar_c();
    gemm_g2<28, false>(acc, c);
    const rsrc_t ro = mk_rsrc(P.out, 2048u * 2048u);
#pragma unroll
    for (int m = 0; m < 2; ++m)
#pragma unroll
      for (int pr = 0; pr < 2; ++pr) {
        const f32x4 a = acc[m][2 * pr], b = acc[m][2 * pr + 1];
        st128<SC1 | 1>(pair_swap((u32x2){pk_bf16(a[0], a[1]), pk_bf16(a[2], a[3])}, (u32x2){pk_bf16(b[0], b[1]), pk_bf16(b[2], b[3])}), ro,
            (128 * c.rp + 32 * w + 2 * c.r16 + m) * 2048 + (64 * c.cj + 32 * pr + 8 * G) * 2, 0);
      }
    if (c.rp == 0 && c.cj == 0 && w == 0 && c.lane == 0)
      __builtin_amdgcn_raw_buffer_store_b64((u32x2){c.tag_lo, c.tag_hi}, c.rws, (int)WS_EPOCH, 0, SC1);
  }
  asm volatile(".Lmk_end_c:");
}

template <int N, int AUX> __device__ __forceinline__ void issue_v2(rsrc_t r, u32 dst, const u32 (&vo)[N], u32 so) {
  sfor<N>([&](auto ic) { constexpr int s = decltype(ic)::v; glds<(s & 1) * 2048, AUX>(r, dst + (s >> 1) * 4096, vo[s], so); });
}
template <int N> __device__ __forceinline__ void issue_f(rsrc_t r, u32 dst, const u32 (&lp)[8], u32 so) {
  sfor<N>([&](auto ic) { constexpr int s = decltype(ic)::v; glds<(s & 3) * 1024, SC1>(r, dst + (s >> 2) * 4096, lp[s], so); });
}
template <int LO, int HI, int NA_, class FA, class FB> __device__ __forceinline__ void issue_rng(FA&& fa, FB&& fb) {
  sfor<HI - LO>([&](auto ic) { constexpr int s = LO + decltype(ic)::v; if constexpr (s < NA_) fa(IC<s>{}); else fb(IC<s - NA_>{}); });
}
struct Off { u32 da, db, sa, sb; };
template <int NT_, int NA_, int SPLIT, int W, class FA, class FB, class NX>
__device__ __forceinline__ void steady(int n, Off& o, FA&& fa, FB&& fb, NX&& next) {
  bar_n();
  issue_rng<0, SPLIT, NA_>([&](auto ic) { fa(ic, o); }, [&](auto ic) { fb(ic, o); });
#pragma unroll 1
  for (int i = 1; i < n; ++i) {
    Off q = next(o);
    asm volatile("" : "+s"(q.da), "+s"(q.db), "+s"(q.sa), "+s"(q.sb));
    issue_rng<SPLIT, NT_, NA_>([&](auto ic) { fa(ic, o); }, [&](auto ic) { fb(ic, o); });
    MK_WAITV(W);
    bar_n();
    issue_rng<0, SPLIT, NA_>([&](auto ic) { fa(ic, q); }, [&](auto ic) { fb(ic, q); });
    o = q;
  }
  issue_rng<SPLIT, NT_, NA_>([&](auto ic) { fa(ic, o); }, [&](auto ic) { fb(ic, o); });
  MK_WAITV(W);
}
template <int NT_, int NA_, int W, class FA, class FB, class NX>
__device__ __forceinline__ void steady2(int lw, int n, Off o, FA&& fa, FB&& fb, NX&& next) {
  if (lw == 0) steady<NT_, NA_, NT_ / 4, W>(n, o, fa, fb, next); else steady<NT_, NA_, NT_ - NT_ / 4, W>(n, o, fa, fb, next);
}
__device__ __forceinline__ u32 tr64_src(int lane, int lw, int ldb_bytes) {
  const int f = ((lane >> 4) & 1) | (lw << 1);
  return (u32)((8 * lw + (lane >> 3)) * ldb_bytes + (((lane & 7) ^ (2 * f)) * 16));
}
template <int T, bool LASTBAR>
__device__ __forceinline__ u32x2 load_g2(const Ctx& c, int lw, const u32 (&lp)[8], u32 abase, rsrc_t rb, const u32 (&vb)[4], u32 fo) {
  constexpr u32 bstep = 64 * 2048;
  const u32 sa = abase + (u32)lw * GA, da = c.lds + G2_A + (u32)(lw * 8192), db = c.lds + G2_B + (u32)(lw * 1024);
  const int ko = (T / 4) * (c.rp >> 2);
  auto kt = [&](int t) { const int k = t + ko; return (u32)(k >= T ? k - T : k); };
  auto A = [&](int t) { issue_f<8>(c.rws, da + (u32)((t & 3) * 16384), lp, sa + kt(t) * 2 * GA); };
  auto B = [&](int t) { issue_v2<4, 0>(rb, db + (u32)((t % G2_NB) * 8192), vb, kt(t) * bstep); };
  A(0); A(1); MK_WAITV(8); bar_n();
  A(2); A(3); MK_WAITV(16); bar_n();
  A(4); B(11); MK_WAITV(20); bar_n();
  A(5); B(12);
  MK_WAITV(24);
  steady2<12, 8, 28>(lw, T - 13, Off{da + 2 * 16384, db + 2 * 8192, sa + kt(6) * 2 * GA, kt(13) * bstep},
      [&](auto ic, const Off& o) { constexpr int s = decltype(ic)::v; glds<(s & 3) * 1024, SC1>(c.rws, o.da + (s >> 2) * 4096, lp[s], o.sa); },
      [&](auto ic, const Off& o) { constexpr int s = decltype(ic)::v; glds<(s & 1) * 2048, 0>(rb, o.db + (s >> 1) * 4096, vb[s], o.sb); },
      [&](const Off& o) { return Off{o.da == da + 3 * 16384 ? da : o.da + 16384, o.db == db + (G2_NB - 1) * 8192 ? db : o.db + 8192,
                                      o.sa == sa + (T - 1) * 2 * GA ? sa : o.sa + 2 * GA, o.sb == (T - 1) * bstep ? 0 : o.sb + bstep}; });
  bar_n(); A(T - 7); MK_WAITV(24);
  bar_n(); A(T - 6); MK_WAITV(20);
  bar_n(); A(T - 5); MK_WAITV(16);
  bar_n(); A(T - 4); MK_WAITV(16);
  bar_n(); A(T - 3); MK_WAITV(16);
  bar_n(); A(T - 2); MK_WAITV(16);
  bar_n(); A(T - 1); MK_WAITV(16);
  bar_n(); MK_WAITV(8);
  bar_n(); MK_WAITV(0);
  bar_n();
  u32x2 fv = {0, 0};
  if (LASTBAR) { fv = poll_issue(c, fo); bar_n(); }
  return fv;
}
template <int T> __device__ __forceinline__ void pre_g2(const Ctx& c, int lw, rsrc_t rb, const u32 (&vb)[4], int t0, int t1) {
  const int ko = (T / 4) * (c.rp >> 2);
#pragma unroll 1
  for (int t = t0; t < t1; ++t) { const int k = t + ko; issue_v2<4, 0>(rb, c.lds + G2_B + (u32)(lw * 1024 + t * 8192), vb, (u32)((k >= T ? k - T : k) * 64 * 2048)); }
}
__device__ __forceinline__ void src_g2(const Ctx& c, int lw, u32 (&vb)[4]) {
  const u32 b0 = tr64_src(c.lane, lw, 2048) + (u32)(128 * c.cj + 4096);
#pragma unroll
  for (int s = 0; s < 4; ++s) vb[s] = b0 + (u32)(s * 16 * 2048 - (s & 1) * 2048);
}

__device__ __forceinline__ void loader(const Params& P, Ctx& c) {
  const int lw = c.wid - 4, lane = c.lane;
  {
    const rsrc_t rx = mk_rsrc((const char*)P.x - 4096, 2048u * 2048u + 4096u);
    const rsrc_t rw = mk_rsrc((const char*)P.wqkv - 4096, 1024u * 6144u + 4096u);
    u32 va[9], vb[12];
    const u32 ch = (u32)((((lane & 7) ^ (4 * lw + (lane >> 4))) * 16) + 4096);
    va[0] = (u32)(max(128 * c.rp - 16 + 2 * (lane >> 3) + lw, 0) * 2048) + ch;
    const u32 a1 = (u32)((128 * c.rp + 16 * lw + 2 * (lane >> 3)) * 2048) + ch;
#pragma unroll
    for (int m = 1; m < 9; ++m) va[m] = a1 + (u32)((32 * ((m - 1) >> 1) + ((m - 1) & 1)) * 2048 - (m & 1) * 2048);
    const u32 b0 = tr64_src(lane, lw, 6144) + (u32)(128 * c.cj + 4096);
#pragma unroll
    for (int s = 0; s < 12; ++s) vb[s] = b0 + (u32)((s >> 2) * 2048 + (s & 3) * 16 * 6144 - (s & 1) * 2048);
    const int ko = 4 * (c.rp >> 2);
    auto I = [&](int t, int st) {
      const u32 sb = c.lds + (u32)(st * P1_ST + lw * 1024);
      const int k = (t + ko) & 15;
      issue_v2<9, 0>(rx, sb, va, (u32)(k * 128));
      issue_v2<12, 0>(rw, sb + P1_A, vb, (u32)(k * 64 * 6144));
    };
    I(0, 0); MK_WAITV(0); bar_n();
    I(1, 1); I(2, 2);
    const u32 d0 = c.lds + (u32)(lw * 1024);
    MK_WAITV(21);
    steady2<21, 9, 21>(lw, 13, Off{d0, d0 + P1_A, (u32)((3 + ko) * 128), (u32)((3 + ko) * 64 * 6144)},
        [&](auto ic, const Off& o) { constexpr int s = decltype(ic)::v; glds<(s & 1) * 2048, 0>(rx, o.da + (s >> 1) * 4096, va[s], o.sa); },
        [&](auto ic, const Off& o) { constexpr int s = decltype(ic)::v; glds<(s & 1) * 2048, 0>(rw, o.db + (s >> 1) * 4096, vb[s], o.sb); },
        [&](const Off& o) { const u32 d = o.da == d0 + 2 * P1_ST ? d0 : o.da + P1_ST; return Off{d, d + P1_A, (o.sa + 128) & 2047, o.sa == 1920 ? 0 : o.sb + 64 * 6144}; });
  }
  const u32 fwo = flag_off(FW, 0, 0) + (u32)((((lane >> 1) & 15) * 16 + c.cj) * 128 + 8 * (lane & 1));
  u32x2 fwv;
  u32 lp[8];
#pragma unroll
  for (int f = 0; f < 8; ++f) lp[f] = frag_off(c, f, lane) - (u32)((f & 3) * 1024);
  {
    const rsrc_t rb = mk_rsrc((const char*)P.wo - 4096, 1024u * 2048u + 4096u);
    u32 vb[4];
    src_g2(c, lw, vb);
    static_assert(6 * 8192 >= P1_ST && G2_PRE * 8192 <= 2 * P1_ST, "B slots 6.. lie in the stage P1 frees first");
    bar_n();
    pre_g2<16>(c, lw, rb, vb, 6, G2_PRE);
    MK_WAITV(4 * (G2_PRE - 6)); bar_n();
    bar_n();
    pre_g2<16>(c, lw, rb, vb, 0, 6);
    get_tag(c);
    const AtPre ap = attention_pre(c);
    bar_h();
    attention(c, ap);
    bar_n();
    pre_g2<16>(c, lw, rb, vb, G2_PRE, G2_NB);
    if (lw == 0) await_panel<9>(c, FO, 0, 8);
    bar_h();
    fwv = load_g2<16, true>(c, lw, lp, WS_O + (u32)(c.rp * 32) * GA, rb, vb, fwo);
  }
  {
    const rsrc_t rs = mk_rsrc((const char*)P.wsp - 4096, 2048u * 4096u + 4096u);
    const u32 bh = WS_H + (u32)(c.rp * 32 + lw) * GA, bw = WS_WINP + (u32)(c.cj * 64 + lw) * GB, bz = WS_Z2N + (u32)(c.cj * 64 + lw) * GB;
    const u32 da = c.lds + (u32)(lw * 8192), db = c.lds + (u32)(lw * 7168), dr = c.lds + (u32)(lw * 1024);
    u32 vs[8];
    const u32 s0 = (u32)((128 * c.rp + 16 * lw + 2 * (lane >> 3)) * 4096 + (((lane & 7) ^ (4 * lw + (lane >> 4))) * 16) + 4096);
#pragma unroll
    for (int m = 0; m < 8; ++m) vs[m] = s0 + (u32)((32 * (m >> 1) + (m & 1)) * 4096 - (m & 1) * 2048);
    bar_h();
    poll_check(c, fwo, fwv);
    auto A = [&](auto tc) {
      constexpr int t = decltype(tc)::v;
      if constexpr (t < 32) issue_f<8>(c.rws, da + g3_a(t), lp, bh + (u32)(t < 16 ? t : t - 22) * 2 * GA);
      else issue_v2<8, 0>(rs, dr + g3_a(t), vs, (u32)((t - 32) * 128));
    };
    auto B = [&](auto tc) {
      constexpr int t = decltype(tc)::v;
      issue_f<7>(c.rws, db + g3_b(t), lp, t < 32 ? bw + (u32)(t < 16 ? t : 16 + ((t + 10) & 15)) * 2 * GB : bz + (u32)(t - 32) * 2 * GB);
    };
    sfor<6>([&](auto tc) { B(tc); });
    bar_h();
    A(IC<0>{}); A(IC<1>{}); MK_WAITV(8); bar_n();
    A(IC<2>{}); A(IC<3>{}); MK_WAITV(16);
    sfor<30>([&](auto pc) {
      constexpr int p = 1 + decltype(pc)::v;
      bar_n();
      if constexpr (p == 30) B(IC<32>{});
      if constexpr (g3_na(p) != 0) A(IC<p + 3>{});
      if constexpr (g3_nb(p) != 0) B(IC<p + 3>{});
      MK_WAITV(p == 1 ? 8 + g3_na(1) : g3_na(p - 1) + g3_nb(p - 1) + g3_na(p) + g3_nb(p));
    });
    steady2<15, 8, 30>(lw, 30, Off{dr + g3_a(34), db + g3_b(34), 2 * 128, bz + 4 * GB},
        [&](auto ic, const Off& o) { constexpr int s = decltype(ic)::v; glds<(s & 1) * 2048, 0>(rs, o.da + (s >> 1) * 4096, vs[s], o.sa); },
        [&](auto ic, const Off& o) { constexpr int s = decltype(ic)::v; glds<(s & 3) * 1024, SC1>(c.rws, o.db + (s >> 2) * 4096, lp[s], o.sb); },
        [&](const Off& o) { return Off{o.da == dr + 5 * 16384 ? dr : o.da + 16384, o.db == db + G3_B + 3 * 14336 ? db + G3_B : o.db + 14336, o.sa + 128, o.sb + 2 * GB}; });
    bar_n();
    MK_WAITV(15); bar_n(); MK_WAITV(0); bar_n();
    bar_n();
  }
  {
    const rsrc_t rb = mk_rsrc((const char*)P.wout - 4096, 1792u * 2048u + 4096u);
    u32 vb[4];
    src_g2(c, lw, vb);
    bar_h();
    pre_g2<28>(c, lw, rb, vb, 0, G2_NB);
    bar_h();
    load_g2<28, false>(c, lw, lp, WS_U + (u32)(c.rp * 56) * GA, rb, vb, 0);
  }
  asm volatile(".Lmk_end_l:");
}

__device__ __forceinline__ void bars(int n) {
#pragma unroll 1
  for (int i = 0; i < n; ++i) bar_n();
}
__device__ __forceinline__ void sumsq2(float& s, float& q, u32 u) {
  float a, b;
  asm("v_lshlrev_b32 %2, 16, %4\n\tv_and_b32 %3, 0xffff0000, %4\n\tv_add_f32 %0, %0, %2\n\tv_fmac_f32 %1, %2, %2\n\tv_add_f32 %0, %0, %3\n\tv_fmac_f32 %1, %3, %3"
      : "+v"(s), "+v"(q), "=&v"(a), "=&v"(b) : "v"(u));
}
__device__ __forceinline__ void warm_slow(const Params& P, const Ctx& c, int row0, int nrow) {
  const int cslow = (c.cj & 8) + ((3 - (int)((size_t)P.wqkv >> 7)) & 7), i = c.lane / 3, g = c.lane - 3 * i;
  if (32 * i < nrow) touch(c, mk_rsrc(P.wqkv, 1024u * 6144u), (u32)(((row0 + 256 * (c.rp >> 2) + (blockIdx.x >> 3) + 32 * i) & 1023) * 6144 + g * 2048 + cslow * 128), 0);
}
__device__ __forceinline__ void load_x(const Params& P, const Ctx& c, int h, u32x4 (&xr)[4]) {
  const rsrc_t rx = mk_rsrc(P.x, 2048u * 2048u);
#pragma unroll
  for (int i = 0; i < 4; ++i)
    xr[i] = __builtin_amdgcn_raw_buffer_load_b128(rx, (128 * c.rp + 8 * c.cj + 4 * (c.wid - 6) + i) * 2048 + ((c.lane * 16 + h * 1024 + 512 * (c.rp >> 2)) & 2047), 0, 0);
}
__device__ __forceinline__ void worker(const Params& P, Ctx& c, u32x4 (&xr0)[4]) {
  refresh(c);
  const int v = c.wid - 6, lane = c.lane;
  const u32 stg = c.lds + WSTG + (u32)(v * 14336);
  u32x4 wr[14];
  {
    u32x4 xr1[4];
    const u32x2 ep = __builtin_amdgcn_raw_buffer_load_b64(c.rws, (int)WS_EPOCH, 0, SC1);
    bar_n();
    { const u64 t = (((u64)ep[1] << 32) | ep[0]) + 1;
      c.tag_lo = __builtin_amdgcn_readfirstlane((u32)t); c.tag_hi = __builtin_amdgcn_readfirstlane((u32)(t >> 32));
      if (v == 1 && lane < 5) lds_st<u32>(c.lds + T_TAG + (u32)(4 * lane), lane == 0 ? c.tag_lo : lane == 1 ? c.tag_hi : 0u); }
    bars(2);
    load_x(P, c, 1, xr1);
    bars(3);
    float sm = 0.f, sr = 0.f;
#pragma unroll
    for (int i = 0; i < 4; ++i) {
      float s = 0.f, q = 0.f;
#pragma unroll
      for (int h = 0; h < 2; ++h)
#pragma unroll
        for (int d = 0; d < 4; ++d) { u32 u = h ? xr1[i][d] : xr0[i][d]; sumsq2(s, q, u); }
      const float mean = sumg(sum16(s)) * (1.0f / 1024.0f);
      const float rs = __builtin_amdgcn_rsqf(fmaxf(sumg(sum16(q)) * (1.0f / 1024.0f) - mean * mean, 0.f) + 1e-5f);
      sm = lane == i ? mean : sm; sr = lane == i ? rs : sr;
      asm volatile("" : "+v"(sm), "+v"(sr));
      if (i == 3) { if (lane < 4) st128<SC1>(tag_pair(c, sm, sr), c.rws, (int)(WS_XSTAT + (u32)((128 * c.rp + 8 * c.cj + 4 * v + lane) * 16)), 0); }
      if (i == 0 && v == 1) warm_slow(P, c, 448, 320);
      bar_n();
    }
    if (v == 0) warm_slow(P, c, 768, 256);
    else touch_code(c);
    bars(2);
    u32x4 xg[2];
    u32 xo[2];
#pragma unroll
    for (int i = 0; i < 2; ++i) {
      const int rho = min(72 * v + 64 * i + lane, 72 * v + 71), m = rho >> 4, sl = rho & 15;
      const int il = m == 0 ? (2 * (sl & 7) + (sl >> 3)) : 16 + 32 * ((m - 1) >> 1) + 2 * sl + ((m - 1) & 1);
      xo[i] = WS_XSTAT + (u32)(max(128 * c.rp - 16 + il, 0) * 16);
      xg[i] = __builtin_amdgcn_raw_buffer_load_b128(c.rws, (int)xo[i], 0, SC1);
    }
    bars(2);
    if (!__all((tag_bad(c, xg[0]) | tag_bad(c, xg[1])) == 0)) {
#pragma unroll 1
      for (int sp = 0; sp < SPIN_CAP; ++sp) {
        asm volatile("" ::: "memory");
#pragma unroll
        for (int i = 0; i < 2; ++i) xg[i] = __builtin_amdgcn_raw_buffer_load_b128(c.rws, (int)xo[i], 0, SC1);
        if (__all((tag_bad(c, xg[0]) | tag_bad(c, xg[1])) == 0)) break;
      }
    }
#pragma unroll
    for (int i = 0; i < 2; ++i) lds_st<u32x2>(c.lds + T_XST + (u32)(min(72 * v + 64 * i + lane, 72 * v + 71) * 8), (u32x2){xg[i][0], xg[i][2]});
    bar_h();
    {
      const rsrc_t rw = mk_rsrc(P.win, 1024u * 7168u);
      int k = lane / 14, ch = lane - 14 * k;
      u32 vo = (u32)((64 * c.rp + k) * 7168 + ((v == 0 ? 1792 : 0) + 112 * c.cj) * 2 + ch * 16);
#pragma unroll
      for (int j = 0; j < 14; ++j) {
        wr[j] = __builtin_amdgcn_raw_buffer_load_b128(rw, (int)vo, 0, 0);
        ch += 8; const bool wrap = ch >= 14; ch -= wrap ? 14 : 0; vo += wrap ? (u32)(5 * 7168 - 96) : (u32)(4 * 7168 + 128);
        if (j == 6) bar_n();
      }
    }
    bar_n();
  }
  const AtPre ap = attention_pre(c);
  bar_n();
  attention(c, ap);
  bar_n();
#pragma unroll
  for (int j = 0; j < 14; ++j) lds_st<u32x4>(stg + (u32)(j * 1024 + lane * 16), wr[j]);
  MK_LGKM0();
  {
    const int odd = c.cj & 1;
    const u32 ta = stg + (u32)((8 * c.g + c.q) * 224);
    const u32 tp = ta + (u32)(((odd ? 16 : 0) + 8 * c.p) * 2), ts = ta + (u32)(((odd ? 0 : 96) + 4 * c.p) * 2);
    const u32 wo_ = WS_WINP + (u32)((c.cj * 2 + v) * 32 + 2 * c.rp) * GB;
    float cs = 0.f;
    sfor<7>([&](auto nc) {
      constexpr int n = decltype(nc)::v, o = n < 6 ? 64 * (n >> 1) + 8 * (n & 1) : 0;
      bf16x8 f2[2];
      lds_tr4<o, o + 896, o + 7168, o + 7168 + 896>(n < 6 ? tp : ts, n < 6 ? tp : ts, f2[0], f2[1]);
      f32x2 s2 = {0.f, 0.f};
#pragma unroll
      for (int kl = 0; kl < 2; ++kl) {
        const u32x4 fr = __builtin_bit_cast(u32x4, f2[kl]);
        st128<SC1>(fr, c.rws, (int)frag_off(c, n, lane), (int)(wo_ + kl * GB));
#pragma unroll
        for (int d = 0; d < 4; ++d) s2 += (f32x2){bf_lo(fr[d]), bf_hi(fr[d])};
      }
      const float t = sumg(s2[0] + s2[1]);
      if constexpr (n < 4) cs = (c.g == n) ? t : cs;
      if constexpr (n == 3) __builtin_amdgcn_raw_buffer_store_b32(asu(cs), c.rws, (int)(WS_CS + (u32)(((c.rp * 16 + c.cj) * 256 + 112 * v + lane) * 4)), 0, SC1);
      if constexpr (n >= 4) cs = (c.g == n - 4) ? t : cs;
    });
    if (lane < 48) __builtin_amdgcn_raw_buffer_store_b32(asu(cs), c.rws, (int)(WS_CS + (u32)(((c.rp * 16 + c.cj) * 256 + 112 * v + 64 + lane) * 4)), 0, SC1);
  }
  bar_h();
  bars(4);
  publish(c, FW, v);
  bars(6);
  if (v == 1) touch_code(c);
  bars(7);
  bar_n();
  const int hi = lane >> 5, l5 = lane & 31;
  u32x4 cp[16];
  if (v == 0) await_panel<14>(c, FHH, 1, 4);
  else {
    poll64(c, flag_off(FW, 0, 0) + (u32)((((lane >> 1) & 15) * 16 + c.cj) * 128 + 8 * (lane & 1)));
#pragma unroll
    for (int j = 0; j < 16; ++j)
      cp[j] = __builtin_amdgcn_raw_buffer_load_b128(c.rws, (int)(WS_CS + (u32)(((hi * 16 + c.cj) * 256 + 112 * (j >> 3) + 4 * min(l5, 27)) * 4)), (j & 7) * 32768, SC1);
  }
  bar_n();
  {
    auto fold = [](float x) { const u32 u = asu(x); auto r = __builtin_amdgcn_permlane32_swap(u, u, false, false); return asf((u32)r[0]) + asf((u32)r[1]); };
    const u32 fso = flag_off(FHS, 0, 0) + (u32)((c.rp * 16 + (lane & 15)) * 128);
    bars(5);
    if (v == 1 && lane == 0) __builtin_amdgcn_raw_buffer_store_b64((u32x2){c.tag_lo, c.tag_hi}, c.rws, (int)flag_off(FHS, c.rp, c.cj), 0, SC1);
    bars(3);
    const u32x2 fsv = poll_issue(c, fso);
    bars(2);
    poll_check(c, fso, fsv);
    u32x4 hp[8];
#pragma unroll
    for (int j = 0; j < 8; ++j) {
      hp[j] = __builtin_amdgcn_raw_buffer_load_b128(c.rws, (int)(WS_HSTAT + (u32)(((c.rp * 16 + hi) * 128 + 64 * v + 2 * l5) * 8)), j * 2048, SC1);
    }
#pragma unroll
    for (int h = 0; h < 2; ++h) {
      bar_n();
      if (v == 1) {
        float c0 = 0.f, c1 = 0.f, c2 = 0.f, c3 = 0.f;
#pragma unroll
        for (int j = 8 * h; j < 8 * h + 8; ++j) { add2(c0, c1, cp[j][0], cp[j][1]); add2(c2, c3, cp[j][2], cp[j][3]); }
        lds_st<f32x4>(c.lds + T_CS + (u32)((112 * h + 4 * min(l5, 27)) * 4), (f32x4){fold(c0), fold(c1), fold(c2), fold(c3)});
      }
    }
    if (v == 0) touch_code(c);
    bars(2);
    float s0 = 0.f, q0 = 0.f, s1 = 0.f, q1 = 0.f;
#pragma unroll
    for (int j = 0; j < 8; ++j) { add2(s0, q0, hp[j][0], hp[j][1]); add2(s1, q1, hp[j][2], hp[j][3]); }
    {
      s0 = fold(s0); q0 = fold(q0); s1 = fold(s1); q1 = fold(q1);
      const float sx = hi ? s1 : s0, qx = hi ? q1 : q0;
      const float mean = sx * (1.0f / 1024.0f);
      const float rs = __builtin_amdgcn_rsqf(fmaxf(qx * (1.0f / 1024.0f) - mean * mean, 0.f) + 1e-5f);
      lds_st<float>(c.lds + T_HM + (u32)((64 * v + 2 * l5 + hi) * 4), mean);
      lds_st<float>(c.lds + T_HR + (u32)((64 * v + 2 * l5 + hi) * 4), rs);
    }
    bar_n();
    bar_h(); bar_n();
#pragma unroll
    for (int h = 0; h < 2; ++h)
      touch(c, mk_rsrc(P.wsp, 2048u * 4096u), (u32)((lane >> 4) * 4096 + (lane & 15) * 128), blockIdx.x * 32768u + v * 16384u + h * 2048u);
    bars(SWEEP_T - 16);
    u32x4 zp[16];
    auto sweep = [&]() {
#pragma unroll
      for (int j = 0; j < 16; ++j) zp[j] = __builtin_amdgcn_raw_buffer_load_b128(c.rws, (int)(WS_ZSTAT + (u32)((c.rp * 2048 + 64 * v + lane) * 16)), j * 2048, SC1);
    };
    sweep();
    bars(NORM_T - 1 - SWEEP_T);
    float zs = 0.f, zq = 0.f;
    u32 bad = 0;
    auto half = [&](int j0) {
#pragma unroll
      for (int j = j0; j < j0 + 8; ++j) {
        u32 t1, t2;
        asm("v_bitop3_b32 %3, %5, %6, %8 bitop3:0x96\n\tv_bitop3_b32 %4, %7, %9, %8 bitop3:0x69\n\tv_add_f32 %0, %0, %9\n\tv_add_f32 %1, %1, %6\n\tv_or3_b32 %2, %2, %3, %4"
            : "+v"(zs), "+v"(zq), "+v"(bad), "=&v"(t1), "=&v"(t2) : "v"(zp[j][1]), "v"(zp[j][2]), "v"(zp[j][3]), "s"(c.tag_lo), "v"(zp[j][0]));
      }
    };
    half(0);
    asm volatile("" : "+v"(zs), "+v"(zq), "+v"(bad));
    bar_n();
    half(8);
    if (!__all(bad == 0)) {
#pragma unroll 1
      for (int sp = 0; sp < SPIN_CAP; ++sp) {
        asm volatile("" ::: "memory");
        sweep();
        zs = 0.f; zq = 0.f; bad = 0;
        half(0); half(8);
        if (__all(bad == 0)) break;
      }
    }
    const float zm = zs * (1.0f / 1792.0f);
    const float zr = __builtin_amdgcn_rsqf(fmaxf(zq * (1.0f / 1792.0f) - zm * zm, 0.f) + 1e-5f);
    lds_st<float>(c.lds + T_ZM + (u32)((64 * v + lane) * 4), -zm * zr);
    lds_st<float>(c.lds + T_ZR + (u32)((64 * v + lane) * 4), zr);
    bar_h();
    bars(FLAG_T - NORM_T);
    if (v == 1 && lane == 0) __builtin_amdgcn_raw_buffer_store_b64((u32x2){c.tag_lo, c.tag_hi}, c.rws, (int)flag_off(FZ, c.rp, c.cj), 0, SC1);
    bars(26 - FLAG_T);
    const u32 fo = flag_off(FZ, 0, 0) + (u32)(((lane & 15) * 16 + c.cj) * 128);
    u32x2 fv = {0, 0};
    if (v == 0) fv = poll_issue(c, fo);
    bars(2);
    if (v == 0) poll_check(c, fo, fv);
    bars(24);
    if (v == 1) touch_code(c);
    if (v == 0) {
      const int cs5 = (c.cj & 8) + ((3 - (int)((size_t)P.wout >> 7)) & 7);
      if (lane < 56) touch(c, mk_rsrc(P.wout, 1792u * 2048u), (u32)(((blockIdx.x >> 3) + 32 * lane) * 2048 + cs5 * 128), 0);
    }
    bars(11);
  }
  bar_n();
  if (v == 0) await_panel<16>(c, FU, 2, 4);
  bar_n();
  asm volatile(".Lmk_end_w:");
}

__global__ __launch_bounds__(512, 2) void fused_block(Params P) {
  extern __shared__ __attribute__((aligned(16))) char smem[];
  asm volatile(".Lmk_code_begin:");
  Ctx c;
  c.lane = threadIdx.x & 63;
  c.wid = __builtin_amdgcn_readfirstlane(threadIdx.x >> 6);
  const int xcd = blockIdx.x & 7, idx = blockIdx.x >> 3;
  c.rp = (xcd & 3) * 4 + (idx & 3);
  c.cj = (xcd >> 2) * 8 + (idx >> 2);
  c.r16 = c.lane & 15; c.g = c.lane >> 4; c.q = (c.lane >> 2) & 3; c.p = c.lane & 3;
  c.lds = (u32)(size_t)(LAS char*)smem;
  c.rws = mk_rsrc(P.ws, WS_END);
  c.skip = 7 - ((3 - (int)((size_t)P.ws >> 7)) & 7);
  c.tag_lo = 0; c.tag_hi = 0;
  u32x4 xr0[4];
  if (c.wid == 7) touch_code(c);
  if (c.wid >= 6) {
    __builtin_amdgcn_s_sleep(5);
    if (c.wid == 6) warm_slow(P, c, 64, 384);
    load_x(P, c, 0, xr0);
  }
  if (__builtin_expect((c.wid >> 1) == 2, 1)) loader(P, c); else if (c.wid < 4) consumer(P, c); else worker(P, c, xr0);
}

}

extern "C" void kernel_launch(void* const* d_in, const int* in_sizes, int n_in, void* d_out, int out_size, void* d_ws, size_t ws_size,
                              hipStream_t stream) {
  (void)in_sizes; (void)n_in; (void)out_size; (void)ws_size;
  mk::Params P;
  P.x = (const mk::u16*)d_in[0]; P.wqkv = (const mk::u16*)d_in[1]; P.wo = (const mk::u16*)d_in[2];
  P.win = (const mk::u16*)d_in[4]; P.wsp = (const mk::u16*)d_in[5]; P.bsp = (const mk::u16*)d_in[6]; P.wout = (const mk::u16*)d_in[7];
  P.out = (mk::u16*)d_out; P.ws = (char*)d_ws;
  (void)hipFuncSetAttribute((const void*)mk::fused_block, hipFuncAttributeMaxDynamicSharedMemorySize, mk::LDS_BYTES);
  hipLaunchKernelGGL(mk::fused_block, dim3(256), dim3(512), mk::LDS_BYTES, stream, P);
}
```

```cpp
#include <hip/hip_runtime.h>

namespace mk {

#define LAS __attribute__((address_space(3)))
typedef LAS void* lds_vp;
typedef unsigned short u16;
typedef unsigned int u32;
typedef unsigned long long u64;
typedef short bf16x8 __attribute__((ext_vector_type(8)));
typedef short bf16x4 __attribute__((ext_vector_type(4)));
typedef float f32x2 __attribute__((ext_vector_type(2)));
typedef float f32x4 __attribute__((ext_vector_type(4)));
typedef u32 u32x2 __attribute__((ext_vector_type(2)));
typedef u32 u32x4 __attribute__((ext_vector_type(4)));
typedef __bf16 bf16x2n __attribute__((ext_vector_type(2)));
typedef __amdgpu_buffer_rsrc_t rsrc_t;

constexpr u32 WS_EPOCH = 0;
constexpr u32 WS_FLAGS = 4096;
enum { FHS = 0, FW = 1, FO = 2, FHH = 3, FZ = 4, FU = 5 };
constexpr u32 WS_XSTAT = 256 * 1024;
constexpr u32 WS_HSTAT = 512 * 1024;
constexpr u32 WS_CS    = 768 * 1024;
constexpr u32 WS_ZSTAT = 1024 * 1024;
constexpr u32 GA = 10240, GB = 8192;
constexpr u32 WS_O     = 2u << 20;
constexpr u32 WS_H     = 8u << 20;
constexpr u32 WS_WINP  = 14u << 20;
constexpr u32 WS_Z2N   = 23u << 20;
constexpr u32 WS_U     = 32u << 20;
constexpr u32 WS_END   = 42u << 20;
__host__ __device__ constexpr u32 flag_off(int kind, int rp, int cj) { return WS_FLAGS + (u32)((kind * 256 + rp * 16 + cj) * 128); }

constexpr int P1_A = 144 * 128, P1_B = 192 * 128, P1_ST = P1_A + P1_B + 32;
constexpr int P1_ONES = P1_A + P1_B;
constexpr int G2_NB = 11, G2_B = 0, G2_A = G2_NB * 8192;
constexpr int WSTG = G2_A;
constexpr int G2_PRE = 10, AT_BASE = G2_PRE * 8192, AT_RS = 80, AT_BLK = 72 * AT_RS;
constexpr int G3_B = 6 * 16384;
__host__ __device__ constexpr int g3_a(int t) { return (t < 8 ? t % 4 : (t + 2) % 6) * 16384; }
__host__ __device__ constexpr int g3_b(int t) { return t == 4 || t == 5 ? t * 16384 : G3_B + (t % 4) * 14336; }
__host__ __device__ constexpr int g3_na(int p) { return p + 3 >= 16 && p + 3 < 22 ? 0 : 8; }
__host__ __device__ constexpr int g3_nb(int p) { return p + 3 < 6 || p == 29 ? 0 : p == 30 ? 14 : 7; }
constexpr int TAB = 156 * 1024;
constexpr int T_XST = TAB;
constexpr int T_ZM = TAB, T_ZR = TAB + 512, T_HM = TAB + 1152, T_HR = T_HM + 512, T_CS = T_HR + 512;
constexpr int T_TAG = T_CS + 896, T_CNT = T_TAG + 8, T_DUMMY = T_TAG + 32;
constexpr int LDS_BYTES = 160 * 1024;
static_assert(3 * P1_ST <= TAB && WSTG + 2 * 14336 <= TAB, "lds");
static_assert(AT_BASE + 12 * AT_BLK <= TAB, "lds");
static_assert(G2_A + 4 * 16384 <= TAB, "lds");
static_assert(G3_B + 4 * 14336 <= TAB, "lds");
static_assert(T_DUMMY + 256 <= LDS_BYTES, "lds");

constexpr int SWEEP_T = 18;
constexpr int NORM_T = 21;
constexpr int FLAG_T = 24;

struct Params {
  const u16* x; const u16* wqkv; const u16* wo; const u16* win; const u16* wsp; const u16* bsp; const u16* wout;
  u16* out; char* ws;
};

template <int I> struct IC { static constexpr int v = I; };
template <int N, int I = 0, class F> __device__ __forceinline__ void sfor(F&& f) { if constexpr (I < N) { f(IC<I>{}); sfor<N, I + 1>(f); } }
#define MK_WAITV(n) asm volatile("s_waitcnt vmcnt(%0)" ::"i"(n) : "memory")
#define MK_LGKM0() __builtin_amdgcn_s_waitcnt(0xC07F)
#define MK_SB() __builtin_amdgcn_sched_barrier(0)
__device__ __forceinline__ void bar_h() { asm volatile("s_waitcnt lgkmcnt(0)\n\ts_barrier" ::: "memory"); }
__device__ __forceinline__ void bar_n() { asm volatile("s_barrier" ::: "memory"); }
__device__ __forceinline__ void bar_c() { asm volatile("" ::: "memory"); __builtin_amdgcn_s_barrier(); asm volatile("" ::: "memory"); }

__device__ __forceinline__ rsrc_t mk_rsrc(const void* p, u32 bytes) { return __builtin_amdgcn_make_buffer_rsrc((void*)p, 0, bytes, 0x00020000); }
template <int OFF, int AUX> __device__ __forceinline__ void glds(rsrc_t r, u32 ldsdst, u32 voff, u32 soff) {
  __builtin_amdgcn_raw_ptr_buffer_load_lds(r, (lds_vp)(size_t)ldsdst, 16, (int)voff, (int)soff, OFF, AUX);
}
constexpr int SC1 = 16;
template <int AUX> __device__ __forceinline__ void st128(u32x4 v, rsrc_t r, int voff, int soff) {
  __builtin_amdgcn_raw_buffer_store_b128(v, r, voff, soff, AUX);
  asm volatile("s_nop 1" ::"v"(v) : "memory");
}
__device__ __forceinline__ u32 pk_bf16(float lo, float hi) { f32x2 v = {lo, hi}; return __builtin_bit_cast(u32, __builtin_convertvector(v, bf16x2n)); }
__device__ __forceinline__ float asf(u32 u) { return __builtin_bit_cast(float, u); }
__device__ __forceinline__ u32 asu(float f) { return __builtin_bit_cast(u32, f); }
__device__ __forceinline__ float bf_lo(u32 u) { return __builtin_bit_cast(float, u << 16); }
__device__ __forceinline__ float bf_hi(u32 u) { return __builtin_bit_cast(float, u & 0xffff0000u); }
template <int CTRL> __device__ __forceinline__ float dpp(float x) {
  return __builtin_bit_cast(float, __builtin_amdgcn_update_dpp(0, __builtin_bit_cast(int, x), CTRL, 0xf, 0xf, true));
}
__device__ __forceinline__ float sum16(float x) { x += dpp<0xB1>(x); x += dpp<0x4E>(x); x += dpp<0x141>(x); x += dpp<0x140>(x); return x; }
__device__ __forceinline__ float sumg(float x) {
  u32 u = __builtin_bit_cast(u32, x);
  auto a = __builtin_amdgcn_permlane16_swap(u, u, false, false);
  x = __builtin_bit_cast(float, (u32)a[0]) + __builtin_bit_cast(float, (u32)a[1]);
  u = __builtin_bit_cast(u32, x);
  auto b = __builtin_amdgcn_permlane32_swap(u, u, false, false);
  return __builtin_bit_cast(float, (u32)b[0]) + __builtin_bit_cast(float, (u32)b[1]);
}
__device__ __forceinline__ u32x4 pair_swap(u32x2 a, u32x2 b) {
  auto r0 = __builtin_amdgcn_permlane16_swap(a[0], b[0], false, false);
  auto r1 = __builtin_amdgcn_permlane16_swap(a[1], b[1], false, false);
  return (u32x4){(u32)r0[0], (u32)r1[0], (u32)r0[1], (u32)r1[1]};
}
__device__ __forceinline__ void add2(float& s, float& q, u32 a, u32 b) {
  asm("v_add_f32 %0, %0, %2\n\tv_add_f32 %1, %1, %3" : "+v"(s), "+v"(q) : "v"(a), "v"(b));
}
template <class T> __device__ __forceinline__ T lds_ld(u32 a) { return *(const LAS T*)(size_t)a; }
template <class T> __device__ __forceinline__ void lds_st(u32 a, T v) { *(LAS T*)(size_t)a = v; }
__device__ __forceinline__ bf16x4 lds_tr(u32 a) { return __builtin_amdgcn_ds_read_tr16_b64_v4i16((LAS bf16x4*)(size_t)a); }
template <int O0, int O1, int O2, int O3> __device__ __forceinline__ void lds_tr4(u32 a, u32 b, bf16x8& f0, bf16x8& f1) {
  bf16x4 r0, r1, r2, r3;
  asm volatile("ds_read_b64_tr_b16 %0, %4 offset:%6\n\tds_read_b64_tr_b16 %1, %5 offset:%7\n\tds_read_b64_tr_b16 %2, %4 offset:%8\n\tds_read_b64_tr_b16 %3, %5 offset:%9\n\ts_waitcnt lgkmcnt(0)"
               : "=&v"(r0), "=&v"(r1), "=&v"(r2), "=&v"(r3) : "v"(a), "v"(b), "i"(O0), "i"(O1), "i"(O2), "i"(O3) : "memory");
  f0 = __builtin_shufflevector(r0, r1, 0, 1, 2, 3, 4, 5, 6, 7); f1 = __builtin_shufflevector(r2, r3, 0, 1, 2, 3, 4, 5, 6, 7);
}
__device__ __forceinline__ u32 opaque(u32 x) { asm("" : "+v"(x)); return x; }

struct Ctx {
  int lane, wid, rp, cj, r16, g, q, p;
  u32 lds;
  u32 tag_lo, tag_hi;
  int skip;
  rsrc_t rws;
};
__device__ __forceinline__ u32 frag_off(const Ctx& c, int f, int p) {
  const u32 l = (u32)(8 * f + (p >> 3));
  return (l + (((l + (u32)c.skip) * 37u) >> 8)) * 128u + (u32)((p & 7) * 16);
}
__device__ __forceinline__ void refresh(Ctx& c) {
  u32 t = threadIdx.x;
  asm volatile("" : "+v"(t));
  c.lane = t & 63; c.r16 = t & 15; c.g = (t >> 4) & 3; c.q = (t >> 2) & 3; c.p = t & 3;
}
constexpr int SPIN_CAP = 1 << 17;

__device__ __forceinline__ void get_tag(Ctx& c) {
  const u32x2 tg = lds_ld<u32x2>(c.lds + T_TAG);
  c.tag_lo = __builtin_amdgcn_readfirstlane(tg[0]); c.tag_hi = __builtin_amdgcn_readfirstlane(tg[1]);
}
__device__ __forceinline__ void publish(const Ctx& c, int kind, int slot) {
  asm volatile("s_waitcnt vmcnt(0)" ::: "memory");
  if (c.lane == 0) __builtin_amdgcn_raw_buffer_store_b64((u32x2){c.tag_lo, c.tag_hi}, c.rws, (int)(flag_off(kind, c.rp, c.cj) + 8 * slot), 0, SC1);
}
template <int PRE = 0> __device__ __forceinline__ void poll64(const Ctx& c, u32 voff) {
  if (PRE) __builtin_amdgcn_s_sleep(PRE);
#pragma unroll 1
  for (int s = 0; s < SPIN_CAP; ++s) {
    asm volatile("" ::: "memory");
    const u32x2 v = __builtin_amdgcn_raw_buffer_load_b64(c.rws, (int)voff, 0, SC1);
    if (__all(v[0] == c.tag_lo && v[1] == c.tag_hi)) break;
    __builtin_amdgcn_s_sleep(1);
  }
  asm volatile("" ::: "memory");
}
__device__ __forceinline__ void publish_last(const Ctx& c, int kind, int idx, u32 n) {
  MK_WAITV(0);
  if (c.lane == 0) {
    u32 old;
    asm volatile("ds_add_rtn_u32 %0, %1, %2\n\ts_waitcnt lgkmcnt(0)" : "=&v"(old) : "v"(c.lds + T_CNT + 4 * idx), "v"(1u) : "memory");
    if (old == n - 1) __builtin_amdgcn_raw_buffer_store_b64((u32x2){c.tag_lo, c.tag_hi}, c.rws, (int)flag_off(kind, c.rp, c.cj), 0, SC1);
  }
}
template <int PRE> __device__ __forceinline__ void await_panel(const Ctx& c, int kind, int idx, u32 n) {
#pragma unroll 1
  for (int s = 0; s < (1 << 22); ++s) {
    if (__builtin_amdgcn_readfirstlane(__hip_atomic_load((LAS u32*)(size_t)(c.lds + T_CNT + 4 * idx), __ATOMIC_RELAXED, __HIP_MEMORY_SCOPE_WORKGROUP)) >= n) break;
    __builtin_amdgcn_s_sleep(1);
  }
  poll64<PRE>(c, flag_off(kind, 0, 0) + (u32)((c.rp * 16 + (c.lane & 15)) * 128));
}
__device__ __forceinline__ u32x2 poll_issue(const Ctx& c, u32 voff) { return __builtin_amdgcn_raw_buffer_load_b64(c.rws, (int)voff, 0, SC1); }
__device__ __forceinline__ void poll_check(const Ctx& c, u32 voff, u32x2 v) {
  if (!__all(v[0] == c.tag_lo && v[1] == c.tag_hi)) poll64(c, voff);
  asm volatile("" ::: "memory");
}
__device__ __forceinline__ u32x4 tag_pair(const Ctx& c, float a, float b) {
  return (u32x4){asu(a), c.tag_lo ^ asu(b), asu(b), ~c.tag_lo ^ asu(a)};
}
__device__ __forceinline__ u32 tag_bad(const Ctx& c, u32x4 v) { return (v[1] ^ v[2] ^ c.tag_lo) | (v[3] ^ v[0] ^ ~c.tag_lo); }
__device__ __forceinline__ void touch(const Ctx& c, rsrc_t r, u32 voff, u32 soff) {
  __builtin_amdgcn_raw_ptr_buffer_load_lds(r, (lds_vp)(size_t)(c.lds + T_DUMMY), 4, (int)voff, (int)soff, 0, 0);
}
__device__ __forceinline__ void touch_code(const Ctx& c) {
  {
    u64 pc0; u32 back, e0, e1, e2;
    asm volatile("s_getpc_b64 %0\n.Lmk_pc%=:\n\ts_mov_b32 %1, .Lmk_pc%=-.Lmk_code_begin\n\ts_mov_b32 %2, .Lmk_end_c-.Lmk_code_begin\n\t"
                 "s_mov_b32 %3, .Lmk_end_l-.Lmk_code_begin\n\ts_mov_b32 %4, .Lmk_end_w-.Lmk_code_begin" : "=s"(pc0), "=s"(back), "=s"(e0), "=s"(e1), "=s"(e2));
    const rsrc_t rc = mk_rsrc((const void*)((pc0 - back) & ~127ull), max(e0, max(e1, e2)));
    if (c.lane < 24) touch(c, rc, (u32)(((blockIdx.x >> 3) + 32 * c.lane) * 128), 0);
  }
}

struct Win { u32 b[3]; };
__device__ __forceinline__ Win mk_win(u32 base) { Win w; w.b[0] = opaque(base); w.b[1] = opaque(base + 65536u); w.b[2] = opaque(base + 131072u); return w; }
template <int OFF> __device__ __forceinline__ u32 wad(const Win& w) { return w.b[OFF >> 16] + (u32)(OFF & 0xffff); }

template <int KS, int MT, int NT> struct Set { bf16x8 a[KS][MT]; bf16x8 b[KS][NT]; bf16x4 t[KS][NT]; };

__device__ __forceinline__ void mfma_acc(f32x4& c, const bf16x8& a, const bf16x8& b) {
  asm volatile("v_mfma_f32_16x16x32_bf16 %0, %1, %2, %0" : "+v"(c) : "v"(a), "v"(b));
}
__device__ __forceinline__ void mfma_new(f32x4& c, const bf16x8& a, const bf16x8& b) {
  asm volatile("v_mfma_f32_16x16x32_bf16 %0, %1, %2, 0" : "=&v"(c) : "v"(a), "v"(b));
}
__device__ __forceinline__ void mfma_in() { MK_SB(); asm volatile("s_nop 1"); MK_SB(); }
__device__ __forceinline__ void mfma_out() { MK_SB(); asm volatile("s_nop 7\n\ts_nop 1"); MK_SB(); }
template <int KS, int MT, int NT, bool SWAP, int NRD, bool FRESH = false, class GAP, class RD>
__device__ __forceinline__ void part(f32x4 (&acc)[MT][NT], const Set<KS, MT, NT>& X, GAP, RD&& rd) {
  constexpr int NMF = KS * MT * NT;
  MK_SB();
  sfor<NMF>([&](auto ic) {
    constexpr int i = decltype(ic)::v, ks = i / (MT * NT), m = (i / NT) % MT, n = i % NT;
    if constexpr (FRESH) mfma_new(acc[m][n], X.b[ks][n], X.a[ks][m]);
    else if constexpr (SWAP) mfma_acc(acc[m][n], X.b[ks][n], X.a[ks][m]); else mfma_acc(acc[m][n], X.a[ks][m], X.b[ks][n]);
    MK_SB();
    sfor<NRD>([&](auto kc) { if constexpr (GAP::gap(decltype(kc)::v) == i) rd(kc); });
    if constexpr (i == NMF - 2) MK_LGKM0();
    MK_SB();
  });
}
struct Gap1 { static constexpr int gap(int k) { return k; } };
struct GapG2 { static constexpr int gap(int k) { return k < 2 ? k : (k < 10 ? 2 + (k - 2) / 2 : (k < 12 ? k - 4 : 8 + (k - 12) / 2)); } };

struct AdP1 { Win a[2], a0[2], b[3]; };
template <int ST, int KSI, int K> __device__ __forceinline__ void rd_p1(const AdP1& ad, Set<1, 9, 3>& Y) {
  constexpr int SB = ST * P1_ST;
  if constexpr (K == 0) Y.a[0][0] = lds_ld<bf16x8>(wad<SB>(ad.a0[KSI]));
  else if constexpr (K <= 6) {
    constexpr int n = (K - 1) >> 1, h = (K - 1) & 1;
    const bf16x4 v = lds_tr(wad<SB + P1_A + 4096 * KSI + 512 * h>(ad.b[n]));
    if constexpr (h == 0) Y.t[0][n] = v; else Y.b[0][n] = __builtin_shufflevector(Y.t[0][n], v, 0, 1, 2, 3, 4, 5, 6, 7);
  } else { constexpr int m = K - 6; Y.a[0][m] = lds_ld<bf16x8>(wad<SB + m * 2048>(ad.a[KSI])); }
}
struct AdG3 { Win af, ar[2], b; };
template <int T, int KSI, int K> __device__ __forceinline__ void rd_g3(const AdG3& ad, Set<1, 2, 7>& Y) {
  constexpr int SA = g3_a(T), SB = g3_b(T);
  if constexpr (K == 0 || K == 8) {
    constexpr int m = K == 0 ? 0 : 1;
    if constexpr (T >= 32) Y.a[0][m] = lds_ld<bf16x8>(wad<SA + m * 2048>(ad.ar[KSI]));
    else Y.a[0][m] = lds_ld<bf16x8>(wad<SA + KSI * 8192 + m * 1024>(ad.af));
  } else { constexpr int n = K - 1; Y.b[0][n] = lds_ld<bf16x8>(wad<SB + (KSI * 7 + n) * 1024>(ad.b)); }
}
struct AdG2 { Win a, b[4]; };
template <int T, int K> __device__ __forceinline__ void rd_g2(const AdG2& ad, Set<2, 2, 4>& Y) {
  constexpr int ks = K / 10, k = K % 10;
  if constexpr (k < 2) Y.a[ks][k] = lds_ld<bf16x8>(wad<G2_A + (T % 4) * 16384 + ks * 8192 + k * 1024>(ad.a));
  else {
    constexpr int n = (k - 2) >> 1, h = (k - 2) & 1;
    const bf16x4 v = lds_tr(wad<G2_B + (T % G2_NB) * 8192 + 4096 * ks + 512 * h>(ad.b[n]));
    if constexpr (h == 0) Y.t[ks][n] = v; else Y.b[ks][n] = __builtin_shufflevector(Y.t[ks][n], v, 0, 1, 2, 3, 4, 5, 6, 7);
  }
}
__device__ __forceinline__ u32 tr64_lane(const Ctx& c, int strip) {
  const int f = (c.q >> 1) | ((c.g & 1) << 1);
  return (u32)((strip >> 2) * 8192 + 128 * (8 * c.g + c.q) + 16 * ((2 * (strip & 3) + (c.p >> 1)) ^ (2 * f)) + 8 * (c.p & 1));
}

template <int ST, bool LAST> __device__ __forceinline__ void tile_p1(f32x4 (&acc)[9][3], Set<1, 9, 3>& X, Set<1, 9, 3>& Y, const AdP1& ad) {
  part<1, 9, 3, true, 15>(acc, X, Gap1{}, [&](auto kc) { rd_p1<ST, 1, decltype(kc)::v>(ad, Y); });
  bar_c();
  part<1, 9, 3, true, LAST ? 0 : 15>(acc, Y, Gap1{}, [&](auto kc) { rd_p1<(ST + 1) % 3, 0, decltype(kc)::v>(ad, X); });
}
template <int T, class H>
__device__ __forceinline__ void tile_g3(f32x4 (&acc)[2][7], Set<1, 2, 7>& X, Set<1, 2, 7>& Y, const AdG3& ad, H&& hook) {
  part<1, 2, 7, (T >= 16), 9, T == 16>(acc, X, Gap1{}, [&](auto kc) { rd_g3<T, 1, decltype(kc)::v>(ad, Y); });
  hook();
  MK_SB();
  bar_c();
  part<1, 2, 7, (T >= 16), T < 63 ? 9 : 0>(acc, Y, Gap1{}, [&](auto kc) { rd_g3<T + 1, 0, decltype(kc)::v>(ad, X); });
}
template <int NT, bool LASTBAR> __device__ __forceinline__ void gemm_g2(f32x4 (&acc)[2][4], const Ctx& c) {
  AdG2 ad;
  ad.a = mk_win(c.lds + (u32)(c.wid * 2048 + c.lane * 16));
#pragma unroll
  for (int n = 0; n < 4; ++n) ad.b[n] = mk_win(c.lds + tr64_lane(c, n));
  Set<2, 2, 4> X, Y;
  bar_c();
  sfor<20>([&](auto kc) { rd_g2<0, decltype(kc)::v>(ad, X); });
  MK_LGKM0();
  sfor<NT>([&](auto tc) {
    constexpr int t = decltype(tc)::v;
    if constexpr (t + 1 < NT || LASTBAR) bar_c();
    part<2, 2, 4, true, (t + 1 < NT) ? 20 : 0>(acc, (t & 1) ? Y : X, GapG2{}, [&](auto kc) { rd_g2<t + 1, decltype(kc)::v>(ad, (t & 1) ? X : Y); });
  });
  mfma_out();
}

struct AtPre { u32 q, k0, k1, v0, v1, o[2]; u64 ok[8]; };
__device__ __forceinline__ AtPre attention_pre(const Ctx& c) {
  const int hh = c.wid & 1, T = c.wid >> 1;
  const int lo = (c.rp == 0 && T == 0) ? 8 : 0;
  const u32 bh = c.lds + AT_BASE + (u32)(hh * 2 * AT_BLK);
  AtPre a;
  a.q = bh + (u32)((8 + 16 * T + c.r16) * AT_RS + 16 * c.g);
  a.k0 = bh + (u32)(4 * AT_BLK + (16 * T + c.r16) * AT_RS + 16 * c.g);
  a.k1 = bh + (u32)(4 * AT_BLK + min(16 * T + 16 + c.r16, 71) * AT_RS + 16 * c.g);
  const int v0 = 16 * T + 4 * c.g + c.q;
  a.v0 = bh + (u32)(8 * AT_BLK + v0 * AT_RS + 8 * c.p);
  a.v1 = bh + (u32)(8 * AT_BLK + min(v0 + 16, 71) * AT_RS + 8 * c.p);
  const int G = ((c.g & 1) << 1) | (c.g >> 1);
#pragma unroll
  for (int par = 0; par < 2; ++par) a.o[par] = WS_O + (u32)(c.rp * 32 + 2 * c.cj + hh) * GA + frag_off(c, 2 * T + par, 16 * G + c.r16);
#pragma unroll
  for (int j = 0; j < 8; ++j) {
    const int ak = 16 * (j >> 2) + 4 * c.g + (j & 3), w = 8 + c.r16 - ak;
    a.ok[j] = __ballot((w >= 0) && (w <= 7) && (ak >= lo));
  }
  asm volatile("" : "+v"(a.q), "+v"(a.k0), "+v"(a.k1), "+v"(a.v0), "+v"(a.v1), "+v"(a.o[0]), "+v"(a.o[1]));
  asm volatile("" : "+s"(a.ok[0]), "+s"(a.ok[1]), "+s"(a.ok[2]), "+s"(a.ok[3]), "+s"(a.ok[4]), "+s"(a.ok[5]), "+s"(a.ok[6]), "+s"(a.ok[7]));
  return a;
}
__device__ __forceinline__ float vmax(float a, float b) { return __builtin_elementwise_maximum(a, b); }
__device__ __forceinline__ float vmax3(float a, float b, float d) { return vmax(vmax(a, b), d); }
__device__ __forceinline__ float vsel(u64 m, float a, float b) { return __builtin_amdgcn_inverse_ballot_w64(m) ? a : b; }
__device__ __forceinline__ void attention(const Ctx& c, const AtPre& a) {
  const float sc = 0.17677669529663687f * 1.4426950408889634f;
  const f32x4 zero = {0.f, 0.f, 0.f, 0.f};
  bf16x8 qf[2], kf[2][2], vf[2][2];
#pragma unroll
  for (int par = 0; par < 2; ++par) {
    kf[par][0] = lds_ld<bf16x8>(a.k0 + par * AT_BLK); qf[par] = lds_ld<bf16x8>(a.q + par * AT_BLK); kf[par][1] = lds_ld<bf16x8>(a.k1 + par * AT_BLK);
  }
  bf16x4 r[8];
  asm volatile("ds_read_b64_tr_b16 %0, %8\n\tds_read_b64_tr_b16 %1, %9\n\tds_read_b64_tr_b16 %2, %8 offset:32\n\tds_read_b64_tr_b16 %3, %9 offset:32\n\t"
               "ds_read_b64_tr_b16 %4, %8 offset:%10\n\tds_read_b64_tr_b16 %5, %9 offset:%10\n\tds_read_b64_tr_b16 %6, %8 offset:%11\n\tds_read_b64_tr_b16 %7, %9 offset:%11"
               : "=&v"(r[0]), "=&v"(r[1]), "=&v"(r[2]), "=&v"(r[3]), "=&v"(r[4]), "=&v"(r[5]), "=&v"(r[6]), "=&v"(r[7])
               : "v"(a.v0), "v"(a.v1), "i"(AT_BLK), "i"(AT_BLK + 32));
  MK_SB();
  f32x4 s[2][2];
#pragma unroll
  for (int par = 0; par < 2; ++par)
#pragma unroll
    for (int k2 = 0; k2 < 2; ++k2) s[par][k2] = __builtin_amdgcn_mfma_f32_16x16x32_bf16(kf[par][k2], qf[par], zero, 0, 0, 0);
  float e[2][8], mx[2];
  const float ninf = -3.0e38f;
#pragma unroll
  for (int par = 0; par < 2; ++par) {
#pragma unroll
    for (int j = 0; j < 8; ++j) { const float t = s[par][j >> 2][j & 3]; e[par][j] = vsel(a.ok[j], t, ninf); }
    mx[par] = vmax3(vmax3(e[par][0], e[par][1], e[par][2]), vmax3(e[par][3], e[par][4], e[par][5]), vmax(e[par][6], e[par][7]));
  }
  {
    auto p = __builtin_amdgcn_permlane16_swap(asu(mx[0]), asu(mx[1]), false, false);
    const float m = vmax(asf((u32)p[0]), asf((u32)p[1]));
    auto q = __builtin_amdgcn_permlane32_swap(asu(m), asu(m), false, false);
    const float t = vmax(asf((u32)q[0]), asf((u32)q[1]));
    auto u = __builtin_amdgcn_permlane16_swap(asu(t), asu(t), false, false);
    mx[0] = asf((u32)u[0]) * -sc; mx[1] = asf((u32)u[1]) * -sc;
  }
  asm volatile("s_waitcnt lgkmcnt(0)" : "+v"(r[0]), "+v"(r[1]), "+v"(r[2]), "+v"(r[3]), "+v"(r[4]), "+v"(r[5]), "+v"(r[6]), "+v"(r[7]));
  const bf16x8 ones = __builtin_bit_cast(bf16x8, (u32x4){0x3f803f80u, 0x3f803f80u, 0x3f803f80u, 0x3f803f80u});
  f32x4 o[2][2], sum[2];
#pragma unroll
  for (int par = 0; par < 2; ++par) {
    vf[par][0] = __builtin_shufflevector(r[4 * par], r[4 * par + 1], 0, 1, 2, 3, 4, 5, 6, 7);
    vf[par][1] = __builtin_shufflevector(r[4 * par + 2], r[4 * par + 3], 0, 1, 2, 3, 4, 5, 6, 7);
#pragma unroll
    for (int j = 0; j < 8; ++j) e[par][j] = __builtin_amdgcn_exp2f(__builtin_fmaf(e[par][j], sc, mx[par]));
    const bf16x8 pf = __builtin_bit_cast(bf16x8, (u32x4){pk_bf16(e[par][0], e[par][1]), pk_bf16(e[par][2], e[par][3]), pk_bf16(e[par][4], e[par][5]), pk_bf16(e[par][6], e[par][7])});
    sum[par] = __builtin_amdgcn_mfma_f32_16x16x32_bf16(ones, pf, zero, 0, 0, 0);
    o[par][0] = __builtin_amdgcn_mfma_f32_16x16x32_bf16(vf[par][0], pf, zero, 0, 0, 0);
    o[par][1] = __builtin_amdgcn_mfma_f32_16x16x32_bf16(vf[par][1], pf, zero, 0, 0, 0);
  }
#pragma unroll
  for (int par = 0; par < 2; ++par) {
    const float inv = __builtin_amdgcn_rcpf(sum[par][0]);
    const f32x4 o0 = o[par][0] * inv, o1 = o[par][1] * inv;
    st128<SC1>(pair_swap((u32x2){pk_bf16(o0[0], o0[1]), pk_bf16(o0[2], o0[3])}, (u32x2){pk_bf16(o1[0], o1[1]), pk_bf16(o1[2], o1[3])}),
                                           c.rws, (int)a.o[par], 0);
  }
  publish_last(c, FO, 0, 8);
}

__device__ __forceinline__ float red8(const float (&v)[8], int r16) {
  const bool b2 = r16 & 4, b1 = r16 & 2, b0 = r16 & 1;
  float u[4], t[2];
#pragma unroll
  for (int i = 0; i < 4; ++i) u[i] = (b2 ? v[4 + i] : v[i]) + dpp<0x141>(b2 ? v[i] : v[4 + i]);
#pragma unroll
  for (int i = 0; i < 2; ++i) t[i] = (b1 ? u[2 + i] : u[i]) + dpp<0x4E>(b1 ? u[i] : u[2 + i]);
  const float x = (b0 ? t[1] : t[0]) + dpp<0xB1>(b0 ? t[0] : t[1]);
  return x + dpp<0x128>(x);
}

__device__ __forceinline__ void consumer(const Params& P, Ctx& c) {
  refresh(c);
  const int w = c.wid;
  const rsrc_t rx = mk_rsrc(P.x, 2048u * 2048u);
  const AtPre ap = attention_pre(c);
  {
    if (c.lane < 24) lds_st<u32>(c.lds + (u32)((c.lane >> 3) * P1_ST + P1_ONES + 4 * (c.lane & 7)), (c.lane & 4) ? 0u : 0x3f803f80u);
    AdP1 ad;
#pragma unroll
    for (int ks = 0; ks < 2; ++ks) {
      const u32 a = (u32)(c.r16 * 128 + (((4 * ks + c.g) ^ (c.r16 >> 1)) * 16));
      ad.a[ks] = mk_win(c.lds + a);
      ad.a0[ks] = mk_win(c.lds + (c.r16 == 0 ? (u32)P1_ONES : w == 0 ? (u32)P1_ONES + 16 : a));
    }
#pragma unroll
    for (int n = 0; n < 3; ++n) ad.b[n] = mk_win(c.lds + tr64_lane(c, 3 * w + n));
    f32x4 acc[9][3];
#pragma unroll
    for (int m = 0; m < 9; ++m)
#pragma unroll
      for (int n = 0; n < 3; ++n) { acc[m][n] = (f32x4){0.f, 0.f, 0.f, 0.f}; asm volatile("" : "+v"(acc[m][n])); }
    u32 s0[3], s1[3];
#pragma unroll
    for (int n = 0; n < 3; ++n) {
      const int S = 3 * w + n;
      const u32 sb = c.lds + AT_BASE + (u32)((S >> 1) * 2 * AT_BLK + (S & 1) * 32 + 8 * c.g);
      s0[n] = sb + (u32)((c.r16 >> 3) * AT_BLK + (c.r16 & 7) * AT_RS); s1[n] = sb + (u32)((8 + c.r16) * AT_RS);
      asm volatile("" : "+v"(s0[n]), "+v"(s1[n]));
    }
    Set<1, 9, 3> X, Y;
    MK_LGKM0();
    bar_c();
    sfor<15>([&](auto kc) { rd_p1<0, 0, decltype(kc)::v>(ad, X); });
    MK_LGKM0();
#pragma unroll 1
    for (int it = 0; it < 5; ++it) { tile_p1<0, false>(acc, X, Y, ad); tile_p1<1, false>(acc, X, Y, ad); tile_p1<2, false>(acc, X, Y, ad); }
    part<1, 9, 3, true, 15>(acc, X, Gap1{}, [&](auto kc) { rd_p1<0, 1, decltype(kc)::v>(ad, Y); });
    bar_c();
    refresh(c);
    f32x2 xs[9];
    const u32 xa = opaque(c.lds + T_XST + (u32)(c.r16 * 8));
    part<1, 9, 3, true, 9>(acc, Y, Gap1{}, [&](auto kc) { constexpr int m = decltype(kc)::v; xs[m] = lds_ld<f32x2>(xa + m * 128); });
    mfma_out();
#pragma unroll
    for (int n = 0; n < 3; ++n) {
      f32x4 cs;
#pragma unroll
      for (int r = 0; r < 4; ++r) cs[r] = dpp<0x150>(acc[0][n][r]);
#pragma unroll
      for (int m = 0; m < 9; ++m) {
        const f32x4 v = (acc[m][n] - xs[m][0] * cs) * xs[m][1];
        lds_st<u32x2>(m == 0 ? s0[n] : s1[n] + (u32)(((m - 1) & 1) * AT_BLK + ((m - 1) >> 1) * 16 * AT_RS), (u32x2){pk_bf16(v[0], v[1]), pk_bf16(v[2], v[3])});
      }
    }
  }
  MK_LGKM0();
  bar_c();
  get_tag(c);
  int G = ((c.g & 1) << 1) | (c.g >> 1);
  u32x4 xr[2][2];
#pragma unroll
  for (int m = 0; m < 2; ++m)
#pragma unroll
    for (int pr = 0; pr < 2; ++pr)
      xr[m][pr] = __builtin_amdgcn_raw_buffer_load_b128(rx, (128 * c.rp + 32 * w + 2 * c.r16 + m) * 2048 + (64 * c.cj + 32 * pr + 8 * G) * 2, 0, 0);
  const u32 bspw = *(const u32*)(P.bsp + 128 * c.rp + 32 * w + 2 * c.r16);
  attention(c, ap);
  bar_c();
  u32x2 hb[2][4];
  {
    f32x4 acc[2][4];
#pragma unroll
    for (int m = 0; m < 2; ++m)
#pragma unroll
      for (int pr = 0; pr < 2; ++pr) {
        const u32x4 t = pair_swap((u32x2){xr[m][pr][0], xr[m][pr][1]}, (u32x2){xr[m][pr][2], xr[m][pr][3]});
        acc[m][2 * pr] = (f32x4){bf_lo(t[0]), bf_hi(t[0]), bf_lo(t[1]), bf_hi(t[1])};
        acc[m][2 * pr + 1] = (f32x4){bf_lo(t[2]), bf_hi(t[2]), bf_lo(t[3]), bf_hi(t[3])};
        asm volatile("" : "+v"(acc[m][2 * pr]), "+v"(acc[m][2 * pr + 1]));
      }
    u32 ho[2];
#pragma unroll
    for (int m = 0; m < 2; ++m) { ho[m] = WS_H + (u32)(c.rp * 32 + 2 * c.cj) * GA + frag_off(c, 2 * w + m, 16 * G + c.r16); asm volatile("" : "+v"(ho[m])); }
    bar_c();
    gemm_g2<16, true>(acc, c);
#pragma unroll
    for (int m = 0; m < 2; ++m) {
#pragma unroll
      for (int n = 0; n < 4; ++n) hb[m][n] = (u32x2){pk_bf16(acc[m][n][0], acc[m][n][1]), pk_bf16(acc[m][n][2], acc[m][n][3])};
#pragma unroll
      for (int pr = 0; pr < 2; ++pr) st128<SC1>(pair_swap(hb[m][2 * pr], hb[m][2 * pr + 1]), c.rws, (int)ho[m], pr * GA);
    }
    bar_c();
    float s[2], q[2];
#pragma unroll
    for (int m = 0; m < 2; ++m) {
      f32x2 s2 = {0.f, 0.f}, q2 = {0.f, 0.f};
#pragma unroll
      for (int n = 0; n < 4; ++n)
#pragma unroll
        for (int d = 0; d < 2; ++d) { const f32x2 a = {bf_lo(hb[m][n][d]), bf_hi(hb[m][n][d])}; s2 += a; q2 += a * a; }
      s[m] = sumg(s2[0] + s2[1]); q[m] = sumg(q2[0] + q2[1]);
    }
    float sv = c.g ? s[1] : s[0], qv = c.g ? q[1] : q[0];
    asm volatile("" : "+v"(sv), "+v"(qv));
    publish_last(c, FHH, 1, 4);
    if (c.g < 2) __builtin_amdgcn_raw_buffer_store_b64((u32x2){asu(sv), asu(qv)}, c.rws,
        (int)(WS_HSTAT + (u32)(((c.rp * 16 + c.cj) * 128 + 32 * w + c.lane) * 8)), 0, SC1);
  }
  {
    AdG3 ad;
    ad.af = mk_win(c.lds + (u32)(w * 2048 + c.lane * 16));
#pragma unroll
    for (int ks = 0; ks < 2; ++ks) ad.ar[ks] = mk_win(c.lds + (u32)((32 * w + c.r16) * 128 + (((4 * ks + c.g) ^ (c.r16 >> 1)) * 16)));
    ad.b = mk_win(c.lds + (u32)(c.lane * 16));
    float bsp[2] = {bf_lo(bspw), bf_hi(bspw)};
    f32x4 acc[2][7];
#pragma unroll
    for (int m = 0; m < 2; ++m)
#pragma unroll
      for (int n = 0; n < 7; ++n) { acc[m][n] = (f32x4){0.f, 0.f, 0.f, 0.f}; asm volatile("" : "+v"(acc[m][n])); }
    asm volatile("" : "+v"(bsp[0]), "+v"(bsp[1]));
    const u32 tg = opaque(c.lds + TAB + (u32)((32 * w + 4 * c.g) * 4));
    const u32 tr = opaque(c.lds + TAB + (u32)((32 * w + c.r16) * 4));
    Set<1, 2, 7> X, Y;
    bar_c();
    bar_c();
    sfor<9>([&](auto kc) { rd_g3<0, 0, decltype(kc)::v>(ad, X); });
    MK_LGKM0();
    sfor<16>([&](auto tc) { tile_g3<decltype(tc)::v>(acc, X, Y, ad, [&] { if constexpr (decltype(tc)::v == 3) MK_WAITV(0); }); });
    f32x4 hm[2], hr[2];
    float c2[7];
#pragma unroll
    for (int m = 0; m < 2; ++m) {
      hm[m] = lds_ld<f32x4>(tg + (T_HM - TAB) + 64 * m); hr[m] = lds_ld<f32x4>(tg + (T_HR - TAB) + 64 * m);
    }
#pragma unroll
    for (int n = 0; n < 7; ++n) c2[n] = lds_ld<float>(c.lds + T_CS + (u32)((16 * n + c.r16) * 4));
    mfma_out();
    f32x4 z2[2][7];
    {
      f32x4 sm[2] = {{0.f, 0.f, 0.f, 0.f}, {0.f, 0.f, 0.f, 0.f}}, sq[2] = {{0.f, 0.f, 0.f, 0.f}, {0.f, 0.f, 0.f, 0.f}}, ga[2], gb[2];
#pragma unroll
      for (int m = 0; m < 2; ++m) { ga[m] = hr[m] * -2.30220820f; gb[m] = hr[m] * hr[m] * hr[m] * -0.10294324f; }
#pragma unroll
      for (int n = 0; n < 7; ++n) {
#pragma unroll
        for (int m = 0; m < 2; ++m) {
          const f32x4 t = acc[m][n] - hm[m] * c2[n];
          f32x4 y = t * (t * t * gb[m] + ga[m]);
          asm("" : "+v"(y));
          f32x4 e;
#pragma unroll
          for (int i = 0; i < 4; ++i) e[i] = __builtin_amdgcn_exp2f(y[i]);
          e += 1.0f;
#pragma unroll
          for (int i = 0; i < 4; ++i) e[i] = __builtin_amdgcn_rcpf(e[i]);
          const f32x4 z = t * e;
          z2[m][n] = z; sm[m] += z; sq[m] += z * z;
        }
      }
#pragma unroll
      for (int m = 0; m < 2; ++m) { sm[m] *= hr[m]; sq[m] *= hr[m] * hr[m]; }
      const float sv[8] = {sm[0][0], sm[0][1], sm[0][2], sm[0][3], sm[1][0], sm[1][1], sm[1][2], sm[1][3]};
      const float qv[8] = {sq[0][0], sq[0][1], sq[0][2], sq[0][3], sq[1][0], sq[1][1], sq[1][2], sq[1][3]};
      const float s = red8(sv, c.r16), q = red8(qv, c.r16);
      if (c.r16 < 8) st128<SC1>(tag_pair(c, s, q), c.rws,
          (int)(WS_ZSTAT + (u32)(((c.rp * 16 + c.cj) * 128 + 32 * w + 16 * (c.r16 >> 2) + 4 * c.g + (c.r16 & 3)) * 16)), 0);
    }
    f32x4 acz[2][7];
    sfor<16>([&](auto tc) {
      constexpr int t = 16 + decltype(tc)::v;
      tile_g3<t>(acz, X, Y, ad, [&] { if constexpr (t == FLAG_T) MK_WAITV(0); });
      if constexpr (t == NORM_T) {
        refresh(c);
        f32x4 zr[2], zo[2];
#pragma unroll
        for (int m = 0; m < 2; ++m) {
          zr[m] = lds_ld<f32x4>(tg + (T_ZR - TAB) + 64 * m) * lds_ld<f32x4>(tg + (T_HR - TAB) + 64 * m);
          zo[m] = lds_ld<f32x4>(tg + (T_ZM - TAB) + 64 * m);
        }
#pragma unroll
        for (int n = 0; n < 7; ++n) {
          const f32x4 a = z2[0][n] * zr[0] + zo[0], b = z2[1][n] * zr[1] + zo[1];
          const u32x4 zf = {pk_bf16(a[0], b[0]), pk_bf16(a[1], b[1]), pk_bf16(a[2], b[2]), pk_bf16(a[3], b[3])};
          st128<SC1>(zf, c.rws, (int)frag_off(c, n, c.lane), (int)(WS_Z2N + (u32)(c.cj * 64 + 4 * c.rp + w) * GB));
        }
      }
    });
    auto& z1 = acz;
    float hm1[2], hr1[2];
    const u32 ca = opaque(c.lds + T_CS + (u32)((112 + 4 * c.g) * 4));
    f32x2 cn = lds_ld<f32x2>(ca);
#pragma unroll
    for (int m = 0; m < 2; ++m) {
      hm1[m] = lds_ld<float>(tr + (T_HM - TAB) + 64 * m); hr1[m] = lds_ld<float>(tr + (T_HR - TAB) + 64 * m);
#pragma unroll
      for (int n = 0; n < 7; ++n) {
        acc[m][n] = (f32x4){bsp[m], bsp[m], bsp[m], bsp[m]};
        asm volatile("" : "+v"(acc[m][n]));
      }
    }
    u32 up[2], us[2];
    mfma_in();
    sfor<32>([&](auto tc) {
      constexpr int i = decltype(tc)::v;
      tile_g3<32 + i>(acc, X, Y, ad, [&] {
        if constexpr (i < 28) {
          constexpr int m = i / 14, n = (i % 14) / 2, h = 2 * (i & 1);
          const f32x2 x = ((f32x2){z1[m][n][h], z1[m][n][h + 1]} - hm1[m] * cn) * hr1[m];
          if constexpr (i < 27) cn = lds_ld<f32x2>(ca + 64 * (((i + 1) % 14) / 2) + 8 * ((i + 1) & 1));
          f32x2 y = x * (x * x * -0.10294324f + -2.30220820f);
          asm("" : "+v"(y));
          f32x2 e = {__builtin_amdgcn_exp2f(y[0]), __builtin_amdgcn_exp2f(y[1])};
          e += 1.0f;
          f32x2 z = x * (f32x2){__builtin_amdgcn_rcpf(e[0]), __builtin_amdgcn_rcpf(e[1])};
          asm volatile("" : "+v"(z));
          z1[m][n][h] = z[0]; z1[m][n][h + 1] = z[1];
        } else if constexpr (i < 30) {
          constexpr int m = i - 28;
          refresh(c);
          up[m] = frag_off(c, 2 * w + m, c.lane);
          us[m] = frag_off(c, 2 * w + m, 16 * (2 * (c.cj & 1) + (c.g >> 1)) + c.r16) + (u32)(8 * (c.g & 1));
          asm volatile("" : "+v"(up[m]), "+v"(us[m]));
        }
      });
    });
    mfma_out();
    {
      const int odd = c.cj & 1, kp = (7 * c.cj + 1) >> 1, ksg = (7 * c.cj + (odd ? -1 : 6)) >> 1;
      const u32 gp = WS_U + (u32)(c.rp * 56 + kp) * GA, gs = WS_U + (u32)(c.rp * 56 + ksg) * GA;
#pragma unroll
      for (int m = 0; m < 2; ++m) {
        u32x2 uv[7];
#pragma unroll
        for (int n = 0; n < 7; ++n) {
          const f32x4 u = z1[m][n] * acc[m][n];
          uv[n] = (u32x2){pk_bf16(u[0], u[1]), pk_bf16(u[2], u[3])};
          if (n & 1) st128<SC1>((u32x4){uv[n - 1][0], uv[n - 1][1], uv[n][0], uv[n][1]}, c.rws, (int)up[m], (int)(gp + (n >> 1) * GA));
        }
        __builtin_amdgcn_raw_buffer_store_b64(uv[6], c.rws, (int)us[m], (int)gs, SC1);
      }
      bar_c();
      publish_last(c, FU, 2, 4);
    }
  }
  {
    refresh(c);
    G = ((c.g & 1) << 1) | (c.g >> 1);
    f32x4 acc[2][4];
#pragma unroll
    for (int m = 0; m < 2; ++m)
#pragma unroll
      for (int n = 0; n < 4; ++n) {
        acc[m][n] = (f32x4){bf_lo(hb[m][n][0]), bf_hi(hb[m][n][0]), bf_lo(hb[m][n][1]), bf_hi(hb[m][n][1])};
        asm volatile("" : "+v"(acc[m][n]));
      }
    const rsrc_t ro = mk_rsrc(P.out, 2048u * 2048u);
    u32 oo = (u32)((128 * c.rp + 32 * w + 2 * c.r16) * 2048 + (64 * c.cj + 8 * G) * 2);
    asm volatile("" : "+v"(oo));
    bar_c();
    gemm_g2<28, false>(acc, c);
#pragma unroll
    for (int m = 0; m < 2; ++m)
#pragma unroll
      for (int pr = 0; pr < 2; ++pr) {
        const f32x4 a = acc[m][2 * pr], b = acc[m][2 * pr + 1];
        st128<SC1 | 1>(pair_swap((u32x2){pk_bf16(a[0], a[1]), pk_bf16(a[2], a[3])}, (u32x2){pk_bf16(b[0], b[1]), pk_bf16(b[2], b[3])}), ro,
            (int)(oo + m * 2048 + pr * 64), 0);
      }
    if (c.rp == 0 && c.cj == 0 && w == 0 && c.lane == 0)
      __builtin_amdgcn_raw_buffer_store_b64((u32x2){c.tag_lo, c.tag_hi}, c.rws, (int)WS_EPOCH, 0, SC1);
  }
  asm volatile(".Lmk_end_c:");
}

template <int N, int AUX> __device__ __forceinline__ void issue_v2(rsrc_t r, u32 dst, const u32 (&vo)[N], u32 so) {
  sfor<N>([&](auto ic) { constexpr int s = decltype(ic)::v; glds<(s & 1) * 2048, AUX>(r, dst + (s >> 1) * 4096, vo[s], so); });
}
template <int N> __device__ __forceinline__ void issue_f(rsrc_t r, u32 dst, const u32 (&lp)[8], u32 so) {
  sfor<N>([&](auto ic) { constexpr int s = decltype(ic)::v; glds<(s & 3) * 1024, SC1>(r, dst + (s >> 2) * 4096, lp[s], so); });
}
template <int LO, int HI, int NA_, class FA, class FB> __device__ __forceinline__ void issue_rng(FA&& fa, FB&& fb) {
  sfor<HI - LO>([&](auto ic) { constexpr int s = LO + decltype(ic)::v; if constexpr (s < NA_) fa(IC<s>{}); else fb(IC<s - NA_>{}); });
}
struct Off { u32 da, db, sa, sb; };
template <int NT_, int NA_, int SPLIT, int W, class FA, class FB, class NX>
__device__ __forceinline__ void steady(int n, Off& o, FA&& fa, FB&& fb, NX&& next) {
  bar_n();
  issue_rng<0, SPLIT, NA_>([&](auto ic) { fa(ic, o); }, [&](auto ic) { fb(ic, o); });
#pragma unroll 1
  for (int i = 1; i < n; ++i) {
    Off q = next(o);
    asm volatile("" : "+s"(q.da), "+s"(q.db), "+s"(q.sa), "+s"(q.sb));
    issue_rng<SPLIT, NT_, NA_>([&](auto ic) { fa(ic, o); }, [&](auto ic) { fb(ic, o); });
    MK_WAITV(W);
    bar_n();
    issue_rng<0, SPLIT, NA_>([&](auto ic) { fa(ic, q); }, [&](auto ic) { fb(ic, q); });
    o = q;
  }
  issue_rng<SPLIT, NT_, NA_>([&](auto ic) { fa(ic, o); }, [&](auto ic) { fb(ic, o); });
  MK_WAITV(W);
}
template <int NT_, int NA_, int W, class FA, class FB, class NX>
__device__ __forceinline__ void steady2(int lw, int n, Off o, FA&& fa, FB&& fb, NX&& next) {
  if (lw == 0) steady<NT_, NA_, NT_ / 4, W>(n, o, fa, fb, next); else steady<NT_, NA_, NT_ - NT_ / 4, W>(n, o, fa, fb, next);
}
__device__ __forceinline__ u32 tr64_src(int lane, int lw, int ldb_bytes) {
  const int f = ((lane >> 4) & 1) | (lw << 1);
  return (u32)((8 * lw + (lane >> 3)) * ldb_bytes + (((lane & 7) ^ (2 * f)) * 16));
}
template <int T, bool LASTBAR>
__device__ __forceinline__ u32x2 load_g2(const Ctx& c, int lw, const u32 (&lp)[8], u32 abase, rsrc_t rb, const u32 (&vb)[4], u32 fo) {
  constexpr u32 bstep = 64 * 2048;
  const u32 sa = abase + (u32)lw * GA, da = c.lds + G2_A + (u32)(lw * 8192), db = c.lds + G2_B + (u32)(lw * 1024);
  const int ko = (T / 4) * (c.rp >> 2);
  auto kt = [&](int t) { const int k = t + ko; return (u32)(k >= T ? k - T : k); };
  auto A = [&](int t) { issue_f<8>(c.rws, da + (u32)((t & 3) * 16384), lp, sa + kt(t) * 2 * GA); };
  auto B = [&](int t) { issue_v2<4, 0>(rb, db + (u32)((t % G2_NB) * 8192), vb, kt(t) * bstep); };
  A(0); A(1); MK_WAITV(8); bar_n();
  A(2); A(3); MK_WAITV(16); bar_n();
  A(4); B(11); MK_WAITV(20); bar_n();
  A(5); B(12);
  MK_WAITV(24);
  steady2<12, 8, 28>(lw, T - 13, Off{da + 2 * 16384, db + 2 * 8192, sa + kt(6) * 2 * GA, kt(13) * bstep},
      [&](auto ic, const Off& o) { constexpr int s = decltype(ic)::v; glds<(s & 3) * 1024, SC1>(c.rws, o.da + (s >> 2) * 4096, lp[s], o.sa); },
      [&](auto ic, const Off& o) { constexpr int s = decltype(ic)::v; glds<(s & 1) * 2048, 0>(rb, o.db + (s >> 1) * 4096, vb[s], o.sb); },
      [&](const Off& o) { return Off{o.da == da + 3 * 16384 ? da : o.da + 16384, o.db == db + (G2_NB - 1) * 8192 ? db : o.db + 8192,
                                      o.sa == sa + (T - 1) * 2 * GA ? sa : o.sa + 2 * GA, o.sb == (T - 1) * bstep ? 0 : o.sb + bstep}; });
  bar_n(); A(T - 7); MK_WAITV(24);
  bar_n(); A(T - 6); MK_WAITV(20);
  bar_n(); A(T - 5); MK_WAITV(16);
  bar_n(); A(T - 4); MK_WAITV(16);
  bar_n(); A(T - 3); MK_WAITV(16);
  bar_n(); A(T - 2); MK_WAITV(16);
  bar_n(); A(T - 1); MK_WAITV(16);
  bar_n(); MK_WAITV(8);
  bar_n(); MK_WAITV(0);
  bar_n();
  u32x2 fv = {0, 0};
  if (LASTBAR) { fv = poll_issue(c, fo); bar_n(); }
  return fv;
}
template <int T> __device__ __forceinline__ void pre_g2(const Ctx& c, int lw, rsrc_t rb, const u32 (&vb)[4], int t0, int t1) {
  const int ko = (T / 4) * (c.rp >> 2);
#pragma unroll 1
  for (int t = t0; t < t1; ++t) { const int k = t + ko; issue_v2<4, 0>(rb, c.lds + G2_B + (u32)(lw * 1024 + t * 8192), vb, (u32)((k >= T ? k - T : k) * 64 * 2048)); }
}
__device__ __forceinline__ void src_g2(const Ctx& c, int lw, u32 (&vb)[4]) {
  const u32 b0 = tr64_src(c.lane, lw, 2048) + (u32)(128 * c.cj + 4096);
#pragma unroll
  for (int s = 0; s < 4; ++s) vb[s] = b0 + (u32)(s * 16 * 2048 - (s & 1) * 2048);
}

__device__ __forceinline__ void loader(const Params& P, Ctx& c) {
  const int lw = c.wid - 4, lane = c.lane;
  {
    const rsrc_t rx = mk_rsrc((const char*)P.x - 4096, 2048u * 2048u + 4096u);
    const rsrc_t rw = mk_rsrc((const char*)P.wqkv - 4096, 1024u * 6144u + 4096u);
    u32 va[9], vb[12];
    const u32 ch = (u32)((((lane & 7) ^ (4 * lw + (lane >> 4))) * 16) + 4096);
    va[0] = (u32)(max(128 * c.rp - 16 + 2 * (lane >> 3) + lw, 0) * 2048) + ch;
    const u32 a1 = (u32)((128 * c.rp + 16 * lw + 2 * (lane >> 3)) * 2048) + ch;
#pragma unroll
    for (int m = 1; m < 9; ++m) va[m] = a1 + (u32)((32 * ((m - 1) >> 1) + ((m - 1) & 1)) * 2048 - (m & 1) * 2048);
    const u32 b0 = tr64_src(lane, lw, 6144) + (u32)(128 * c.cj + 4096);
#pragma unroll
    for (int s = 0; s < 12; ++s) vb[s] = b0 + (u32)((s >> 2) * 2048 + (s & 3) * 16 * 6144 - (s & 1) * 2048);
    const int ko = 4 * (c.rp >> 2);
    auto I = [&](int t, int st) {
      const u32 sb = c.lds + (u32)(st * P1_ST + lw * 1024);
      const int k = (t + ko) & 15;
      issue_v2<9, 0>(rx, sb, va, (u32)(k * 128));
      issue_v2<12, 0>(rw, sb + P1_A, vb, (u32)(k * 64 * 6144));
    };
    I(0, 0); MK_WAITV(0); bar_n();
    I(1, 1); I(2, 2);
    const u32 d0 = c.lds + (u32)(lw * 1024);
    MK_WAITV(21);
    steady2<21, 9, 21>(lw, 13, Off{d0, d0 + P1_A, (u32)((3 + ko) * 128), (u32)((3 + ko) * 64 * 6144)},
        [&](auto ic, const Off& o) { constexpr int s = decltype(ic)::v; glds<(s & 1) * 2048, 0>(rx, o.da + (s >> 1) * 4096, va[s], o.sa); },
        [&](auto ic, const Off& o) { constexpr int s = decltype(ic)::v; glds<(s & 1) * 2048, 0>(rw, o.db + (s >> 1) * 4096, vb[s], o.sb); },
        [&](const Off& o) { const u32 d = o.da == d0 + 2 * P1_ST ? d0 : o.da + P1_ST; return Off{d, d + P1_A, (o.sa + 128) & 2047, o.sa == 1920 ? 0 : o.sb + 64 * 6144}; });
  }
  const u32 fwo = flag_off(FW, 0, 0) + (u32)((((lane >> 1) & 15) * 16 + c.cj) * 128 + 8 * (lane & 1));
  u32x2 fwv;
  u32 lp[8];
#pragma unroll
  for (int f = 0; f < 8; ++f) lp[f] = frag_off(c, f, lane) - (u32)((f & 3) * 1024);
  {
    const rsrc_t rb = mk_rsrc((const char*)P.wo - 4096, 1024u * 2048u + 4096u);
    u32 vb[4];
    src_g2(c, lw, vb);
    static_assert(6 * 8192 >= P1_ST && G2_PRE * 8192 <= 2 * P1_ST, "B slots 6.. lie in the stage P1 frees first");
    bar_n();
    pre_g2<16>(c, lw, rb, vb, 6, G2_PRE);
    MK_WAITV(4 * (G2_PRE - 6)); bar_n();
    bar_n();
    pre_g2<16>(c, lw, rb, vb, 0, 6);
    get_tag(c);
    const AtPre ap = attention_pre(c);
    bar_h();
    attention(c, ap);
    bar_n();
    pre_g2<16>(c, lw, rb, vb, G2_PRE, G2_NB);
    if (lw == 0) await_panel<9>(c, FO, 0, 8);
    bar_h();
    fwv = load_g2<16, true>(c, lw, lp, WS_O + (u32)(c.rp * 32) * GA, rb, vb, fwo);
  }
  {
    const rsrc_t rs = mk_rsrc((const char*)P.wsp - 4096, 2048u * 4096u + 4096u);
    const u32 bh = WS_H + (u32)(c.rp * 32 + lw) * GA, bw = WS_WINP + (u32)(c.cj * 64 + lw) * GB, bz = WS_Z2N + (u32)(c.cj * 64 + lw) * GB;
    const u32 da = c.lds + (u32)(lw * 8192), db = c.lds + (u32)(lw * 7168), dr = c.lds + (u32)(lw * 1024);
    u32 vs[8];
    const u32 s0 = (u32)((128 * c.rp + 16 * lw + 2 * (lane >> 3)) * 4096 + (((lane & 7) ^ (4 * lw + (lane >> 4))) * 16) + 4096);
#pragma unroll
    for (int m = 0; m < 8; ++m) vs[m] = s0 + (u32)((32 * (m >> 1) + (m & 1)) * 4096 - (m & 1) * 2048);
    bar_h();
    poll_check(c, fwo, fwv);
    auto A = [&](auto tc) {
      constexpr int t = decltype(tc)::v;
      if constexpr (t < 32) issue_f<8>(c.rws, da + g3_a(t), lp, bh + (u32)(t < 16 ? t : t - 22) * 2 * GA);
      else issue_v2<8, 0>(rs, dr + g3_a(t), vs, (u32)((t - 32) * 128));
    };
    auto B = [&](auto tc) {
      constexpr int t = decltype(tc)::v;
      issue_f<7>(c.rws, db + g3_b(t), lp, t < 32 ? bw + (u32)(t < 16 ? t : 16 + ((t + 10) & 15)) * 2 * GB : bz + (u32)(t - 32) * 2 * GB);
    };
    sfor<6>([&](auto tc) { B(tc); });
    bar_h();
    A(IC<0>{}); A(IC<1>{}); MK_WAITV(8); bar_n();
    A(IC<2>{}); A(IC<3>{}); MK_WAITV(16);
    sfor<30>([&](auto pc) {
      constexpr int p = 1 + decltype(pc)::v;
      bar_n();
      if constexpr (p == 30) B(IC<32>{});
      if constexpr (g3_na(p) != 0) A(IC<p + 3>{});
      if constexpr (g3_nb(p) != 0) B(IC<p + 3>{});
      MK_WAITV(p == 1 ? 8 + g3_na(1) : g3_na(p - 1) + g3_nb(p - 1) + g3_na(p) + g3_nb(p));
    });
    steady2<15, 8, 30>(lw, 30, Off{dr + g3_a(34), db + g3_b(34), 2 * 128, bz + 4 * GB},
        [&](auto ic, const Off& o) { constexpr int s = decltype(ic)::v; glds<(s & 1) * 2048, 0>(rs, o.da + (s >> 1) * 4096, vs[s], o.sa); },
        [&](auto ic, const Off& o) { constexpr int s = decltype(ic)::v; glds<(s & 3) * 1024, SC1>(c.rws, o.db + (s >> 2) * 4096, lp[s], o.sb); },
        [&](const Off& o) { return Off{o.da == dr + 5 * 16384 ? dr : o.da + 16384, o.db == db + G3_B + 3 * 14336 ? db + G3_B : o.db + 14336, o.sa + 128, o.sb + 2 * GB}; });
    bar_n();
    MK_WAITV(15); bar_n(); MK_WAITV(0); bar_n();
    bar_n();
  }
  {
    const rsrc_t rb = mk_rsrc((const char*)P.wout - 4096, 1792u * 2048u + 4096u);
    u32 vb[4];
    src_g2(c, lw, vb);
    bar_h();
    pre_g2<28>(c, lw, rb, vb, 0, G2_NB);
    bar_h();
    load_g2<28, false>(c, lw, lp, WS_U + (u32)(c.rp * 56) * GA, rb, vb, 0);
  }
  asm volatile(".Lmk_end_l:");
}

__device__ __forceinline__ void bars(int n) {
#pragma unroll 1
  for (int i = 0; i < n; ++i) bar_n();
}
__device__ __forceinline__ void sumsq2(float& s, float& q, u32 u) {
  float a, b;
  asm("v_lshlrev_b32 %2, 16, %4\n\tv_and_b32 %3, 0xffff0000, %4\n\tv_add_f32 %0, %0, %2\n\tv_fmac_f32 %1, %2, %2\n\tv_add_f32 %0, %0, %3\n\tv_fmac_f32 %1, %3, %3"
      : "+v"(s), "+v"(q), "=&v"(a), "=&v"(b) : "v"(u));
}
__device__ __forceinline__ void warm_slow(const Params& P, const Ctx& c, int row0, int nrow) {
  const int cslow = (c.cj & 8) + ((3 - (int)((size_t)P.wqkv >> 7)) & 7), i = c.lane / 3, g = c.lane - 3 * i;
  if (32 * i < nrow) touch(c, mk_rsrc(P.wqkv, 1024u * 6144u), (u32)(((row0 + 256 * (c.rp >> 2) + (blockIdx.x >> 3) + 32 * i) & 1023) * 6144 + g * 2048 + cslow * 128), 0);
}
__device__ __forceinline__ void load_x(const Params& P, const Ctx& c, int h, u32x4 (&xr)[4]) {
  const rsrc_t rx = mk_rsrc(P.x, 2048u * 2048u);
#pragma unroll
  for (int i = 0; i < 4; ++i)
    xr[i] = __builtin_amdgcn_raw_buffer_load_b128(rx, (128 * c.rp + 8 * c.cj + 4 * (c.wid - 6) + i) * 2048 + ((c.lane * 16 + h * 1024 + 512 * (c.rp >> 2)) & 2047), 0, 0);
}
__device__ __forceinline__ void worker(const Params& P, Ctx& c, u32x4 (&xr0)[4]) {
  refresh(c);
  const int v = c.wid - 6, lane = c.lane;
  const u32 stg = c.lds + WSTG + (u32)(v * 14336);
  u32x4 wr[14];
  {
    u32x4 xr1[4];
    const u32x2 ep = __builtin_amdgcn_raw_buffer_load_b64(c.rws, (int)WS_EPOCH, 0, SC1);
    bar_n();
    { const u64 t = (((u64)ep[1] << 32) | ep[0]) + 1;
      c.tag_lo = __builtin_amdgcn_readfirstlane((u32)t); c.tag_hi = __builtin_amdgcn_readfirstlane((u32)(t >> 32));
      if (v == 1 && lane < 5) lds_st<u32>(c.lds + T_TAG + (u32)(4 * lane), lane == 0 ? c.tag_lo : lane == 1 ? c.tag_hi : 0u); }
    bars(2);
    load_x(P, c, 1, xr1);
    bars(3);
    float sm = 0.f, sr = 0.f;
#pragma unroll
    for (int i = 0; i < 4; ++i) {
      float s = 0.f, q = 0.f;
#pragma unroll
      for (int h = 0; h < 2; ++h)
#pragma unroll
        for (int d = 0; d < 4; ++d) { u32 u = h ? xr1[i][d] : xr0[i][d]; sumsq2(s, q, u); }
      const float mean = sumg(sum16(s)) * (1.0f / 1024.0f);
      const float rs = __builtin_amdgcn_rsqf(fmaxf(sumg(sum16(q)) * (1.0f / 1024.0f) - mean * mean, 0.f) + 1e-5f);
      sm = lane == i ? mean : sm; sr = lane == i ? rs : sr;
      asm volatile("" : "+v"(sm), "+v"(sr));
      if (i == 3) { if (lane < 4) st128<SC1>(tag_pair(c, sm, sr), c.rws, (int)(WS_XSTAT + (u32)((128 * c.rp + 8 * c.cj + 4 * v + lane) * 16)), 0); }
      if (i == 0 && v == 1) warm_slow(P, c, 448, 320);
      bar_n();
    }
    if (v == 0) warm_slow(P, c, 768, 256);
    else touch_code(c);
    bars(2);
    u32x4 xg[2];
    u32 xo[2];
#pragma unroll
    for (int i = 0; i < 2; ++i) {
      const int rho = min(72 * v + 64 * i + lane, 72 * v + 71), m = rho >> 4, sl = rho & 15;
      const int il = m == 0 ? (2 * (sl & 7) + (sl >> 3)) : 16 + 32 * ((m - 1) >> 1) + 2 * sl + ((m - 1) & 1);
      xo[i] = WS_XSTAT + (u32)(max(128 * c.rp - 16 + il, 0) * 16);
      xg[i] = __builtin_amdgcn_raw_buffer_load_b128(c.rws, (int)xo[i], 0, SC1);
    }
    bars(2);
    if (!__all((tag_bad(c, xg[0]) | tag_bad(c, xg[1])) == 0)) {
#pragma unroll 1
      for (int sp = 0; sp < SPIN_CAP; ++sp) {
        asm volatile("" ::: "memory");
#pragma unroll
        for (int i = 0; i < 2; ++i) xg[i] = __builtin_amdgcn_raw_buffer_load_b128(c.rws, (int)xo[i], 0, SC1);
        if (__all((tag_bad(c, xg[0]) | tag_bad(c, xg[1])) == 0)) break;
      }
    }
#pragma unroll
    for (int i = 0; i < 2; ++i) lds_st<u32x2>(c.lds + T_XST + (u32)(min(72 * v + 64 * i + lane, 72 * v + 71) * 8), (u32x2){xg[i][0], xg[i][2]});
    bar_h();
    {
      const rsrc_t rw = mk_rsrc(P.win, 1024u * 7168u);
      int k = lane / 14, ch = lane - 14 * k;
      u32 vo = (u32)((64 * c.rp + k) * 7168 + ((v == 0 ? 1792 : 0) + 112 * c.cj) * 2 + ch * 16);
#pragma unroll
      for (int j = 0; j < 14; ++j) {
        wr[j] = __builtin_amdgcn_raw_buffer_load_b128(rw, (int)vo, 0, 0);
        ch += 8; const bool wrap = ch >= 14; ch -= wrap ? 14 : 0; vo += wrap ? (u32)(5 * 7168 - 96) : (u32)(4 * 7168 + 128);
        if (j == 6) bar_n();
      }
    }
    bar_n();
  }
  const AtPre ap = attention_pre(c);
  bar_n();
  attention(c, ap);
  bar_n();
#pragma unroll
  for (int j = 0; j < 14; ++j) lds_st<u32x4>(stg + (u32)(j * 1024 + lane * 16), wr[j]);
  MK_LGKM0();
  {
    const int odd = c.cj & 1;
    const u32 ta = stg + (u32)((8 * c.g + c.q) * 224);
    const u32 tp = ta + (u32)(((odd ? 16 : 0) + 8 * c.p) * 2), ts = ta + (u32)(((odd ? 0 : 96) + 4 * c.p) * 2);
    const u32 wo_ = WS_WINP + (u32)((c.cj * 2 + v) * 32 + 2 * c.rp) * GB;
    float cs = 0.f;
    sfor<7>([&](auto nc) {
      constexpr int n = decltype(nc)::v, o = n < 6 ? 64 * (n >> 1) + 8 * (n & 1) : 0;
      bf16x8 f2[2];
      lds_tr4<o, o + 896, o + 7168, o + 7168 + 896>(n < 6 ? tp : ts, n < 6 ? tp : ts, f2[0], f2[1]);
      f32x2 s2 = {0.f, 0.f};
#pragma unroll
      for (int kl = 0; kl < 2; ++kl) {
        const u32x4 fr = __builtin_bit_cast(u32x4, f2[kl]);
        st128<SC1>(fr, c.rws, (int)frag_off(c, n, lane), (int)(wo_ + kl * GB));
#pragma unroll
        for (int d = 0; d < 4; ++d) s2 += (f32x2){bf_lo(fr[d]), bf_hi(fr[d])};
      }
      const float t = sumg(s2[0] + s2[1]);
      if constexpr (n < 4) cs = (c.g == n) ? t : cs;
      if constexpr (n == 3) __builtin_amdgcn_raw_buffer_store_b32(asu(cs), c.rws, (int)(WS_CS + (u32)(((c.rp * 16 + c.cj) * 256 + 112 * v + lane) * 4)), 0, SC1);
      if constexpr (n >= 4) cs = (c.g == n - 4) ? t : cs;
    });
    if (lane < 48) __builtin_amdgcn_raw_buffer_store_b32(asu(cs), c.rws, (int)(WS_CS + (u32)(((c.rp * 16 + c.cj) * 256 + 112 * v + 64 + lane) * 4)), 0, SC1);
  }
  bar_h();
  bars(4);
  publish(c, FW, v);
  bars(6);
  if (v == 1) touch_code(c);
  bars(7);
  bar_n();
  const int hi = lane >> 5, l5 = lane & 31;
  u32x4 cp[16];
  if (v == 0) await_panel<14>(c, FHH, 1, 4);
  else {
    poll64(c, flag_off(FW, 0, 0) + (u32)((((lane >> 1) & 15) * 16 + c.cj) * 128 + 8 * (lane & 1)));
#pragma unroll
    for (int j = 0; j < 16; ++j)
      cp[j] = __builtin_amdgcn_raw_buffer_load_b128(c.rws, (int)(WS_CS + (u32)(((hi * 16 + c.cj) * 256 + 112 * (j >> 3) + 4 * min(l5, 27)) * 4)), (j & 7) * 32768, SC1);
  }
  bar_n();
  {
    auto fold = [](float x) { const u32 u = asu(x); auto r = __builtin_amdgcn_permlane32_swap(u, u, false, false); return asf((u32)r[0]) + asf((u32)r[1]); };
    const u32 fso = flag_off(FHS, 0, 0) + (u32)((c.rp * 16 + (lane & 15)) * 128);
    bars(5);
    if (v == 1 && lane == 0) __builtin_amdgcn_raw_buffer_store_b64((u32x2){c.tag_lo, c.tag_hi}, c.rws, (int)flag_off(FHS, c.rp, c.cj), 0, SC1);
    bars(3);
    const u32x2 fsv = poll_issue(c, fso);
    bars(2);
    poll_check(c, fso, fsv);
    u32x4 hp[8];
#pragma unroll
    for (int j = 0; j < 8; ++j) {
      hp[j] = __builtin_amdgcn_raw_buffer_load_b128(c.rws, (int)(WS_HSTAT + (u32)(((c.rp * 16 + hi) * 128 + 64 * v + 2 * l5) * 8)), j * 2048, SC1);
    }
#pragma unroll
    for (int h = 0; h < 2; ++h) {
      bar_n();
      if (v == 1) {
        float c0 = 0.f, c1 = 0.f, c2 = 0.f, c3 = 0.f;
#pragma unroll
        for (int j = 8 * h; j < 8 * h + 8; ++j) { add2(c0, c1, cp[j][0], cp[j][1]); add2(c2, c3, cp[j][2], cp[j][3]); }
        lds_st<f32x4>(c.lds + T_CS + (u32)((112 * h + 4 * min(l5, 27)) * 4), (f32x4){fold(c0), fold(c1), fold(c2), fold(c3)});
      }
    }
    if (v == 0) touch_code(c);
    bars(2);
    float s0 = 0.f, q0 = 0.f, s1 = 0.f, q1 = 0.f;
#pragma unroll
    for (int j = 0; j < 8; ++j) { add2(s0, q0, hp[j][0], hp[j][1]); add2(s1, q1, hp[j][2], hp[j][3]); }
    {
      s0 = fold(s0); q0 = fold(q0); s1 = fold(s1); q1 = fold(q1);
      const float sx = hi ? s1 : s0, qx = hi ? q1 : q0;
      const float mean = sx * (1.0f / 1024.0f);
      const float rs = __builtin_amdgcn_rsqf(fmaxf(qx * (1.0f / 1024.0f) - mean * mean, 0.f) + 1e-5f);
      lds_st<float>(c.lds + T_HM + (u32)((64 * v + 2 * l5 + hi) * 4), mean);
      lds_st<float>(c.lds + T_HR + (u32)((64 * v + 2 * l5 + hi) * 4), rs);
    }
    bar_n();
    bar_h(); bar_n();
#pragma unroll
    for (int h = 0; h < 2; ++h)
      touch(c, mk_rsrc(P.wsp, 2048u * 4096u), (u32)((lane >> 4) * 4096 + (lane & 15) * 128), blockIdx.x * 32768u + v * 16384u + h * 2048u);
    bars(SWEEP_T - 16);
    u32x4 zp[16];
    auto sweep = [&]() {
#pragma unroll
      for (int j = 0; j < 16; ++j) zp[j] = __builtin_amdgcn_raw_buffer_load_b128(c.rws, (int)(WS_ZSTAT + (u32)((c.rp * 2048 + 64 * v + lane) * 16)), j * 2048, SC1);
    };
    sweep();
    bars(NORM_T - 1 - SWEEP_T);
    float zs = 0.f, zq = 0.f;
    u32 bad = 0;
    auto half = [&](int j0) {
#pragma unroll
      for (int j = j0; j < j0 + 8; ++j) {
        u32 t1, t2;
        asm("v_bitop3_b32 %3, %5, %6, %8 bitop3:0x96\n\tv_bitop3_b32 %4, %7, %9, %8 bitop3:0x69\n\tv_add_f32 %0, %0, %9\n\tv_add_f32 %1, %1, %6\n\tv_or3_b32 %2, %2, %3, %4"
            : "+v"(zs), "+v"(zq), "+v"(bad), "=&v"(t1), "=&v"(t2) : "v"(zp[j][1]), "v"(zp[j][2]), "v"(zp[j][3]), "s"(c.tag_lo), "v"(zp[j][0]));
      }
    };
    half(0);
    asm volatile("" : "+v"(zs), "+v"(zq), "+v"(bad));
    bar_n();
    half(8);
    if (!__all(bad == 0)) {
#pragma unroll 1
      for (int sp = 0; sp < SPIN_CAP; ++sp) {
        asm volatile("" ::: "memory");
        sweep();
        zs = 0.f; zq = 0.f; bad = 0;
        half(0); half(8);
        if (__all(bad == 0)) break;
      }
    }
    const float zm = zs * (1.0f / 1792.0f);
    const float zr = __builtin_amdgcn_rsqf(fmaxf(zq * (1.0f / 1792.0f) - zm * zm, 0.f) + 1e-5f);
    lds_st<float>(c.lds + T_ZM + (u32)((64 * v + lane) * 4), -zm * zr);
    lds_st<float>(c.lds + T_ZR + (u32)((64 * v + lane) * 4), zr);
    bar_h();
    bars(FLAG_T - NORM_T);
    if (v == 1 && lane == 0) __builtin_amdgcn_raw_buffer_store_b64((u32x2){c.tag_lo, c.tag_hi}, c.rws, (int)flag_off(FZ, c.rp, c.cj), 0, SC1);
    bars(26 - FLAG_T);
    const u32 fo = flag_off(FZ, 0, 0) + (u32)(((lane & 15) * 16 + c.cj) * 128);
    u32x2 fv = {0, 0};
    if (v == 0) fv = poll_issue(c, fo);
    bars(2);
    if (v == 0) poll_check(c, fo, fv);
    bars(24);
    if (v == 1) touch_code(c);
    if (v == 0) {
      const int cs5 = (c.cj & 8) + ((3 - (int)((size_t)P.wout >> 7)) & 7);
      if (lane < 56) touch(c, mk_rsrc(P.wout, 1792u * 2048u), (u32)(((blockIdx.x >> 3) + 32 * lane) * 2048 + cs5 * 128), 0);
    }
    bars(11);
  }
  bar_n();
  if (v == 0) await_panel<16>(c, FU, 2, 4);
  bar_n();
  asm volatile(".Lmk_end_w:");
}

__global__ __launch_bounds__(512, 2) void fused_block(Params P) {
  extern __shared__ __attribute__((aligned(16))) char smem[];
  asm volatile(".Lmk_code_begin:");
  Ctx c;
  c.lane = threadIdx.x & 63;
  c.wid = __builtin_amdgcn_readfirstlane(threadIdx.x >> 6);
  const int xcd = blockIdx.x & 7, idx = blockIdx.x >> 3;
  c.rp = (xcd & 3) * 4 + (idx & 3);
  c.cj = (xcd >> 2) * 8 + (idx >> 2);
  c.r16 = c.lane & 15; c.g = c.lane >> 4; c.q = (c.lane >> 2) & 3; c.p = c.lane & 3;
  c.lds = (u32)(size_t)(LAS char*)smem;
  c.rws = mk_rsrc(P.ws, WS_END);
  c.skip = 7 - ((3 - (int)((size_t)P.ws >> 7)) & 7);
  c.tag_lo = 0; c.tag_hi = 0;
  u32x4 xr0[4];
  if (c.wid == 7) touch_code(c);
  if (c.wid >= 6) {
    __builtin_amdgcn_s_sleep(5);
    if (c.wid == 6) warm_slow(P, c, 64, 384);
    load_x(P, c, 0, xr0);
  }
  if (__builtin_expect((c.wid >> 1) == 2, 1)) loader(P, c); else if (c.wid < 4) consumer(P, c); else worker(P, c, xr0);
}

}

extern "C" void kernel_launch(void* const* d_in, const int* in_sizes, int n_in, void* d_out, int out_size, void* d_ws, size_t ws_size,
                              hipStream_t stream) {
  (void)in_sizes; (void)n_in; (void)out_size; (void)ws_size;
  mk::Params P;
  P.x = (const mk::u16*)d_in[0]; P.wqkv = (const mk::u16*)d_in[1]; P.wo = (const mk::u16*)d_in[2];
  P.win = (const mk::u16*)d_in[4]; P.wsp = (const mk::u16*)d_in[5]; P.bsp = (const mk::u16*)d_in[6]; P.wout = (const mk::u16*)d_in[7];
  P.out = (mk::u16*)d_out; P.ws = (char*)d_ws;
  (void)hipFuncSetAttribute((const void*)mk::fused_block, hipFuncAttributeMaxDynamicSharedMemorySize, mk::LDS_BYTES);
  hipLaunchKernelGGL(mk::fused_block, dim3(256), dim3(512), mk::LDS_BYTES, stream, P);
}
```
